```python
import torch

def _softmax(x, axis=-1):
    return torch.softmax(x, dim=axis)

def _gelu(x):
    return torch.nn.functional.gelu(x, approximate='tanh')

def _silu(x):
    return torch.nn.functional.silu(x)

def _sigmoid(x):
    return torch.sigmoid(x)

def _layernorm(x, axis=-1, eps=1e-05):
    m = x.mean(dim=axis, keepdim=True)
    v = x.var(dim=axis, keepdim=True, unbiased=False)
    return (x - m) / torch.sqrt(v + eps)

def _scatter_add(tgt, idx, src, axis=0):
    return tgt.clone().index_add_(axis, idx, src)

def _segment_sum(src, seg, n):
    out = torch.zeros((n,) + src.shape[1:], dtype=src.dtype)
    return out.index_add_(0, seg, src)

def _pad(x, pw):
    flat = tuple((v for pair in reversed(pw) for v in pair))
    return torch.nn.functional.pad(x, flat)
kernel_name = 'adaLN_cond__sliding_block_cached__dense_activation_relu__prompt'

def setup_inputs():

    _w = lambda a: (lambda t: t.to(torch.bfloat16) if t.is_floating_point() else t)(torch.as_tensor(a))

    def _A_setup():
        B, Sq, D = (1, 2048, 448)
        Hq, Hkv, Sk, Dh = (16, 8, 2048, 256)
        assert Hq % Hkv == 0, (Hq, Hkv)
        return {'x': _w((lambda *s: torch.randn(*s, dtype=torch.bfloat16) * 0.05)(B, Sq, D)), 'k_cache': _w((lambda *s: torch.randn(*s, dtype=torch.bfloat16) * 3.2)(B, Hkv, Sk, Dh)), 'v_cache': _w((lambda *s: torch.randn(*s, dtype=torch.bfloat16) * 0.4)(B, Hkv, Sk, Dh)), 'wq': _w((lambda *s: torch.randn(*s, dtype=torch.bfloat16) * 3.2)(D, Hq * Dh) * D ** (-0.5)), 'wo': _w((lambda *s: torch.randn(*s, dtype=torch.bfloat16) * 0.4)(Hq * Dh, D) * (Hq * Dh) ** (-0.5))}
    _A_in = _A_setup()

    def _B_setup():
        B, S, D, Dff = (1, 2048, 448, 1792)
        return {'x': _w((lambda *s: torch.randn(*s, dtype=torch.bfloat16) * 0.05)(B, S, D)), 'w1': _w((lambda *s: torch.randn(*s, dtype=torch.bfloat16) * 0.05)(D, Dff)), 'w2': _w((lambda *s: torch.randn(*s, dtype=torch.bfloat16) * 0.05)(Dff, D))}
    _B_in = _B_setup()
    return {'A_x': _A_in['x'], 'A_k_cache': _A_in['k_cache'], 'A_v_cache': _A_in['v_cache'], 'A_wq': _A_in['wq'], 'A_wo': _A_in['wo'], 'B_x': _B_in['x'], 'B_w1': _B_in['w1'], 'B_w2': _B_in['w2'], 'c': _w((lambda *s: torch.randn(*s, dtype=torch.bfloat16) * 0.1)(1, 448) * 8.0), 'w_mod': _w((lambda *s: torch.randn(*s, dtype=torch.bfloat16) * 0.1)(448, 6 * 448) * 448 ** (-0.5) * 8.0)}

def reference(A_x, A_k_cache, A_v_cache, A_wq, A_wo, B_x, B_w1, B_w2, c, w_mod):

    def _A_ref(x, k_cache, v_cache, wq, wo):
        B, Sq, D = x.shape
        Hq, Dh = (16, 256)
        Hkv, Sk = (k_cache.shape[1], k_cache.shape[2])
        W = min(4096, max(256, Sk // 4))
        rep = Hq // Hkv
        q = (lambda x, *d: x.permute(*d))((x @ wq).reshape(B, Sq, Hq, Dh), 0, 2, 1, 3)
        k = (lambda x, n, axis: torch.repeat_interleave(x, n, dim=axis))(k_cache[:, :, Sk - W:, :], rep, axis=1)
        v = (lambda x, n, axis: torch.repeat_interleave(x, n, dim=axis))(v_cache[:, :, Sk - W:, :], rep, axis=1)
        scores = torch.einsum('bhqd,bhkd->bhqk', q, k) * Dh ** (-0.5)
        p = _softmax(scores, axis=-1)
        o = torch.einsum('bhqk,bhkd->bhqd', p, v)
        return (lambda x, *d: x.permute(*d))(o, 0, 2, 1, 3).reshape(B, Sq, Hq * Dh) @ wo

    def _B_ref(x, w1, w2):
        h = torch.einsum('bsd,df->bsf', x, w1)
        zero = torch.zeros_like(h)
        h = torch.maximum(h, zero)
        return torch.einsum('bsf,fd->bsd', h, w2)
    x = A_x
    mod = (_silu(c) @ w_mod).reshape(c.shape[0], 6, 1, -1)
    sa, ca, ga, sb, cb, gb = (mod[:, 0].clone(), mod[:, 1].clone(), mod[:, 2].clone(), mod[:, 3].clone(), mod[:, 4].clone(), mod[:, 5].clone())
    h = _layernorm(x, axis=-1) * (1 + ca) + sa
    x = x + ga * _A_ref(h, A_k_cache, A_v_cache, A_wq, A_wo)
    h = _layernorm(x, axis=-1) * (1 + cb) + sb
    x = x + gb * _B_ref(h, B_w1, B_w2)
    return x
```

```cpp
#include <hip/hip_runtime.h>
#include <hip/hip_bf16.h>

__global__ __launch_bounds__(256, 1) void k_block(const char* x, const char* kc, const char* vc, const char* wq, const char* wo,
                                                  const char* w1, const char* w2, const char* c, const char* wm, char* ws, char* out) {
  __shared__ char lds[162816];
  unsigned b = blockIdx.x;
  asm volatile("s_getpc_b64 s[40:41]\nv_lshrrev_b32 v1, 2, %1\nv_lshlrev_b32 v1, 7, v1\nglobal_load_dword a255, v1, s[40:41]\n"
               : "+s"(b) : "v"(threadIdx.x) : "s40", "s41", "v1", "a255");
  const unsigned w = __builtin_amdgcn_readfirstlane(threadIdx.x >> 6);
  const unsigned g = b & 7, T = b >> 3;
  const unsigned lg = g + 8 * (b >> 5), col0 = lg < 42 ? lg * 64 + ((b >> 3) & 3) * 16 : 0, f = g < 7 ? g : 6;
  const unsigned go = lg < 42 ? 102400 + col0 * 8 : 0x7ff00000u, nwm = lg < 42 ? 448 * 5376 - col0 * 2 : 0;
  const unsigned one = ((lg >= 7 && lg < 14) || (lg >= 28 && lg < 35)) ? 0x3f800000u : 0;
  const unsigned n1 = g < 7 ? 447 * 3584 + 512 : 0, n2 = g < 7 ? 256 * 896 : 0, nx = g < 7 ? 28672 : 0;
  asm volatile(
"s_mov_b32 s71, %[w]\n"
"s_lshl_b32 s68, s71, 12\n"
"s_mov_b64 s[60:61], %[ws]\n"
"s_mov_b32 s62, 131072\n"
"s_mov_b32 s63, 0x00020000\n"
"s_mov_b64 s[80:81], %[wm]\n"
"s_mov_b32 s82, %[nwm]\n"
"s_mov_b32 s83, 0x00020000\n"
"s_mov_b64 s[76:77], %[c]\n"
"s_mov_b32 s78, 896\n"
"s_mov_b32 s79, 0x00020000\n"
"v_mbcnt_lo_u32_b32 v0, -1, 0\n"
"v_mbcnt_hi_u32_b32 v0, -1, v0\n"
"v_mov_b32 v24, 0\n"
"buffer_load_dword v22, v24, s[60:63], 0 offen sc1\n"
"v_lshrrev_b32 v24, 1, v0\n"
"s_lshl_b32 s75, s71, 5\n"
"v_add_u32 v24, s75, v24\n"
"v_and_b32 v25, 1, v0\n"
"v_lshlrev_b32 v25, 4, v25\n"
"s_movk_i32 s75, 5376\n"
"v_mad_u32_u24 v25, v24, s75, v25\n"
"v_lshlrev_b32 v24, 1, v24\n"
"s_mov_b32 s75, 0\n"
"buffer_load_dwordx4 v[32:35], v25, s[80:83], s75 offen\n"
"buffer_load_ushort v48, v24, s[76:79], 0 offen offset:0\n"
"s_mov_b32 s75, 688128\n"
"buffer_load_dwordx4 v[36:39], v25, s[80:83], s75 offen\n"
"buffer_load_ushort v49, v24, s[76:79], 0 offen offset:256\n"
"s_mov_b32 s75, 1376256\n"
"buffer_load_dwordx4 v[40:43], v25, s[80:83], s75 offen\n"
"buffer_load_ushort v50, v24, s[76:79], 0 offen offset:512\n"
"s_mov_b32 s75, 2064384\n"
"buffer_load_dwordx4 v[44:47], v25, s[80:83], s75 offen\n"
"buffer_load_ushort v51, v24, s[76:79], 0 offen offset:768\n"
"s_mul_i32 s85, %[T], 57344\n"
"s_mov_b64 s[56:57], %[x]\n"
"s_add_u32 s56, s56, s85\n"
"s_addc_u32 s57, s57, 0\n"
"s_mov_b32 s58, 57344\n"
"s_mov_b32 s59, 0x00020000\n"
"s_mul_i32 s69, %[rr], 28672\n"
"s_mul_i32 s75, %[ss], 896\n"
"v_add_u32 v126, s75, v0\n"
"s_mul_i32 s70, s71, 14336\n"
"s_add_u32 s70, s70, 65536\n"
"s_mov_b64 s[40:41], %[wq]\n"
"s_mov_b32 s42, 3662848\n"
"s_mov_b32 s43, 0x00020000\n"
"s_lshl_b32 s75, s71, 15\n"
"v_xor_b32 v27, 0, v0\n"
"v_lshlrev_b32 v27, 4, v27\n"
"v_add_u32 v27, s75, v27\n"
"v_add_u32 v1, 0, v27\n"
"v_xor_b32 v27, 4, v0\n"
"v_lshlrev_b32 v27, 4, v27\n"
"v_add_u32 v27, s75, v27\n"
"v_add_u32 v2, 7168, v27\n"
"v_xor_b32 v27, 8, v0\n"
"v_lshlrev_b32 v27, 4, v27\n"
"v_add_u32 v27, s75, v27\n"
"v_add_u32 v3, 14336, v27\n"
"v_xor_b32 v27, 12, v0\n"
"v_lshlrev_b32 v27, 4, v27\n"
"v_add_u32 v27, s75, v27\n"
"v_add_u32 v4, 21504, v27\n"
"s_add_u32 m0, s70, 0\n"
"v_mul_u32_u24 v127, 9363, v126\n"
"v_bfe_u32 v127, v127, 20, 3\n"
"v_xor_b32 v127, v127, v126\n"
"v_lshlrev_b32 v127, 4, v127\n"
"buffer_load_dwordx4 v127, s[56:59], s69 offen lds\n"
"v_add_u32 v126, 64, v126\n"
"s_mov_b64 s[44:45], %[k]\n"
"s_mov_b32 s46, 262144\n"
"s_mov_b32 s47, 0x00020000\n"
"s_mov_b64 s[48:49], %[v]\n"
"s_mov_b32 s50, 262144\n"
"s_mov_b32 s51, 0x00020000\n"
"s_mov_b64 s[52:53], %[wo]\n"
"s_mov_b32 s54, 458752\n"
"s_mov_b32 s55, 0x00020000\n"
"s_add_u32 s85, s85, s69\n"
"s_mul_i32 s75, %[g], 1835008\n"
"s_add_u32 s85, s85, s75\n"
"s_lshl_b32 s75, %[ss], 11\n"
"s_add_u32 s85, s85, s75\n"
"s_add_u32 s85, s85, 1966080\n"
"s_mov_b64 s[64:65], %[ws]\n"
"s_add_u32 s64, s64, s85\n"
"s_addc_u32 s65, s65, 0\n"
"s_mov_b32 s66, 14706688\n"
"s_mov_b32 s67, 0x00020000\n"
"s_mov_b32 s73, 0xbdb8aa3b\n"
"s_add_u32 m0, s70, 1024\n"
"v_mul_u32_u24 v127, 9363, v126\n"
"v_bfe_u32 v127, v127, 20, 3\n"
"v_xor_b32 v127, v127, v126\n"
"v_lshlrev_b32 v127, 4, v127\n"
"buffer_load_dwordx4 v127, s[56:59], s69 offen lds\n"
"v_add_u32 v126, 64, v126\n"
"v_lshlrev_b32 v23, 4, v0\n"
"s_getpc_b64 s[76:77]\n"
".Lpc_%=:\n"
"s_mov_b32 s78, .Lend_%=-.Lpc_%=\n"
"s_lshl_b32 s75, %[T], 2\n"
"s_add_u32 s75, s75, s71\n"
"s_mul_i32 s75, s75, 5\n"
"v_min_u32 v30, 4, v0\n"
"v_add_u32 v30, s75, v30\n"
"v_lshrrev_b32 v31, 0, v30\n"
"v_mul_u32_u24 v31, 0, v31\n"
"v_lshl_add_u32 v30, v30, 7, v31\n"
"buffer_load_dword a230, v30, s[76:79], 0 offen\n"
"v_lshrrev_b32 v30, 5, v0\n"
"v_bfe_u32 v29, v0, 2, 2\n"
"v_bfe_u32 v26, v0, 4, 1\n"
"v_lshlrev_b32 v26, 5, v26\n"
"v_and_b32 v27, 1, v0\n"
"v_lshl_or_b32 v26, v27, 4, v26\n"
"v_bfe_u32 v27, v0, 1, 1\n"
"v_lshl_or_b32 v26, v27, 3, v26\n"
"v_lshl_add_u32 v27, v30, 3, v29\n"
"v_lshl_add_u32 v28, v27, 10, v26\n"
"v_lshl_add_u32 v28, %[ss], 9, v28\n"
"v_xor_b32 v27, 0, v29\n"
"v_lshl_add_u32 v5, v27, 6, v28\n"
"v_xor_b32 v27, 1, v29\n"
"v_lshl_add_u32 v6, v27, 6, v28\n"
"v_xor_b32 v27, 2, v29\n"
"v_lshl_add_u32 v7, v27, 6, v28\n"
"v_xor_b32 v27, 3, v29\n"
"v_lshl_add_u32 v8, v27, 6, v28\n"
"s_add_u32 m0, s70, 2048\n"
"v_mul_u32_u24 v127, 9363, v126\n"
"v_bfe_u32 v127, v127, 20, 3\n"
"v_xor_b32 v127, v127, v126\n"
"v_lshlrev_b32 v127, 4, v127\n"
"buffer_load_dwordx4 v127, s[56:59], s69 offen lds\n"
"v_add_u32 v126, 64, v126\n"
"v_lshlrev_b32 v17, 6, v30\n"
"v_add_u32 v17, 131072, v17\n"
"v_lshl_add_u32 v17, %[ss], 7, v17\n"
"s_mul_i32 s75, %[rr], 28672\n"
"s_add_u32 s75, s75, 65536\n"
"v_add_u32 v125, s75, v23\n"
"v_lshl_add_u32 v124, %[ss], 10, v125\n"
"v_and_b32 v25, 0x13, v0\n"
"v_bfe_u32 v26, v0, 2, 1\n"
"v_lshl_or_b32 v25, v26, 3, v25\n"
"v_bfe_u32 v26, v0, 3, 1\n"
"v_lshl_or_b32 v25, v26, 2, v25\n"
"v_and_b32 v26, 15, v25\n"
"v_xor_b32 v26, v26, v30\n"
"v_lshlrev_b32 v25, 9, v25\n"
"v_xor_b32 v27, 0, v26\n"
"v_lshl_add_u32 v9, v27, 4, v25\n"
"v_xor_b32 v27, 2, v26\n"
"v_lshl_add_u32 v10, v27, 4, v25\n"
"v_xor_b32 v27, 4, v26\n"
"s_add_u32 m0, s70, 3072\n"
"v_mul_u32_u24 v127, 9363, v126\n"
"v_bfe_u32 v127, v127, 20, 3\n"
"v_xor_b32 v127, v127, v126\n"
"v_lshlrev_b32 v127, 4, v127\n"
"buffer_load_dwordx4 v127, s[56:59], s69 offen lds\n"
"v_add_u32 v126, 64, v126\n"
"v_lshl_add_u32 v11, v27, 4, v25\n"
"v_xor_b32 v27, 6, v26\n"
"v_lshl_add_u32 v12, v27, 4, v25\n"
"v_xor_b32 v27, 8, v26\n"
"v_lshl_add_u32 v13, v27, 4, v25\n"
"v_xor_b32 v27, 10, v26\n"
"v_lshl_add_u32 v14, v27, 4, v25\n"
"v_xor_b32 v27, 12, v26\n"
"v_lshl_add_u32 v15, v27, 4, v25\n"
"v_xor_b32 v27, 14, v26\n"
"v_lshl_add_u32 v16, v27, 4, v25\n"
"v_lshrrev_b32 v24, 2, v0\n"
"v_lshrrev_b32 v25, 4, v0\n"
"v_xor_b32 v24, v24, v25\n"
"v_and_b32 v24, 1, v24\n"
"v_cmp_eq_u32 vcc, 0, v24\n"
"v_mov_b32 v25, 0x3f803f80\n"
"s_nop 0\n"
"v_cndmask_b32 v24, 0, v25, vcc\n"
"v_accvgpr_write_b32 a176, v24\n"
"v_accvgpr_write_b32 a177, v24\n"
"s_add_u32 m0, s70, 4096\n"
"v_mul_u32_u24 v127, 9363, v126\n"
"v_bfe_u32 v127, v127, 20, 3\n"
"v_xor_b32 v127, v127, v126\n"
"v_lshlrev_b32 v127, 4, v127\n"
"buffer_load_dwordx4 v127, s[56:59], s69 offen lds\n"
"v_add_u32 v126, 64, v126\n"
"v_accvgpr_write_b32 a178, v24\n"
"v_accvgpr_write_b32 a179, v24\n"
"v_accvgpr_write_b32 a248, -1\n"
"v_accvgpr_write_b32 a249, -1\n"
"v_accvgpr_write_b32 a250, -1\n"
"v_accvgpr_write_b32 a251, -1\n"
"v_and_b32 v24, 1, v0\n"
"v_lshlrev_b32 v24, 4, v24\n"
"v_mov_b32 v25, 0x3f80\n"
"v_lshlrev_b32 v24, v24, v25\n"
"v_bfe_u32 v25, v0, 1, 3\n"
"v_cmp_eq_u32 vcc, 0, v25\n"
"s_nop 1\n"
"v_cndmask_b32 v26, 0, v24, vcc\n"
"v_accvgpr_write_b32 a180, v26\n"
"v_cmp_eq_u32 vcc, 1, v25\n"
"s_nop 1\n"
"v_cndmask_b32 v26, 0, v24, vcc\n"
"v_accvgpr_write_b32 a181, v26\n"
"v_cmp_eq_u32 vcc, 2, v25\n"
"s_nop 1\n"
"v_cndmask_b32 v26, 0, v24, vcc\n"
"v_accvgpr_write_b32 a182, v26\n"
"s_add_u32 m0, s70, 5120\n"
"v_mul_u32_u24 v127, 9363, v126\n"
"v_bfe_u32 v127, v127, 20, 3\n"
"v_xor_b32 v127, v127, v126\n"
"v_lshlrev_b32 v127, 4, v127\n"
"buffer_load_dwordx4 v127, s[56:59], s69 offen lds\n"
"v_add_u32 v126, 64, v126\n"
"v_cmp_eq_u32 vcc, 3, v25\n"
"s_nop 1\n"
"v_cndmask_b32 v26, 0, v24, vcc\n"
"v_accvgpr_write_b32 a183, v26\n"
"v_cmp_eq_u32 vcc, 4, v25\n"
"s_nop 1\n"
"v_cndmask_b32 v26, 0, v24, vcc\n"
"v_accvgpr_write_b32 a184, v26\n"
"v_cmp_eq_u32 vcc, 5, v25\n"
"s_nop 1\n"
"v_cndmask_b32 v26, 0, v24, vcc\n"
"v_accvgpr_write_b32 a185, v26\n"
"v_cmp_eq_u32 vcc, 6, v25\n"
"s_nop 1\n"
"v_cndmask_b32 v26, 0, v24, vcc\n"
"v_accvgpr_write_b32 a186, v26\n"
"v_cmp_eq_u32 vcc, 7, v25\n"
"s_nop 1\n"
"v_cndmask_b32 v26, 0, v24, vcc\n"
"v_accvgpr_write_b32 a187, v26\n"
"v_lshrrev_b32 v25, 3, v0\n"
"v_xor_b32 v25, v25, v0\n"
"v_and_b32 v25, 4, v25\n"
"v_cmp_eq_u32 vcc, 0, v25\n"
"v_bfe_u32 v25, v0, 1, 1\n"
"s_add_u32 m0, s70, 6144\n"
"v_mul_u32_u24 v127, 9363, v126\n"
"v_bfe_u32 v127, v127, 20, 3\n"
"v_xor_b32 v127, v127, v126\n"
"v_lshlrev_b32 v127, 4, v127\n"
"buffer_load_dwordx4 v127, s[56:59], s69 offen lds\n"
"v_add_u32 v126, 64, v126\n"
"v_bfe_u32 v26, v0, 3, 2\n"
"v_cndmask_b32 v24, 0, v24, vcc\n"
"v_lshl_or_b32 v25, v26, 1, v25\n"
"v_cmp_eq_u32 vcc, 0, v25\n"
"s_nop 1\n"
"v_cndmask_b32 v26, 0, v24, vcc\n"
"v_accvgpr_write_b32 a240, v26\n"
"v_cmp_eq_u32 vcc, 1, v25\n"
"s_nop 1\n"
"v_cndmask_b32 v26, 0, v24, vcc\n"
"v_accvgpr_write_b32 a241, v26\n"
"v_cmp_eq_u32 vcc, 2, v25\n"
"s_nop 1\n"
"v_cndmask_b32 v26, 0, v24, vcc\n"
"v_accvgpr_write_b32 a242, v26\n"
"v_cmp_eq_u32 vcc, 3, v25\n"
"s_nop 1\n"
"v_cndmask_b32 v26, 0, v24, vcc\n"
"v_accvgpr_write_b32 a243, v26\n"
"v_cmp_eq_u32 vcc, 4, v25\n"
"s_nop 1\n"
"v_cndmask_b32 v26, 0, v24, vcc\n"
"v_accvgpr_write_b32 a244, v26\n"
"v_cmp_eq_u32 vcc, 5, v25\n"
"s_nop 1\n"
"v_cndmask_b32 v26, 0, v24, vcc\n"
"s_add_u32 m0, s70, 7168\n"
"v_mul_u32_u24 v127, 9363, v126\n"
"v_bfe_u32 v127, v127, 20, 3\n"
"v_xor_b32 v127, v127, v126\n"
"v_lshlrev_b32 v127, 4, v127\n"
"buffer_load_dwordx4 v127, s[56:59], s69 offen lds\n"
"v_add_u32 v126, 64, v126\n"
"v_accvgpr_write_b32 a245, v26\n"
"v_cmp_eq_u32 vcc, 6, v25\n"
"s_nop 1\n"
"v_cndmask_b32 v26, 0, v24, vcc\n"
"v_accvgpr_write_b32 a246, v26\n"
"v_cmp_eq_u32 vcc, 7, v25\n"
"s_nop 1\n"
"v_cndmask_b32 v26, 0, v24, vcc\n"
"v_accvgpr_write_b32 a247, v26\n"
"s_add_u32 m0, s70, 8192\n"
"v_mul_u32_u24 v127, 9363, v126\n"
"v_bfe_u32 v127, v127, 20, 3\n"
"v_xor_b32 v127, v127, v126\n"
"v_lshlrev_b32 v127, 4, v127\n"
"buffer_load_dwordx4 v127, s[56:59], s69 offen lds\n"
"v_add_u32 v126, 64, v126\n"
"s_add_u32 m0, s70, 9216\n"
"v_mul_u32_u24 v127, 9363, v126\n"
"v_bfe_u32 v127, v127, 20, 3\n"
"v_xor_b32 v127, v127, v126\n"
"v_lshlrev_b32 v127, 4, v127\n"
"buffer_load_dwordx4 v127, s[56:59], s69 offen lds\n"
"v_add_u32 v126, 64, v126\n"
"s_add_u32 m0, s70, 10240\n"
"v_mul_u32_u24 v127, 9363, v126\n"
"v_bfe_u32 v127, v127, 20, 3\n"
"v_xor_b32 v127, v127, v126\n"
"v_lshlrev_b32 v127, 4, v127\n"
"buffer_load_dwordx4 v127, s[56:59], s69 offen lds\n"
"v_add_u32 v126, 64, v126\n"
"s_add_u32 m0, s70, 11264\n"
"v_mul_u32_u24 v127, 9363, v126\n"
"v_bfe_u32 v127, v127, 20, 3\n"
"v_xor_b32 v127, v127, v126\n"
"v_lshlrev_b32 v127, 4, v127\n"
"buffer_load_dwordx4 v127, s[56:59], s69 offen lds\n"
"v_add_u32 v126, 64, v126\n"
"s_add_u32 m0, s70, 12288\n"
"v_mul_u32_u24 v127, 9363, v126\n"
"v_bfe_u32 v127, v127, 20, 3\n"
"v_xor_b32 v127, v127, v126\n"
"v_lshlrev_b32 v127, 4, v127\n"
"buffer_load_dwordx4 v127, s[56:59], s69 offen lds\n"
"v_add_u32 v126, 64, v126\n"
"s_add_u32 m0, s70, 13312\n"
"v_mul_u32_u24 v127, 9363, v126\n"
"v_bfe_u32 v127, v127, 20, 3\n"
"v_xor_b32 v127, v127, v126\n"
"v_lshlrev_b32 v127, 4, v127\n"
"buffer_load_dwordx4 v127, s[56:59], s69 offen lds\n"
"v_add_u32 v126, 64, v126\n"
"s_waitcnt vmcnt(23)\n"
"v_readfirstlane_b32 s72, v22\n"
"s_add_u32 s72, s72, 1\n"
"s_waitcnt vmcnt(21)\n"
"v_lshlrev_b32 v48, 16, v48\n"
"s_waitcnt vmcnt(19)\n"
"v_lshlrev_b32 v49, 16, v49\n"
"s_waitcnt vmcnt(17)\n"
"v_lshlrev_b32 v50, 16, v50\n"
"s_waitcnt vmcnt(15)\n"
"v_lshlrev_b32 v51, 16, v51\n"
"v_mul_f32 v52, 0xbfb8aa3b, v48\n"
"v_mul_f32 v53, 0xbfb8aa3b, v49\n"
"v_mul_f32 v54, 0xbfb8aa3b, v50\n"
"v_mul_f32 v55, 0xbfb8aa3b, v51\n"
"v_exp_f32 v52, v52\n"
"v_exp_f32 v53, v53\n"
"v_exp_f32 v54, v54\n"
"v_exp_f32 v55, v55\n"
"v_add_f32 v52, 1.0, v52\n"
"v_add_f32 v53, 1.0, v53\n"
"v_add_f32 v54, 1.0, v54\n"
"v_add_f32 v55, 1.0, v55\n"
"v_rcp_f32 v52, v52\n"
"v_rcp_f32 v53, v53\n"
"v_rcp_f32 v54, v54\n"
"v_rcp_f32 v55, v55\n"
"v_mul_f32 v48, v48, v52\n"
"v_mul_f32 v49, v49, v53\n"
"v_mul_f32 v50, v50, v54\n"
"v_mul_f32 v51, v51, v55\n"
"v_cvt_pk_bf16_f32 v48, v48, v48\n"
"v_cvt_pk_bf16_f32 v49, v49, v49\n"
"v_cvt_pk_bf16_f32 v50, v50, v50\n"
"v_cvt_pk_bf16_f32 v51, v51, v51\n"
"v_lshlrev_b32 v48, 16, v48\n"
"v_lshlrev_b32 v49, 16, v49\n"
"v_lshlrev_b32 v50, 16, v50\n"
"v_lshlrev_b32 v51, 16, v51\n"
"v_lshlrev_b32 v24, 16, v32\n"
"v_and_b32 v25, 0xffff0000, v32\n"
"v_mul_f32 v56, v24, v48\n"
"v_mul_f32 v57, v25, v48\n"
"v_lshlrev_b32 v24, 16, v33\n"
"v_and_b32 v25, 0xffff0000, v33\n"
"v_mul_f32 v58, v24, v48\n"
"v_mul_f32 v59, v25, v48\n"
"v_lshlrev_b32 v24, 16, v34\n"
"v_and_b32 v25, 0xffff0000, v34\n"
"v_mul_f32 v60, v24, v48\n"
"v_mul_f32 v61, v25, v48\n"
"v_lshlrev_b32 v24, 16, v35\n"
"v_and_b32 v25, 0xffff0000, v35\n"
"v_mul_f32 v62, v24, v48\n"
"v_mul_f32 v63, v25, v48\n"
"v_lshlrev_b32 v24, 16, v36\n"
"v_and_b32 v25, 0xffff0000, v36\n"
"v_fmac_f32 v56, v24, v49\n"
"v_fmac_f32 v57, v25, v49\n"
"v_lshlrev_b32 v24, 16, v37\n"
"v_and_b32 v25, 0xffff0000, v37\n"
"v_fmac_f32 v58, v24, v49\n"
"v_fmac_f32 v59, v25, v49\n"
"v_lshlrev_b32 v24, 16, v38\n"
"v_and_b32 v25, 0xffff0000, v38\n"
"v_fmac_f32 v60, v24, v49\n"
"v_fmac_f32 v61, v25, v49\n"
"v_lshlrev_b32 v24, 16, v39\n"
"v_and_b32 v25, 0xffff0000, v39\n"
"v_fmac_f32 v62, v24, v49\n"
"v_fmac_f32 v63, v25, v49\n"
"v_lshlrev_b32 v24, 16, v40\n"
"v_and_b32 v25, 0xffff0000, v40\n"
"v_fmac_f32 v56, v24, v50\n"
"v_fmac_f32 v57, v25, v50\n"
"v_lshlrev_b32 v24, 16, v41\n"
"v_and_b32 v25, 0xffff0000, v41\n"
"v_fmac_f32 v58, v24, v50\n"
"v_fmac_f32 v59, v25, v50\n"
"v_lshlrev_b32 v24, 16, v42\n"
"v_and_b32 v25, 0xffff0000, v42\n"
"v_fmac_f32 v60, v24, v50\n"
"v_fmac_f32 v61, v25, v50\n"
"v_lshlrev_b32 v24, 16, v43\n"
"v_and_b32 v25, 0xffff0000, v43\n"
"v_fmac_f32 v62, v24, v50\n"
"v_fmac_f32 v63, v25, v50\n"
"v_lshlrev_b32 v24, 16, v44\n"
"v_and_b32 v25, 0xffff0000, v44\n"
"v_fmac_f32 v56, v24, v51\n"
"v_fmac_f32 v57, v25, v51\n"
"v_lshlrev_b32 v24, 16, v45\n"
"v_and_b32 v25, 0xffff0000, v45\n"
"v_fmac_f32 v58, v24, v51\n"
"v_fmac_f32 v59, v25, v51\n"
"v_lshlrev_b32 v24, 16, v46\n"
"v_and_b32 v25, 0xffff0000, v46\n"
"v_fmac_f32 v60, v24, v51\n"
"v_fmac_f32 v61, v25, v51\n"
"v_lshlrev_b32 v24, 16, v47\n"
"v_and_b32 v25, 0xffff0000, v47\n"
"v_fmac_f32 v62, v24, v51\n"
"v_fmac_f32 v63, v25, v51\n"
"v_add_f32 v56, v56, v56 quad_perm:[2,3,0,1] row_mask:0xf bank_mask:0xf\n"
"v_add_f32 v57, v57, v57 quad_perm:[2,3,0,1] row_mask:0xf bank_mask:0xf\n"
"v_add_f32 v58, v58, v58 quad_perm:[2,3,0,1] row_mask:0xf bank_mask:0xf\n"
"v_add_f32 v59, v59, v59 quad_perm:[2,3,0,1] row_mask:0xf bank_mask:0xf\n"
"v_add_f32 v60, v60, v60 quad_perm:[2,3,0,1] row_mask:0xf bank_mask:0xf\n"
"v_add_f32 v61, v61, v61 quad_perm:[2,3,0,1] row_mask:0xf bank_mask:0xf\n"
"v_add_f32 v62, v62, v62 quad_perm:[2,3,0,1] row_mask:0xf bank_mask:0xf\n"
"v_add_f32 v63, v63, v63 quad_perm:[2,3,0,1] row_mask:0xf bank_mask:0xf\n"
"v_add_f32 v56, v56, v56 row_ror:4 row_mask:0xf bank_mask:0xf\n"
"v_add_f32 v57, v57, v57 row_ror:4 row_mask:0xf bank_mask:0xf\n"
"v_add_f32 v58, v58, v58 row_ror:4 row_mask:0xf bank_mask:0xf\n"
"v_add_f32 v59, v59, v59 row_ror:4 row_mask:0xf bank_mask:0xf\n"
"v_add_f32 v60, v60, v60 row_ror:4 row_mask:0xf bank_mask:0xf\n"
"v_add_f32 v61, v61, v61 row_ror:4 row_mask:0xf bank_mask:0xf\n"
"v_add_f32 v62, v62, v62 row_ror:4 row_mask:0xf bank_mask:0xf\n"
"v_add_f32 v63, v63, v63 row_ror:4 row_mask:0xf bank_mask:0xf\n"
"v_add_f32 v56, v56, v56 row_ror:8 row_mask:0xf bank_mask:0xf\n"
"v_add_f32 v57, v57, v57 row_ror:8 row_mask:0xf bank_mask:0xf\n"
"v_add_f32 v58, v58, v58 row_ror:8 row_mask:0xf bank_mask:0xf\n"
"v_add_f32 v59, v59, v59 row_ror:8 row_mask:0xf bank_mask:0xf\n"
"v_add_f32 v60, v60, v60 row_ror:8 row_mask:0xf bank_mask:0xf\n"
"v_add_f32 v61, v61, v61 row_ror:8 row_mask:0xf bank_mask:0xf\n"
"v_add_f32 v62, v62, v62 row_ror:8 row_mask:0xf bank_mask:0xf\n"
"v_add_f32 v63, v63, v63 row_ror:8 row_mask:0xf bank_mask:0xf\n"
"v_lshrrev_b32 v24, 4, v0\n"
"v_and_b32 v25, 1, v0\n"
"v_lshlrev_b32 v25, 5, v25\n"
"v_lshl_add_u32 v25, v24, 6, v25\n"
"s_lshl_b32 s75, s71, 8\n"
"s_add_u32 s75, s75, 144384\n"
"v_add_u32 v25, s75, v25\n"
"ds_write_b128 v25, v[56:59]\n"
"ds_write_b128 v25, v[60:63] offset:16\n"
"v_and_b32 v26, 15, v0\n"
"v_lshlrev_b32 v27, 2, v26\n"
"v_lshl_add_u32 v27, v24, 8, v27\n"
"v_add_u32 v27, 144384, v27\n"
"v_and_b32 v28, 31, v0\n"
"v_bfe_u32 v29, v0, 1, 3\n"
"v_lshrrev_b32 v30, 5, v0\n"
"v_lshl_or_b32 v30, %[ss], 1, v30\n"
"v_xor_b32 v29, v29, v30\n"
"v_mul_u32_u24 v28, 896, v28\n"
"s_mul_i32 s75, %[rr], 28672\n"
"s_add_u32 s75, s75, 65536\n"
"v_add_u32 v28, s75, v28\n"
"v_xor_b32 v30, 0, v29\n"
"v_lshl_add_u32 v52, v30, 4, v28\n"
"v_xor_b32 v30, 2, v29\n"
"v_lshl_add_u32 v53, v30, 4, v28\n"
"v_xor_b32 v30, 4, v29\n"
"v_lshl_add_u32 v54, v30, 4, v28\n"
"v_xor_b32 v30, 6, v29\n"
"v_lshl_add_u32 v55, v30, 4, v28\n"
"s_waitcnt lgkmcnt(0)\n"
"s_barrier\n"
"ds_read_b32 v56, v27 offset:0\n"
"ds_read_b32 v57, v27 offset:64\n"
"ds_read_b32 v58, v27 offset:128\n"
"ds_read_b32 v59, v27 offset:192\n"
"s_lshl_b32 s75, s71, 6\n"
"v_add_u32 v24, s75, v0\n"
"v_cmp_gt_u32 vcc, 16, v24\n"
"v_lshlrev_b32 v26, 3, v26\n"
"v_add_u32 v26, %[go], v26\n"
"v_mov_b32 v25, 0x7ff00000\n"
"v_cndmask_b32 v26, v25, v26, vcc\n"
"v_mov_b32 v27, 0\n"
"v_cndmask_b32 v27, v25, v27, vcc\n"
"s_lshl_b32 s75, %[b], 2\n"
"v_mov_b32 v61, s72\n"
"s_waitcnt lgkmcnt(2)\n"
"v_add_f32 v56, v56, v57\n"
"s_waitcnt lgkmcnt(0)\n"
"v_add_f32 v58, v58, v59\n"
"v_add_f32 v56, v56, v58\n"
"v_mov_b32 v57, v56\n"
"s_nop 1\n"
"v_permlane16_swap_b32 v56, v57\n"
"v_add_f32 v56, v56, v57\n"
"v_mov_b32 v57, v56\n"
"s_nop 1\n"
"v_permlane32_swap_b32 v56, v57\n"
"v_add_f32 v56, v56, v57\n"
"v_cvt_pk_bf16_f32 v56, v56, v56\n"
"v_lshlrev_b32 v56, 16, v56\n"
"v_add_f32 v56, %[one], v56\n"
"v_cvt_pk_bf16_f32 v56, v56, v56\n"
"v_lshlrev_b32 v60, 16, v56\n"
"buffer_store_dwordx2 v[60:61], v26, s[60:63], 0 offen sc1\n"
"buffer_store_dword v61, v27, s[60:63], s75 offen offset:1024 sc1\n"
"s_lshl_b32 s75, %[T], 2\n"
"s_add_u32 s75, s75, s71\n"
"s_mul_i32 s75, s75, 28\n"
"v_min_u32 v30, 27, v0\n"
"v_add_u32 v30, s75, v30\n"
"v_lshrrev_b32 v31, 3, v30\n"
"v_mul_u32_u24 v31, 7168, v31\n"
"v_lshl_add_u32 v30, v30, 7, v31\n"
"buffer_load_dword a224, v30, s[40:43], 0 offen\n"
"s_waitcnt vmcnt(3)\n"
"s_barrier\n"
"ds_read_b128 v[64:67], v52 offset:0\n"
"ds_read_b128 v[68:71], v54 offset:0\n"
"ds_read_b128 v[72:75], v52 offset:128\n"
"ds_read_b128 v[76:79], v54 offset:128\n"
"ds_read_b128 v[80:83], v52 offset:256\n"
"ds_read_b128 v[84:87], v54 offset:256\n"
"ds_read_b128 v[88:91], v52 offset:384\n"
"ds_read_b128 v[92:95], v54 offset:384\n"
"ds_read_b128 v[96:99], v52 offset:512\n"
"ds_read_b128 v[100:103], v54 offset:512\n"
"ds_read_b128 v[104:107], v52 offset:640\n"
"ds_read_b128 v[108:111], v54 offset:640\n"
"ds_read_b128 v[112:115], v52 offset:768\n"
"ds_read_b128 v[116:119], v54 offset:768\n"
"s_waitcnt lgkmcnt(13)\n"
"v_mfma_f32_32x32x16_bf16 v[32:47], v[64:67], v[64:67], 0\n"
"s_add_u32 m0, s68, 0\n"
"s_mov_b32 s69, 0\n"
"buffer_load_dwordx4 v1, s[40:43], s69 offen offset:0 lds\n"
"v_lshlrev_b32 v128, 16, v64\n"
"v_lshlrev_b32 v130, 16, v65\n"
"v_lshlrev_b32 v132, 16, v66\n"
"v_lshlrev_b32 v134, 16, v67\n"
"v_mfma_f32_16x16x32_bf16 v[56:59], a[176:179], v[64:67], 0\n"
"v_and_b32 v129, 0xffff0000, v64\n"
"v_and_b32 v131, 0xffff0000, v65\n"
"v_and_b32 v133, 0xffff0000, v66\n"
"v_and_b32 v135, 0xffff0000, v67\n"
"s_waitcnt lgkmcnt(12)\n"
"v_mfma_f32_32x32x16_bf16 v[32:47], v[68:71], v[68:71], v[32:47]\n"
"buffer_load_dwordx4 v2, s[40:43], s69 offen offset:1024 lds\n"
"v_lshlrev_b32 v136, 16, v68\n"
"v_lshlrev_b32 v138, 16, v69\n"
"v_lshlrev_b32 v140, 16, v70\n"
"v_lshlrev_b32 v142, 16, v71\n"
"v_mfma_f32_16x16x32_bf16 v[56:59], a[176:179], v[68:71], v[56:59]\n"
"v_and_b32 v137, 0xffff0000, v68\n"
"v_and_b32 v139, 0xffff0000, v69\n"
"v_and_b32 v141, 0xffff0000, v70\n"
"v_and_b32 v143, 0xffff0000, v71\n"
"s_waitcnt lgkmcnt(11)\n"
"v_mfma_f32_32x32x16_bf16 v[32:47], v[72:75], v[72:75], v[32:47]\n"
"buffer_load_dwordx4 v3, s[40:43], s69 offen offset:2048 lds\n"
"v_lshlrev_b32 v144, 16, v72\n"
"v_lshlrev_b32 v146, 16, v73\n"
"v_lshlrev_b32 v148, 16, v74\n"
"v_lshlrev_b32 v150, 16, v75\n"
"v_mfma_f32_16x16x32_bf16 v[56:59], a[176:179], v[72:75], v[56:59]\n"
"v_and_b32 v145, 0xffff0000, v72\n"
"v_and_b32 v147, 0xffff0000, v73\n"
"v_and_b32 v149, 0xffff0000, v74\n"
"v_and_b32 v151, 0xffff0000, v75\n"
"s_waitcnt lgkmcnt(10)\n"
"v_mfma_f32_32x32x16_bf16 v[32:47], v[76:79], v[76:79], v[32:47]\n"
"buffer_load_dwordx4 v4, s[40:43], s69 offen offset:3072 lds\n"
"v_lshlrev_b32 v152, 16, v76\n"
"v_lshlrev_b32 v154, 16, v77\n"
"v_lshlrev_b32 v156, 16, v78\n"
"v_lshlrev_b32 v158, 16, v79\n"
"v_mfma_f32_16x16x32_bf16 v[56:59], a[176:179], v[76:79], v[56:59]\n"
"v_and_b32 v153, 0xffff0000, v76\n"
"v_and_b32 v155, 0xffff0000, v77\n"
"v_and_b32 v157, 0xffff0000, v78\n"
"v_and_b32 v159, 0xffff0000, v79\n"
"s_waitcnt lgkmcnt(9)\n"
"v_mfma_f32_32x32x16_bf16 v[32:47], v[80:83], v[80:83], v[32:47]\n"
"s_add_u32 m0, s68, 16384\n"
"s_mov_b32 s69, 131072\n"
"buffer_load_dwordx4 v1, s[40:43], s69 offen offset:0 lds\n"
"v_lshlrev_b32 v160, 16, v80\n"
"v_lshlrev_b32 v162, 16, v81\n"
"v_lshlrev_b32 v164, 16, v82\n"
"v_lshlrev_b32 v166, 16, v83\n"
"v_mfma_f32_16x16x32_bf16 v[56:59], a[176:179], v[80:83], v[56:59]\n"
"v_and_b32 v161, 0xffff0000, v80\n"
"v_and_b32 v163, 0xffff0000, v81\n"
"v_and_b32 v165, 0xffff0000, v82\n"
"v_and_b32 v167, 0xffff0000, v83\n"
"s_waitcnt lgkmcnt(8)\n"
"v_mfma_f32_32x32x16_bf16 v[32:47], v[84:87], v[84:87], v[32:47]\n"
"buffer_load_dwordx4 v2, s[40:43], s69 offen offset:1024 lds\n"
"v_lshlrev_b32 v168, 16, v84\n"
"v_lshlrev_b32 v170, 16, v85\n"
"v_lshlrev_b32 v172, 16, v86\n"
"v_lshlrev_b32 v174, 16, v87\n"
"v_mfma_f32_16x16x32_bf16 v[56:59], a[176:179], v[84:87], v[56:59]\n"
"v_and_b32 v169, 0xffff0000, v84\n"
"v_and_b32 v171, 0xffff0000, v85\n"
"v_and_b32 v173, 0xffff0000, v86\n"
"v_and_b32 v175, 0xffff0000, v87\n"
"s_waitcnt lgkmcnt(7)\n"
"v_mfma_f32_32x32x16_bf16 v[32:47], v[88:91], v[88:91], v[32:47]\n"
"buffer_load_dwordx4 v3, s[40:43], s69 offen offset:2048 lds\n"
"v_lshlrev_b32 v176, 16, v88\n"
"v_lshlrev_b32 v178, 16, v89\n"
"v_lshlrev_b32 v180, 16, v90\n"
"v_lshlrev_b32 v182, 16, v91\n"
"v_mfma_f32_16x16x32_bf16 v[56:59], a[176:179], v[88:91], v[56:59]\n"
"v_and_b32 v177, 0xffff0000, v88\n"
"v_and_b32 v179, 0xffff0000, v89\n"
"v_and_b32 v181, 0xffff0000, v90\n"
"v_and_b32 v183, 0xffff0000, v91\n"
"s_waitcnt lgkmcnt(6)\n"
"v_mfma_f32_32x32x16_bf16 v[32:47], v[92:95], v[92:95], v[32:47]\n"
"buffer_load_dwordx4 v4, s[40:43], s69 offen offset:3072 lds\n"
"v_lshlrev_b32 v184, 16, v92\n"
"v_lshlrev_b32 v186, 16, v93\n"
"v_lshlrev_b32 v188, 16, v94\n"
"v_lshlrev_b32 v190, 16, v95\n"
"v_mfma_f32_16x16x32_bf16 v[56:59], a[176:179], v[92:95], v[56:59]\n"
"v_and_b32 v185, 0xffff0000, v92\n"
"v_and_b32 v187, 0xffff0000, v93\n"
"v_and_b32 v189, 0xffff0000, v94\n"
"v_and_b32 v191, 0xffff0000, v95\n"
"s_waitcnt lgkmcnt(5)\n"
"v_mfma_f32_32x32x16_bf16 v[32:47], v[96:99], v[96:99], v[32:47]\n"
"s_add_u32 m0, s68, 32768\n"
"s_mov_b32 s69, 262144\n"
"buffer_load_dwordx4 v1, s[40:43], s69 offen offset:0 lds\n"
"v_lshlrev_b32 v192, 16, v96\n"
"v_lshlrev_b32 v194, 16, v97\n"
"v_lshlrev_b32 v196, 16, v98\n"
"v_lshlrev_b32 v198, 16, v99\n"
"v_mfma_f32_16x16x32_bf16 v[56:59], a[176:179], v[96:99], v[56:59]\n"
"v_and_b32 v193, 0xffff0000, v96\n"
"v_and_b32 v195, 0xffff0000, v97\n"
"v_and_b32 v197, 0xffff0000, v98\n"
"v_and_b32 v199, 0xffff0000, v99\n"
"s_waitcnt lgkmcnt(4)\n"
"v_mfma_f32_32x32x16_bf16 v[32:47], v[100:103], v[100:103], v[32:47]\n"
"buffer_load_dwordx4 v2, s[40:43], s69 offen offset:1024 lds\n"
"v_lshlrev_b32 v200, 16, v100\n"
"v_lshlrev_b32 v202, 16, v101\n"
"v_lshlrev_b32 v204, 16, v102\n"
"v_lshlrev_b32 v206, 16, v103\n"
"v_mfma_f32_16x16x32_bf16 v[56:59], a[176:179], v[100:103], v[56:59]\n"
"v_and_b32 v201, 0xffff0000, v100\n"
"v_and_b32 v203, 0xffff0000, v101\n"
"v_and_b32 v205, 0xffff0000, v102\n"
"v_and_b32 v207, 0xffff0000, v103\n"
"s_waitcnt lgkmcnt(3)\n"
"v_mfma_f32_32x32x16_bf16 v[32:47], v[104:107], v[104:107], v[32:47]\n"
"buffer_load_dwordx4 v3, s[40:43], s69 offen offset:2048 lds\n"
"v_lshlrev_b32 v208, 16, v104\n"
"v_lshlrev_b32 v210, 16, v105\n"
"v_lshlrev_b32 v212, 16, v106\n"
"v_lshlrev_b32 v214, 16, v107\n"
"v_mfma_f32_16x16x32_bf16 v[56:59], a[176:179], v[104:107], v[56:59]\n"
"v_and_b32 v209, 0xffff0000, v104\n"
"v_and_b32 v211, 0xffff0000, v105\n"
"v_and_b32 v213, 0xffff0000, v106\n"
"v_and_b32 v215, 0xffff0000, v107\n"
"s_waitcnt lgkmcnt(2)\n"
"v_mfma_f32_32x32x16_bf16 v[32:47], v[108:111], v[108:111], v[32:47]\n"
"buffer_load_dwordx4 v4, s[40:43], s69 offen offset:3072 lds\n"
"v_lshlrev_b32 v216, 16, v108\n"
"v_lshlrev_b32 v218, 16, v109\n"
"v_lshlrev_b32 v220, 16, v110\n"
"v_lshlrev_b32 v222, 16, v111\n"
"v_mfma_f32_16x16x32_bf16 v[56:59], a[176:179], v[108:111], v[56:59]\n"
"v_and_b32 v217, 0xffff0000, v108\n"
"v_and_b32 v219, 0xffff0000, v109\n"
"v_and_b32 v221, 0xffff0000, v110\n"
"v_and_b32 v223, 0xffff0000, v111\n"
"s_waitcnt lgkmcnt(1)\n"
"v_mfma_f32_32x32x16_bf16 v[32:47], v[112:115], v[112:115], v[32:47]\n"
"s_add_u32 m0, s68, 49152\n"
"s_mov_b32 s69, 393216\n"
"buffer_load_dwordx4 v1, s[40:43], s69 offen offset:0 lds\n"
"v_lshlrev_b32 v224, 16, v112\n"
"v_lshlrev_b32 v226, 16, v113\n"
"v_lshlrev_b32 v228, 16, v114\n"
"v_lshlrev_b32 v230, 16, v115\n"
"v_mfma_f32_16x16x32_bf16 v[56:59], a[176:179], v[112:115], v[56:59]\n"
"v_and_b32 v225, 0xffff0000, v112\n"
"v_and_b32 v227, 0xffff0000, v113\n"
"v_and_b32 v229, 0xffff0000, v114\n"
"v_and_b32 v231, 0xffff0000, v115\n"
"s_waitcnt lgkmcnt(0)\n"
"v_mfma_f32_32x32x16_bf16 v[32:47], v[116:119], v[116:119], v[32:47]\n"
"buffer_load_dwordx4 v2, s[40:43], s69 offen offset:1024 lds\n"
"v_lshlrev_b32 v232, 16, v116\n"
"v_lshlrev_b32 v234, 16, v117\n"
"v_lshlrev_b32 v236, 16, v118\n"
"v_lshlrev_b32 v238, 16, v119\n"
"v_mfma_f32_16x16x32_bf16 v[56:59], a[176:179], v[116:119], v[56:59]\n"
"v_and_b32 v233, 0xffff0000, v116\n"
"v_and_b32 v235, 0xffff0000, v117\n"
"v_and_b32 v237, 0xffff0000, v118\n"
"v_and_b32 v239, 0xffff0000, v119\n"
"buffer_load_dwordx4 v3, s[40:43], s69 offen offset:2048 lds\n"
"buffer_load_dwordx4 v4, s[40:43], s69 offen offset:3072 lds\n"
"s_lshl_b32 s74, s71, 10\n"
"s_add_u32 s74, s74, 102400\n"
"s_add_u32 s70, s74, 4096\n"
"s_lshl_b32 s75, %[T], 2\n"
"s_add_u32 s75, s75, s71\n"
"s_mul_i32 s75, s75, 16\n"
"v_min_u32 v30, 15, v0\n"
"v_add_u32 v30, s75, v30\n"
"v_lshrrev_b32 v31, 0, v30\n"
"v_mul_u32_u24 v31, 0, v31\n"
"v_lshl_add_u32 v30, v30, 7, v31\n"
"buffer_load_dword a225, v30, s[44:47], 0 offen\n"
"v_and_b32 v24, 1, v0\n"
"v_cmp_ne_u32 vcc, 0, v24\n"
"s_nop 1\n"
"v_cndmask_b32 v32, v32, v33, vcc\n"
"v_cndmask_b32 v33, v34, v35, vcc\n"
"v_cndmask_b32 v34, v36, v37, vcc\n"
"v_cndmask_b32 v35, v38, v39, vcc\n"
"v_cndmask_b32 v36, v40, v41, vcc\n"
"v_cndmask_b32 v37, v42, v43, vcc\n"
"v_cndmask_b32 v38, v44, v45, vcc\n"
"v_cndmask_b32 v39, v46, v47, vcc\n"
"v_and_b32 v24, 2, v0\n"
"v_cmp_ne_u32 vcc, 0, v24\n"
"s_nop 1\n"
"v_cndmask_b32 v32, v32, v33, vcc\n"
"v_cndmask_b32 v33, v34, v35, vcc\n"
"v_cndmask_b32 v34, v36, v37, vcc\n"
"v_cndmask_b32 v35, v38, v39, vcc\n"
"v_and_b32 v24, 8, v0\n"
"v_cmp_ne_u32 vcc, 0, v24\n"
"s_nop 1\n"
"v_cndmask_b32 v32, v32, v33, vcc\n"
"v_cndmask_b32 v33, v34, v35, vcc\n"
"v_and_b32 v24, 16, v0\n"
"v_cmp_ne_u32 vcc, 0, v24\n"
"s_nop 1\n"
"v_cndmask_b32 v32, v32, v33, vcc\n"
"v_lshrrev_b32 v24, 3, v0\n"
"v_xor_b32 v24, v24, v0\n"
"v_and_b32 v24, 4, v24\n"
"v_cmp_eq_u32 vcc, 0, v24\n"
"s_nop 1\n"
"v_cndmask_b32 v58, 0, v32, vcc\n"
"v_mov_b32 v59, v58\n"
"s_nop 1\n"
"v_permlane32_swap_b32 v58, v59\n"
"v_add_f32 v57, v58, v59\n"
"s_lshl_b32 s75, s71, 9\n"
"s_add_u32 s75, s75, 146432\n"
"v_lshl_add_u32 v24, v0, 3, s75\n"
"ds_write_b64 v24, v[56:57]\n"
"v_xor_b32 v24, 0x200, v24\n"
"s_waitcnt lgkmcnt(0)\n"
"s_barrier\n"
"ds_read_b64 v[60:61], v24\n"
"buffer_load_dwordx4 v[240:243], v23, s[60:63], s74 offen sc1\n"
"buffer_load_dwordx4 v[244:247], v23, s[60:63], s70 offen sc1\n"
"s_waitcnt lgkmcnt(0)\n"
"v_add_f32 v56, v56, v60\n"
"v_add_f32 v58, v57, v61\n"
"v_mul_f32 v56, 0x3b124925, v56\n"
"v_mul_f32 v57, v56, v56\n"
"v_mul_f32 v58, 0x3b124925, v58\n"
"v_sub_f32 v58, v58, v57\n"
"v_max_f32 v58, 0, v58\n"
"v_add_f32 v58, 0x3727c5ac, v58\n"
"v_rsq_f32 v58, v58\n"
"s_nop 0\n"
"v_mul_f32 v56, v56, v58\n"
"v_sub_f32 v56, 0, v56\n"
"v_fma_f32 v128, v128, v58, v56\n"
"v_fma_f32 v129, v129, v58, v56\n"
"v_fma_f32 v130, v130, v58, v56\n"
"v_fma_f32 v131, v131, v58, v56\n"
"v_fma_f32 v132, v132, v58, v56\n"
"v_fma_f32 v133, v133, v58, v56\n"
"v_fma_f32 v134, v134, v58, v56\n"
"v_fma_f32 v135, v135, v58, v56\n"
"v_fma_f32 v136, v136, v58, v56\n"
"v_fma_f32 v137, v137, v58, v56\n"
"v_fma_f32 v138, v138, v58, v56\n"
"v_fma_f32 v139, v139, v58, v56\n"
"v_fma_f32 v140, v140, v58, v56\n"
"v_fma_f32 v141, v141, v58, v56\n"
"v_fma_f32 v142, v142, v58, v56\n"
"v_fma_f32 v143, v143, v58, v56\n"
"v_fma_f32 v144, v144, v58, v56\n"
"v_fma_f32 v145, v145, v58, v56\n"
"v_fma_f32 v146, v146, v58, v56\n"
"v_fma_f32 v147, v147, v58, v56\n"
"v_fma_f32 v148, v148, v58, v56\n"
"v_fma_f32 v149, v149, v58, v56\n"
"v_fma_f32 v150, v150, v58, v56\n"
"v_fma_f32 v151, v151, v58, v56\n"
"v_fma_f32 v152, v152, v58, v56\n"
"v_fma_f32 v153, v153, v58, v56\n"
"v_fma_f32 v154, v154, v58, v56\n"
"v_fma_f32 v155, v155, v58, v56\n"
"v_fma_f32 v156, v156, v58, v56\n"
"v_fma_f32 v157, v157, v58, v56\n"
"v_fma_f32 v158, v158, v58, v56\n"
"v_fma_f32 v159, v159, v58, v56\n"
"v_fma_f32 v160, v160, v58, v56\n"
"v_fma_f32 v161, v161, v58, v56\n"
"v_fma_f32 v162, v162, v58, v56\n"
"v_fma_f32 v163, v163, v58, v56\n"
"v_fma_f32 v164, v164, v58, v56\n"
"v_fma_f32 v165, v165, v58, v56\n"
"v_fma_f32 v166, v166, v58, v56\n"
"v_fma_f32 v167, v167, v58, v56\n"
"v_fma_f32 v168, v168, v58, v56\n"
"v_fma_f32 v169, v169, v58, v56\n"
"v_fma_f32 v170, v170, v58, v56\n"
"v_fma_f32 v171, v171, v58, v56\n"
"v_fma_f32 v172, v172, v58, v56\n"
"v_fma_f32 v173, v173, v58, v56\n"
"v_fma_f32 v174, v174, v58, v56\n"
"v_fma_f32 v175, v175, v58, v56\n"
"v_fma_f32 v176, v176, v58, v56\n"
"v_fma_f32 v177, v177, v58, v56\n"
"v_fma_f32 v178, v178, v58, v56\n"
"v_fma_f32 v179, v179, v58, v56\n"
"v_fma_f32 v180, v180, v58, v56\n"
"v_fma_f32 v181, v181, v58, v56\n"
"v_fma_f32 v182, v182, v58, v56\n"
"v_fma_f32 v183, v183, v58, v56\n"
"v_fma_f32 v184, v184, v58, v56\n"
"v_fma_f32 v185, v185, v58, v56\n"
"v_fma_f32 v186, v186, v58, v56\n"
"v_fma_f32 v187, v187, v58, v56\n"
"v_fma_f32 v188, v188, v58, v56\n"
"v_fma_f32 v189, v189, v58, v56\n"
"v_fma_f32 v190, v190, v58, v56\n"
"v_fma_f32 v191, v191, v58, v56\n"
"v_fma_f32 v192, v192, v58, v56\n"
"v_fma_f32 v193, v193, v58, v56\n"
"v_fma_f32 v194, v194, v58, v56\n"
"v_fma_f32 v195, v195, v58, v56\n"
"v_fma_f32 v196, v196, v58, v56\n"
"v_fma_f32 v197, v197, v58, v56\n"
"v_fma_f32 v198, v198, v58, v56\n"
"v_fma_f32 v199, v199, v58, v56\n"
"v_fma_f32 v200, v200, v58, v56\n"
"v_fma_f32 v201, v201, v58, v56\n"
"v_fma_f32 v202, v202, v58, v56\n"
"v_fma_f32 v203, v203, v58, v56\n"
"v_fma_f32 v204, v204, v58, v56\n"
"v_fma_f32 v205, v205, v58, v56\n"
"v_fma_f32 v206, v206, v58, v56\n"
"v_fma_f32 v207, v207, v58, v56\n"
"v_fma_f32 v208, v208, v58, v56\n"
"v_fma_f32 v209, v209, v58, v56\n"
"v_fma_f32 v210, v210, v58, v56\n"
"v_fma_f32 v211, v211, v58, v56\n"
"v_fma_f32 v212, v212, v58, v56\n"
"v_fma_f32 v213, v213, v58, v56\n"
"v_fma_f32 v214, v214, v58, v56\n"
"v_fma_f32 v215, v215, v58, v56\n"
"v_fma_f32 v216, v216, v58, v56\n"
"v_fma_f32 v217, v217, v58, v56\n"
"v_fma_f32 v218, v218, v58, v56\n"
"v_fma_f32 v219, v219, v58, v56\n"
"v_fma_f32 v220, v220, v58, v56\n"
"v_fma_f32 v221, v221, v58, v56\n"
"v_fma_f32 v222, v222, v58, v56\n"
"v_fma_f32 v223, v223, v58, v56\n"
"v_fma_f32 v224, v224, v58, v56\n"
"v_fma_f32 v225, v225, v58, v56\n"
"v_fma_f32 v226, v226, v58, v56\n"
"v_fma_f32 v227, v227, v58, v56\n"
"v_fma_f32 v228, v228, v58, v56\n"
"v_fma_f32 v229, v229, v58, v56\n"
"v_fma_f32 v230, v230, v58, v56\n"
"v_fma_f32 v231, v231, v58, v56\n"
"v_fma_f32 v232, v232, v58, v56\n"
"v_fma_f32 v233, v233, v58, v56\n"
"v_fma_f32 v234, v234, v58, v56\n"
"v_fma_f32 v235, v235, v58, v56\n"
"v_fma_f32 v236, v236, v58, v56\n"
"v_fma_f32 v237, v237, v58, v56\n"
"v_fma_f32 v238, v238, v58, v56\n"
"v_fma_f32 v239, v239, v58, v56\n"
"s_lshl_b32 s75, s71, 7\n"
"v_lshl_add_u32 v24, v0, 1, s75\n"
"v_add_u32 v24, 0, v24\n"
"v_cmp_gt_u32 vcc, 448, v24\n"
"v_subrev_u32 v25, 448, v24\n"
"v_mov_b32 v27, 32\n"
"v_cndmask_b32 v24, v25, v24, vcc\n"
"v_cndmask_b32 v27, 0, v27, vcc\n"
"v_lshrrev_b32 v25, 3, v24\n"
"v_and_b32 v24, 7, v24\n"
"v_lshl_add_u32 v24, v24, 2, v27\n"
"v_lshl_add_u32 v24, v25, 6, v24\n"
"v_add_u32 v20, 131072, v24\n"
"s_lshl_b32 s75, s71, 7\n"
"v_lshl_add_u32 v24, v0, 1, s75\n"
"v_add_u32 v24, 512, v24\n"
"v_cmp_gt_u32 vcc, 448, v24\n"
"v_subrev_u32 v25, 448, v24\n"
"v_mov_b32 v27, 32\n"
"v_cndmask_b32 v24, v25, v24, vcc\n"
"v_cndmask_b32 v27, 0, v27, vcc\n"
"v_lshrrev_b32 v25, 3, v24\n"
"v_and_b32 v24, 7, v24\n"
"v_lshl_add_u32 v24, v24, 2, v27\n"
"v_lshl_add_u32 v24, v25, 6, v24\n"
"v_add_u32 v21, 131072, v24\n"
"s_waitcnt vmcnt(1)\n"
"v_xor_b32 v241, s72, v241\n"
"v_xor_b32 v243, s72, v243\n"
"s_waitcnt vmcnt(0)\n"
"v_xor_b32 v245, s72, v245\n"
"v_xor_b32 v247, s72, v247\n"
"v_or_b32 v24, v241, v243\n"
"v_or3_b32 v24, v245, v247, v24\n"
"v_cmp_ne_u32 vcc, 0, v24\n"
"s_cmp_eq_u64 vcc, 0\n"
"s_cbranch_scc1 .Lok0_%=\n"
"s_movk_i32 s84, 0x7000\n"
".Lsp0_%=:\n"
"buffer_load_dwordx4 v[240:243], v23, s[60:63], s74 offen sc1\n"
"buffer_load_dwordx4 v[244:247], v23, s[60:63], s70 offen sc1\n"
"s_waitcnt vmcnt(0)\n"
"v_xor_b32 v241, s72, v241\n"
"v_xor_b32 v243, s72, v243\n"
"v_xor_b32 v245, s72, v245\n"
"v_xor_b32 v247, s72, v247\n"
"v_or_b32 v24, v241, v243\n"
"v_or3_b32 v24, v245, v247, v24\n"
"v_cmp_ne_u32 vcc, 0, v24\n"
"s_cmp_eq_u64 vcc, 0\n"
"s_cbranch_scc1 .Lok0_%=\n"
"s_sub_u32 s84, s84, 1\n"
"s_cmp_lg_u32 s84, 0\n"
"s_cbranch_scc1 .Lsp0_%=\n"
".Lok0_%=:\n"
"ds_write2_b32 v20, v240, v242 offset0:0 offset1:1\n"
"ds_write2_b32 v21, v244, v246 offset0:0 offset1:1\n"
"s_waitcnt lgkmcnt(0)\n"
"s_barrier\n"
"ds_read_b128 v[32:35], v17 offset:0\n"
"ds_read_b128 v[36:39], v17 offset:16\n"
"ds_read_b128 v[40:43], v17 offset:32\n"
"ds_read_b128 v[44:47], v17 offset:48\n"
"ds_read_b128 v[48:51], v17 offset:256\n"
"ds_read_b128 v[52:55], v17 offset:272\n"
"ds_read_b128 v[56:59], v17 offset:288\n"
"ds_read_b128 v[60:63], v17 offset:304\n"
"s_waitcnt lgkmcnt(5)\n"
"v_fma_f32 v128, v128, v32, v40\n"
"v_fma_f32 v129, v129, v33, v41\n"
"v_fma_f32 v130, v130, v34, v42\n"
"v_fma_f32 v131, v131, v35, v43\n"
"s_waitcnt lgkmcnt(4)\n"
"v_fma_f32 v132, v132, v36, v44\n"
"v_fma_f32 v133, v133, v37, v45\n"
"v_fma_f32 v134, v134, v38, v46\n"
"v_fma_f32 v135, v135, v39, v47\n"
"ds_read_b128 v[32:35], v17 offset:512\n"
"ds_read_b128 v[36:39], v17 offset:528\n"
"ds_read_b128 v[40:43], v17 offset:544\n"
"ds_read_b128 v[44:47], v17 offset:560\n"
"v_cvt_pk_bf16_f32 v128, v128, v129\n"
"v_cvt_pk_bf16_f32 v129, v130, v131\n"
"v_cvt_pk_bf16_f32 v130, v132, v133\n"
"v_cvt_pk_bf16_f32 v131, v134, v135\n"
"ds_write_b128 v124, v[128:131] offset:0\n"
"s_waitcnt lgkmcnt(6)\n"
"v_fma_f32 v136, v136, v48, v56\n"
"v_fma_f32 v137, v137, v49, v57\n"
"v_fma_f32 v138, v138, v50, v58\n"
"v_fma_f32 v139, v139, v51, v59\n"
"s_waitcnt lgkmcnt(5)\n"
"v_fma_f32 v140, v140, v52, v60\n"
"v_fma_f32 v141, v141, v53, v61\n"
"v_fma_f32 v142, v142, v54, v62\n"
"v_fma_f32 v143, v143, v55, v63\n"
"ds_read_b128 v[48:51], v17 offset:768\n"
"ds_read_b128 v[52:55], v17 offset:784\n"
"ds_read_b128 v[56:59], v17 offset:800\n"
"ds_read_b128 v[60:63], v17 offset:816\n"
"v_cvt_pk_bf16_f32 v136, v136, v137\n"
"v_cvt_pk_bf16_f32 v137, v138, v139\n"
"v_cvt_pk_bf16_f32 v138, v140, v141\n"
"v_cvt_pk_bf16_f32 v139, v142, v143\n"
"ds_write_b128 v124, v[136:139] offset:2048\n"
"s_waitcnt lgkmcnt(7)\n"
"v_fma_f32 v144, v144, v32, v40\n"
"v_fma_f32 v145, v145, v33, v41\n"
"v_fma_f32 v146, v146, v34, v42\n"
"v_fma_f32 v147, v147, v35, v43\n"
"s_waitcnt lgkmcnt(6)\n"
"v_fma_f32 v148, v148, v36, v44\n"
"v_fma_f32 v149, v149, v37, v45\n"
"v_fma_f32 v150, v150, v38, v46\n"
"v_fma_f32 v151, v151, v39, v47\n"
"ds_read_b128 v[32:35], v17 offset:1024\n"
"ds_read_b128 v[36:39], v17 offset:1040\n"
"ds_read_b128 v[40:43], v17 offset:1056\n"
"ds_read_b128 v[44:47], v17 offset:1072\n"
"v_cvt_pk_bf16_f32 v144, v144, v145\n"
"v_cvt_pk_bf16_f32 v145, v146, v147\n"
"v_cvt_pk_bf16_f32 v146, v148, v149\n"
"v_cvt_pk_bf16_f32 v147, v150, v151\n"
"ds_write_b128 v124, v[144:147] offset:4096\n"
"s_waitcnt lgkmcnt(7)\n"
"v_fma_f32 v152, v152, v48, v56\n"
"v_fma_f32 v153, v153, v49, v57\n"
"v_fma_f32 v154, v154, v50, v58\n"
"v_fma_f32 v155, v155, v51, v59\n"
"s_waitcnt lgkmcnt(6)\n"
"v_fma_f32 v156, v156, v52, v60\n"
"v_fma_f32 v157, v157, v53, v61\n"
"v_fma_f32 v158, v158, v54, v62\n"
"v_fma_f32 v159, v159, v55, v63\n"
"ds_read_b128 v[48:51], v17 offset:1280\n"
"ds_read_b128 v[52:55], v17 offset:1296\n"
"ds_read_b128 v[56:59], v17 offset:1312\n"
"ds_read_b128 v[60:63], v17 offset:1328\n"
"v_cvt_pk_bf16_f32 v152, v152, v153\n"
"v_cvt_pk_bf16_f32 v153, v154, v155\n"
"v_cvt_pk_bf16_f32 v154, v156, v157\n"
"v_cvt_pk_bf16_f32 v155, v158, v159\n"
"ds_write_b128 v124, v[152:155] offset:6144\n"
"s_waitcnt lgkmcnt(7)\n"
"v_fma_f32 v160, v160, v32, v40\n"
"v_fma_f32 v161, v161, v33, v41\n"
"v_fma_f32 v162, v162, v34, v42\n"
"v_fma_f32 v163, v163, v35, v43\n"
"s_waitcnt lgkmcnt(6)\n"
"v_fma_f32 v164, v164, v36, v44\n"
"v_fma_f32 v165, v165, v37, v45\n"
"v_fma_f32 v166, v166, v38, v46\n"
"v_fma_f32 v167, v167, v39, v47\n"
"ds_read_b128 v[32:35], v17 offset:1536\n"
"ds_read_b128 v[36:39], v17 offset:1552\n"
"ds_read_b128 v[40:43], v17 offset:1568\n"
"ds_read_b128 v[44:47], v17 offset:1584\n"
"v_cvt_pk_bf16_f32 v160, v160, v161\n"
"v_cvt_pk_bf16_f32 v161, v162, v163\n"
"v_cvt_pk_bf16_f32 v162, v164, v165\n"
"v_cvt_pk_bf16_f32 v163, v166, v167\n"
"ds_write_b128 v124, v[160:163] offset:8192\n"
"s_waitcnt lgkmcnt(7)\n"
"v_fma_f32 v168, v168, v48, v56\n"
"v_fma_f32 v169, v169, v49, v57\n"
"v_fma_f32 v170, v170, v50, v58\n"
"v_fma_f32 v171, v171, v51, v59\n"
"s_waitcnt lgkmcnt(6)\n"
"v_fma_f32 v172, v172, v52, v60\n"
"v_fma_f32 v173, v173, v53, v61\n"
"v_fma_f32 v174, v174, v54, v62\n"
"v_fma_f32 v175, v175, v55, v63\n"
"ds_read_b128 v[48:51], v17 offset:1792\n"
"ds_read_b128 v[52:55], v17 offset:1808\n"
"ds_read_b128 v[56:59], v17 offset:1824\n"
"ds_read_b128 v[60:63], v17 offset:1840\n"
"v_cvt_pk_bf16_f32 v168, v168, v169\n"
"v_cvt_pk_bf16_f32 v169, v170, v171\n"
"v_cvt_pk_bf16_f32 v170, v172, v173\n"
"v_cvt_pk_bf16_f32 v171, v174, v175\n"
"ds_write_b128 v124, v[168:171] offset:10240\n"
"s_waitcnt lgkmcnt(7)\n"
"v_fma_f32 v176, v176, v32, v40\n"
"v_fma_f32 v177, v177, v33, v41\n"
"v_fma_f32 v178, v178, v34, v42\n"
"v_fma_f32 v179, v179, v35, v43\n"
"s_waitcnt lgkmcnt(6)\n"
"v_fma_f32 v180, v180, v36, v44\n"
"v_fma_f32 v181, v181, v37, v45\n"
"v_fma_f32 v182, v182, v38, v46\n"
"v_fma_f32 v183, v183, v39, v47\n"
"ds_read_b128 v[32:35], v17 offset:2048\n"
"ds_read_b128 v[36:39], v17 offset:2064\n"
"ds_read_b128 v[40:43], v17 offset:2080\n"
"ds_read_b128 v[44:47], v17 offset:2096\n"
"v_cvt_pk_bf16_f32 v176, v176, v177\n"
"v_cvt_pk_bf16_f32 v177, v178, v179\n"
"v_cvt_pk_bf16_f32 v178, v180, v181\n"
"v_cvt_pk_bf16_f32 v179, v182, v183\n"
"ds_write_b128 v124, v[176:179] offset:12288\n"
"s_waitcnt lgkmcnt(7)\n"
"v_fma_f32 v184, v184, v48, v56\n"
"v_fma_f32 v185, v185, v49, v57\n"
"v_fma_f32 v186, v186, v50, v58\n"
"v_fma_f32 v187, v187, v51, v59\n"
"s_waitcnt lgkmcnt(6)\n"
"v_fma_f32 v188, v188, v52, v60\n"
"v_fma_f32 v189, v189, v53, v61\n"
"v_fma_f32 v190, v190, v54, v62\n"
"v_fma_f32 v191, v191, v55, v63\n"
"ds_read_b128 v[48:51], v17 offset:2304\n"
"ds_read_b128 v[52:55], v17 offset:2320\n"
"ds_read_b128 v[56:59], v17 offset:2336\n"
"ds_read_b128 v[60:63], v17 offset:2352\n"
"v_cvt_pk_bf16_f32 v184, v184, v185\n"
"v_cvt_pk_bf16_f32 v185, v186, v187\n"
"v_cvt_pk_bf16_f32 v186, v188, v189\n"
"v_cvt_pk_bf16_f32 v187, v190, v191\n"
"ds_write_b128 v124, v[184:187] offset:14336\n"
"s_waitcnt lgkmcnt(7)\n"
"v_fma_f32 v192, v192, v32, v40\n"
"v_fma_f32 v193, v193, v33, v41\n"
"v_fma_f32 v194, v194, v34, v42\n"
"v_fma_f32 v195, v195, v35, v43\n"
"s_waitcnt lgkmcnt(6)\n"
"v_fma_f32 v196, v196, v36, v44\n"
"v_fma_f32 v197, v197, v37, v45\n"
"v_fma_f32 v198, v198, v38, v46\n"
"v_fma_f32 v199, v199, v39, v47\n"
"ds_read_b128 v[32:35], v17 offset:2560\n"
"ds_read_b128 v[36:39], v17 offset:2576\n"
"ds_read_b128 v[40:43], v17 offset:2592\n"
"ds_read_b128 v[44:47], v17 offset:2608\n"
"v_cvt_pk_bf16_f32 v192, v192, v193\n"
"v_cvt_pk_bf16_f32 v193, v194, v195\n"
"v_cvt_pk_bf16_f32 v194, v196, v197\n"
"v_cvt_pk_bf16_f32 v195, v198, v199\n"
"ds_write_b128 v124, v[192:195] offset:16384\n"
"s_waitcnt lgkmcnt(7)\n"
"v_fma_f32 v200, v200, v48, v56\n"
"v_fma_f32 v201, v201, v49, v57\n"
"v_fma_f32 v202, v202, v50, v58\n"
"v_fma_f32 v203, v203, v51, v59\n"
"s_waitcnt lgkmcnt(6)\n"
"v_fma_f32 v204, v204, v52, v60\n"
"v_fma_f32 v205, v205, v53, v61\n"
"v_fma_f32 v206, v206, v54, v62\n"
"v_fma_f32 v207, v207, v55, v63\n"
"ds_read_b128 v[48:51], v17 offset:2816\n"
"ds_read_b128 v[52:55], v17 offset:2832\n"
"ds_read_b128 v[56:59], v17 offset:2848\n"
"ds_read_b128 v[60:63], v17 offset:2864\n"
"v_cvt_pk_bf16_f32 v200, v200, v201\n"
"v_cvt_pk_bf16_f32 v201, v202, v203\n"
"v_cvt_pk_bf16_f32 v202, v204, v205\n"
"v_cvt_pk_bf16_f32 v203, v206, v207\n"
"ds_write_b128 v124, v[200:203] offset:18432\n"
"s_waitcnt lgkmcnt(7)\n"
"v_fma_f32 v208, v208, v32, v40\n"
"v_fma_f32 v209, v209, v33, v41\n"
"v_fma_f32 v210, v210, v34, v42\n"
"v_fma_f32 v211, v211, v35, v43\n"
"s_waitcnt lgkmcnt(6)\n"
"v_fma_f32 v212, v212, v36, v44\n"
"v_fma_f32 v213, v213, v37, v45\n"
"v_fma_f32 v214, v214, v38, v46\n"
"v_fma_f32 v215, v215, v39, v47\n"
"ds_read_b128 v[32:35], v17 offset:3072\n"
"ds_read_b128 v[36:39], v17 offset:3088\n"
"ds_read_b128 v[40:43], v17 offset:3104\n"
"ds_read_b128 v[44:47], v17 offset:3120\n"
"v_cvt_pk_bf16_f32 v208, v208, v209\n"
"v_cvt_pk_bf16_f32 v209, v210, v211\n"
"v_cvt_pk_bf16_f32 v210, v212, v213\n"
"v_cvt_pk_bf16_f32 v211, v214, v215\n"
"ds_write_b128 v124, v[208:211] offset:20480\n"
"s_waitcnt lgkmcnt(7)\n"
"v_fma_f32 v216, v216, v48, v56\n"
"v_fma_f32 v217, v217, v49, v57\n"
"v_fma_f32 v218, v218, v50, v58\n"
"v_fma_f32 v219, v219, v51, v59\n"
"s_waitcnt lgkmcnt(6)\n"
"v_fma_f32 v220, v220, v52, v60\n"
"v_fma_f32 v221, v221, v53, v61\n"
"v_fma_f32 v222, v222, v54, v62\n"
"v_fma_f32 v223, v223, v55, v63\n"
"ds_read_b128 v[48:51], v17 offset:3328\n"
"ds_read_b128 v[52:55], v17 offset:3344\n"
"ds_read_b128 v[56:59], v17 offset:3360\n"
"ds_read_b128 v[60:63], v17 offset:3376\n"
"v_cvt_pk_bf16_f32 v216, v216, v217\n"
"v_cvt_pk_bf16_f32 v217, v218, v219\n"
"v_cvt_pk_bf16_f32 v218, v220, v221\n"
"v_cvt_pk_bf16_f32 v219, v222, v223\n"
"ds_write_b128 v124, v[216:219] offset:22528\n"
"s_waitcnt lgkmcnt(7)\n"
"v_fma_f32 v224, v224, v32, v40\n"
"v_fma_f32 v225, v225, v33, v41\n"
"v_fma_f32 v226, v226, v34, v42\n"
"v_fma_f32 v227, v227, v35, v43\n"
"s_waitcnt lgkmcnt(6)\n"
"v_fma_f32 v228, v228, v36, v44\n"
"v_fma_f32 v229, v229, v37, v45\n"
"v_fma_f32 v230, v230, v38, v46\n"
"v_fma_f32 v231, v231, v39, v47\n"
"v_cvt_pk_bf16_f32 v224, v224, v225\n"
"v_cvt_pk_bf16_f32 v225, v226, v227\n"
"v_cvt_pk_bf16_f32 v226, v228, v229\n"
"v_cvt_pk_bf16_f32 v227, v230, v231\n"
"ds_write_b128 v124, v[224:227] offset:24576\n"
"s_waitcnt lgkmcnt(3)\n"
"v_fma_f32 v232, v232, v48, v56\n"
"v_fma_f32 v233, v233, v49, v57\n"
"v_fma_f32 v234, v234, v50, v58\n"
"v_fma_f32 v235, v235, v51, v59\n"
"s_waitcnt lgkmcnt(2)\n"
"v_fma_f32 v236, v236, v52, v60\n"
"v_fma_f32 v237, v237, v53, v61\n"
"v_fma_f32 v238, v238, v54, v62\n"
"v_fma_f32 v239, v239, v55, v63\n"
"v_cvt_pk_bf16_f32 v232, v232, v233\n"
"v_cvt_pk_bf16_f32 v233, v234, v235\n"
"v_cvt_pk_bf16_f32 v234, v236, v237\n"
"v_cvt_pk_bf16_f32 v235, v238, v239\n"
"ds_write_b128 v124, v[232:235] offset:26624\n"
"s_waitcnt lgkmcnt(0)\n"
"s_barrier\n"
"ds_read_b128 a[64:67], v125 offset:0\n"
"ds_read_b64_tr_b16 a[192:193], v5 offset:0\n"
"ds_read_b64_tr_b16 a[194:195], v5 offset:4096\n"
"ds_read_b64_tr_b16 a[196:197], v6 offset:0\n"
"ds_read_b64_tr_b16 a[198:199], v6 offset:4096\n"
"ds_read_b64_tr_b16 a[200:201], v7 offset:0\n"
"ds_read_b64_tr_b16 a[202:203], v7 offset:4096\n"
"ds_read_b64_tr_b16 a[204:205], v8 offset:0\n"
"ds_read_b64_tr_b16 a[206:207], v8 offset:4096\n"
"ds_read_b128 a[68:71], v125 offset:1024\n"
"ds_read_b128 a[72:75], v125 offset:2048\n"
"ds_read_b128 a[76:79], v125 offset:3072\n"
"ds_read_b128 a[80:83], v125 offset:4096\n"
"ds_read_b128 a[84:87], v125 offset:5120\n"
"s_waitcnt lgkmcnt(9)\n"
"v_mfma_f32_32x32x16_bf16 v[128:143], a[192:195], a[64:67], 0\n"
"ds_read_b64_tr_b16 a[208:209], v5 offset:256\n"
"ds_read_b64_tr_b16 a[210:211], v5 offset:4352\n"
"ds_read_b128 a[88:91], v125 offset:6144\n"
"ds_read_b128 a[92:95], v125 offset:7168\n"
"v_mfma_f32_32x32x16_bf16 v[144:159], a[196:199], a[64:67], 0\n"
"ds_read_b64_tr_b16 a[212:213], v6 offset:256\n"
"ds_read_b64_tr_b16 a[214:215], v6 offset:4352\n"
"s_waitcnt lgkmcnt(13)\n"
"ds_read_b128 a[96:99], v125 offset:8192\n"
"ds_read_b128 a[100:103], v125 offset:9216\n"
"s_waitcnt lgkmcnt(13)\n"
"v_mfma_f32_32x32x16_bf16 v[160:175], a[200:203], a[64:67], 0\n"
"ds_read_b64_tr_b16 a[216:217], v7 offset:256\n"
"ds_read_b64_tr_b16 a[218:219], v7 offset:4352\n"
"s_waitcnt lgkmcnt(13)\n"
"ds_read_b128 a[104:107], v125 offset:10240\n"
"ds_read_b128 a[108:111], v125 offset:11264\n"
"v_mfma_f32_32x32x16_bf16 v[176:191], a[204:207], a[64:67], 0\n"
"s_waitcnt lgkmcnt(13)\n"
"ds_read_b64_tr_b16 a[220:221], v8 offset:256\n"
"ds_read_b64_tr_b16 a[222:223], v8 offset:4352\n"
"s_waitcnt lgkmcnt(13)\n"
"ds_read_b128 a[112:115], v125 offset:12288\n"
"ds_read_b128 a[116:119], v125 offset:13312\n"
"s_waitcnt lgkmcnt(10)\n"
"v_mfma_f32_32x32x16_bf16 v[192:207], a[208:211], a[64:67], 0\n"
"s_barrier\n"
"ds_read_b64_tr_b16 a[192:193], v5 offset:16384\n"
"ds_read_b64_tr_b16 a[194:195], v5 offset:20480\n"
"ds_read_b128 a[120:123], v125 offset:14336\n"
"ds_read_b128 a[124:127], v125 offset:15360\n"
"v_mfma_f32_32x32x16_bf16 v[208:223], a[212:215], a[64:67], 0\n"
"ds_read_b64_tr_b16 a[196:197], v6 offset:16384\n"
"s_waitcnt lgkmcnt(13)\n"
"ds_read_b64_tr_b16 a[198:199], v6 offset:20480\n"
"ds_read_b128 a[128:131], v125 offset:16384\n"
"s_waitcnt lgkmcnt(13)\n"
"ds_read_b128 a[132:135], v125 offset:17408\n"
"s_waitcnt lgkmcnt(10)\n"
"v_mfma_f32_32x32x16_bf16 v[224:239], a[216:219], a[64:67], 0\n"
"ds_read_b64_tr_b16 a[200:201], v7 offset:16384\n"
"ds_read_b64_tr_b16 a[202:203], v7 offset:20480\n"
"ds_read_b128 a[136:139], v125 offset:18432\n"
"ds_read_b128 a[140:143], v125 offset:19456\n"
"v_mfma_f32_32x32x16_bf16 v[240:255], a[220:223], a[64:67], 0\n"
"ds_read_b64_tr_b16 a[204:205], v8 offset:16384\n"
"s_waitcnt lgkmcnt(13)\n"
"ds_read_b64_tr_b16 a[206:207], v8 offset:20480\n"
"ds_read_b128 a[144:147], v125 offset:20480\n"
"s_waitcnt lgkmcnt(13)\n"
"ds_read_b128 a[148:151], v125 offset:21504\n"
"s_waitcnt lgkmcnt(10)\n"
"v_mfma_f32_32x32x16_bf16 v[128:143], a[192:195], a[68:71], v[128:143]\n"
"ds_read_b64_tr_b16 a[208:209], v5 offset:16640\n"
"ds_read_b64_tr_b16 a[210:211], v5 offset:20736\n"
"ds_read_b128 a[152:155], v125 offset:22528\n"
"ds_read_b128 a[156:159], v125 offset:23552\n"
"v_mfma_f32_32x32x16_bf16 v[144:159], a[196:199], a[68:71], v[144:159]\n"
"ds_read_b64_tr_b16 a[212:213], v6 offset:16640\n"
"s_waitcnt lgkmcnt(13)\n"
"ds_read_b64_tr_b16 a[214:215], v6 offset:20736\n"
"ds_read_b128 a[160:163], v125 offset:24576\n"
"s_waitcnt lgkmcnt(13)\n"
"ds_read_b128 a[164:167], v125 offset:25600\n"
"s_waitcnt lgkmcnt(10)\n"
"v_mfma_f32_32x32x16_bf16 v[160:175], a[200:203], a[68:71], v[160:175]\n"
"ds_read_b64_tr_b16 a[216:217], v7 offset:16640\n"
"ds_read_b64_tr_b16 a[218:219], v7 offset:20736\n"
"ds_read_b128 a[168:171], v125 offset:26624\n"
"ds_read_b128 a[172:175], v125 offset:27648\n"
"v_mfma_f32_32x32x16_bf16 v[176:191], a[204:207], a[68:71], v[176:191]\n"
"ds_read_b64_tr_b16 a[220:221], v8 offset:16640\n"
"s_waitcnt lgkmcnt(13)\n"
"ds_read_b64_tr_b16 a[222:223], v8 offset:20736\n"
"s_waitcnt lgkmcnt(2)\n"
"v_mfma_f32_32x32x16_bf16 v[192:207], a[208:211], a[68:71], v[192:207]\n"
"s_barrier\n"
"ds_read_b64_tr_b16 a[192:193], v5 offset:32768\n"
"ds_read_b64_tr_b16 a[194:195], v5 offset:36864\n"
"v_mfma_f32_32x32x16_bf16 v[208:223], a[212:215], a[68:71], v[208:223]\n"
"s_add_u32 m0, s68, 65536\n"
"s_mov_b32 s69, 524288\n"
"buffer_load_dwordx4 v1, s[40:43], s69 offen offset:0 lds\n"
"ds_read_b64_tr_b16 a[196:197], v6 offset:32768\n"
"ds_read_b64_tr_b16 a[198:199], v6 offset:36864\n"
"s_waitcnt lgkmcnt(4)\n"
"v_mfma_f32_32x32x16_bf16 v[224:239], a[216:219], a[68:71], v[224:239]\n"
"buffer_load_dwordx4 v2, s[40:43], s69 offen offset:1024 lds\n"
"ds_read_b64_tr_b16 a[200:201], v7 offset:32768\n"
"ds_read_b64_tr_b16 a[202:203], v7 offset:36864\n"
"v_mfma_f32_32x32x16_bf16 v[240:255], a[220:223], a[68:71], v[240:255]\n"
"buffer_load_dwordx4 v3, s[40:43], s69 offen offset:2048 lds\n"
"ds_read_b64_tr_b16 a[204:205], v8 offset:32768\n"
"ds_read_b64_tr_b16 a[206:207], v8 offset:36864\n"
"s_waitcnt lgkmcnt(4)\n"
"v_mfma_f32_32x32x16_bf16 v[128:143], a[192:195], a[72:75], v[128:143]\n"
"buffer_load_dwordx4 v4, s[40:43], s69 offen offset:3072 lds\n"
"ds_read_b64_tr_b16 a[208:209], v5 offset:33024\n"
"ds_read_b64_tr_b16 a[210:211], v5 offset:37120\n"
"v_mfma_f32_32x32x16_bf16 v[144:159], a[196:199], a[72:75], v[144:159]\n"
"s_add_u32 m0, s68, 81920\n"
"s_mov_b32 s69, 655360\n"
"buffer_load_dwordx4 v1, s[40:43], s69 offen offset:0 lds\n"
"ds_read_b64_tr_b16 a[212:213], v6 offset:33024\n"
"ds_read_b64_tr_b16 a[214:215], v6 offset:37120\n"
"s_waitcnt lgkmcnt(4)\n"
"v_mfma_f32_32x32x16_bf16 v[160:175], a[200:203], a[72:75], v[160:175]\n"
"buffer_load_dwordx4 v2, s[40:43], s69 offen offset:1024 lds\n"
"ds_read_b64_tr_b16 a[216:217], v7 offset:33024\n"
"ds_read_b64_tr_b16 a[218:219], v7 offset:37120\n"
"v_mfma_f32_32x32x16_bf16 v[176:191], a[204:207], a[72:75], v[176:191]\n"
"buffer_load_dwordx4 v3, s[40:43], s69 offen offset:2048 lds\n"
"ds_read_b64_tr_b16 a[220:221], v8 offset:33024\n"
"ds_read_b64_tr_b16 a[222:223], v8 offset:37120\n"
"s_waitcnt lgkmcnt(4)\n"
"v_mfma_f32_32x32x16_bf16 v[192:207], a[208:211], a[72:75], v[192:207]\n"
"s_barrier\n"
"buffer_load_dwordx4 v4, s[40:43], s69 offen offset:3072 lds\n"
"ds_read_b64_tr_b16 a[192:193], v5 offset:49152\n"
"ds_read_b64_tr_b16 a[194:195], v5 offset:53248\n"
"v_mfma_f32_32x32x16_bf16 v[208:223], a[212:215], a[72:75], v[208:223]\n"
"s_add_u32 m0, s68, 98304\n"
"s_mov_b32 s69, 786432\n"
"buffer_load_dwordx4 v1, s[40:43], s69 offen offset:0 lds\n"
"ds_read_b64_tr_b16 a[196:197], v6 offset:49152\n"
"ds_read_b64_tr_b16 a[198:199], v6 offset:53248\n"
"s_waitcnt lgkmcnt(4)\n"
"v_mfma_f32_32x32x16_bf16 v[224:239], a[216:219], a[72:75], v[224:239]\n"
"buffer_load_dwordx4 v2, s[40:43], s69 offen offset:1024 lds\n"
"ds_read_b64_tr_b16 a[200:201], v7 offset:49152\n"
"ds_read_b64_tr_b16 a[202:203], v7 offset:53248\n"
"v_mfma_f32_32x32x16_bf16 v[240:255], a[220:223], a[72:75], v[240:255]\n"
"buffer_load_dwordx4 v3, s[40:43], s69 offen offset:2048 lds\n"
"ds_read_b64_tr_b16 a[204:205], v8 offset:49152\n"
"ds_read_b64_tr_b16 a[206:207], v8 offset:53248\n"
"s_waitcnt lgkmcnt(4)\n"
"v_mfma_f32_32x32x16_bf16 v[128:143], a[192:195], a[76:79], v[128:143]\n"
"buffer_load_dwordx4 v4, s[40:43], s69 offen offset:3072 lds\n"
"ds_read_b64_tr_b16 a[208:209], v5 offset:49408\n"
"ds_read_b64_tr_b16 a[210:211], v5 offset:53504\n"
"v_mfma_f32_32x32x16_bf16 v[144:159], a[196:199], a[76:79], v[144:159]\n"
"s_add_u32 m0, s68, 114688\n"
"s_mov_b32 s69, 917504\n"
"buffer_load_dwordx4 v1, s[40:43], s69 offen offset:0 lds\n"
"ds_read_b64_tr_b16 a[212:213], v6 offset:49408\n"
"ds_read_b64_tr_b16 a[214:215], v6 offset:53504\n"
"s_waitcnt lgkmcnt(4)\n"
"v_mfma_f32_32x32x16_bf16 v[160:175], a[200:203], a[76:79], v[160:175]\n"
"buffer_load_dwordx4 v2, s[40:43], s69 offen offset:1024 lds\n"
"ds_read_b64_tr_b16 a[216:217], v7 offset:49408\n"
"ds_read_b64_tr_b16 a[218:219], v7 offset:53504\n"
"v_mfma_f32_32x32x16_bf16 v[176:191], a[204:207], a[76:79], v[176:191]\n"
"buffer_load_dwordx4 v3, s[40:43], s69 offen offset:2048 lds\n"
"ds_read_b64_tr_b16 a[220:221], v8 offset:49408\n"
"ds_read_b64_tr_b16 a[222:223], v8 offset:53504\n"
"s_waitcnt lgkmcnt(4)\n"
"v_mfma_f32_32x32x16_bf16 v[192:207], a[208:211], a[76:79], v[192:207]\n"
"s_waitcnt vmcnt(11)\n"
"s_barrier\n"
"buffer_load_dwordx4 v4, s[40:43], s69 offen offset:3072 lds\n"
"v_xor_b32 v5, 0x10000, v5\n"
"ds_read_b64_tr_b16 a[192:193], v5 offset:0\n"
"ds_read_b64_tr_b16 a[194:195], v5 offset:4096\n"
"v_mfma_f32_32x32x16_bf16 v[208:223], a[212:215], a[76:79], v[208:223]\n"
"v_xor_b32 v6, 0x10000, v6\n"
"ds_read_b64_tr_b16 a[196:197], v6 offset:0\n"
"ds_read_b64_tr_b16 a[198:199], v6 offset:4096\n"
"s_waitcnt lgkmcnt(4)\n"
"v_mfma_f32_32x32x16_bf16 v[224:239], a[216:219], a[76:79], v[224:239]\n"
"s_add_u32 m0, s68, 0\n"
"s_mov_b32 s69, 1048576\n"
"buffer_load_dwordx4 v1, s[40:43], s69 offen offset:0 lds\n"
"v_xor_b32 v7, 0x10000, v7\n"
"ds_read_b64_tr_b16 a[200:201], v7 offset:0\n"
"ds_read_b64_tr_b16 a[202:203], v7 offset:4096\n"
"v_mfma_f32_32x32x16_bf16 v[240:255], a[220:223], a[76:79], v[240:255]\n"
"v_xor_b32 v8, 0x10000, v8\n"
"ds_read_b64_tr_b16 a[204:205], v8 offset:0\n"
"ds_read_b64_tr_b16 a[206:207], v8 offset:4096\n"
"s_waitcnt lgkmcnt(4)\n"
"v_mfma_f32_32x32x16_bf16 v[128:143], a[192:195], a[80:83], v[128:143]\n"
"buffer_load_dwordx4 v2, s[40:43], s69 offen offset:1024 lds\n"
"ds_read_b64_tr_b16 a[208:209], v5 offset:256\n"
"ds_read_b64_tr_b16 a[210:211], v5 offset:4352\n"
"v_mfma_f32_32x32x16_bf16 v[144:159], a[196:199], a[80:83], v[144:159]\n"
"ds_read_b64_tr_b16 a[212:213], v6 offset:256\n"
"ds_read_b64_tr_b16 a[214:215], v6 offset:4352\n"
"s_waitcnt lgkmcnt(4)\n"
"v_mfma_f32_32x32x16_bf16 v[160:175], a[200:203], a[80:83], v[160:175]\n"
"buffer_load_dwordx4 v3, s[40:43], s69 offen offset:2048 lds\n"
"ds_read_b64_tr_b16 a[216:217], v7 offset:256\n"
"ds_read_b64_tr_b16 a[218:219], v7 offset:4352\n"
"v_mfma_f32_32x32x16_bf16 v[176:191], a[204:207], a[80:83], v[176:191]\n"
"ds_read_b64_tr_b16 a[220:221], v8 offset:256\n"
"ds_read_b64_tr_b16 a[222:223], v8 offset:4352\n"
"s_waitcnt lgkmcnt(4)\n"
"v_mfma_f32_32x32x16_bf16 v[192:207], a[208:211], a[80:83], v[192:207]\n"
"s_waitcnt vmcnt(11)\n"
"s_barrier\n"
"buffer_load_dwordx4 v4, s[40:43], s69 offen offset:3072 lds\n"
"ds_read_b64_tr_b16 a[192:193], v5 offset:16384\n"
"ds_read_b64_tr_b16 a[194:195], v5 offset:20480\n"
"v_mfma_f32_32x32x16_bf16 v[208:223], a[212:215], a[80:83], v[208:223]\n"
"ds_read_b64_tr_b16 a[196:197], v6 offset:16384\n"
"ds_read_b64_tr_b16 a[198:199], v6 offset:20480\n"
"s_waitcnt lgkmcnt(4)\n"
"v_mfma_f32_32x32x16_bf16 v[224:239], a[216:219], a[80:83], v[224:239]\n"
"s_add_u32 m0, s68, 16384\n"
"s_mov_b32 s69, 1179648\n"
"buffer_load_dwordx4 v1, s[40:43], s69 offen offset:0 lds\n"
"ds_read_b64_tr_b16 a[200:201], v7 offset:16384\n"
"ds_read_b64_tr_b16 a[202:203], v7 offset:20480\n"
"v_mfma_f32_32x32x16_bf16 v[240:255], a[220:223], a[80:83], v[240:255]\n"
"ds_read_b64_tr_b16 a[204:205], v8 offset:16384\n"
"ds_read_b64_tr_b16 a[206:207], v8 offset:20480\n"
"s_waitcnt lgkmcnt(4)\n"
"v_mfma_f32_32x32x16_bf16 v[128:143], a[192:195], a[84:87], v[128:143]\n"
"buffer_load_dwordx4 v2, s[40:43], s69 offen offset:1024 lds\n"
"ds_read_b64_tr_b16 a[208:209], v5 offset:16640\n"
"ds_read_b64_tr_b16 a[210:211], v5 offset:20736\n"
"v_mfma_f32_32x32x16_bf16 v[144:159], a[196:199], a[84:87], v[144:159]\n"
"ds_read_b64_tr_b16 a[212:213], v6 offset:16640\n"
"ds_read_b64_tr_b16 a[214:215], v6 offset:20736\n"
"s_waitcnt lgkmcnt(4)\n"
"v_mfma_f32_32x32x16_bf16 v[160:175], a[200:203], a[84:87], v[160:175]\n"
"buffer_load_dwordx4 v3, s[40:43], s69 offen offset:2048 lds\n"
"ds_read_b64_tr_b16 a[216:217], v7 offset:16640\n"
"ds_read_b64_tr_b16 a[218:219], v7 offset:20736\n"
"v_mfma_f32_32x32x16_bf16 v[176:191], a[204:207], a[84:87], v[176:191]\n"
"ds_read_b64_tr_b16 a[220:221], v8 offset:16640\n"
"ds_read_b64_tr_b16 a[222:223], v8 offset:20736\n"
"s_waitcnt lgkmcnt(4)\n"
"v_mfma_f32_32x32x16_bf16 v[192:207], a[208:211], a[84:87], v[192:207]\n"
"s_waitcnt vmcnt(11)\n"
"s_barrier\n"
"buffer_load_dwordx4 v4, s[40:43], s69 offen offset:3072 lds\n"
"ds_read_b64_tr_b16 a[192:193], v5 offset:32768\n"
"ds_read_b64_tr_b16 a[194:195], v5 offset:36864\n"
"v_mfma_f32_32x32x16_bf16 v[208:223], a[212:215], a[84:87], v[208:223]\n"
"ds_read_b64_tr_b16 a[196:197], v6 offset:32768\n"
"ds_read_b64_tr_b16 a[198:199], v6 offset:36864\n"
"s_waitcnt lgkmcnt(4)\n"
"v_mfma_f32_32x32x16_bf16 v[224:239], a[216:219], a[84:87], v[224:239]\n"
"s_add_u32 m0, s68, 32768\n"
"s_mov_b32 s69, 1310720\n"
"buffer_load_dwordx4 v1, s[40:43], s69 offen offset:0 lds\n"
"ds_read_b64_tr_b16 a[200:201], v7 offset:32768\n"
"ds_read_b64_tr_b16 a[202:203], v7 offset:36864\n"
"v_mfma_f32_32x32x16_bf16 v[240:255], a[220:223], a[84:87], v[240:255]\n"
"ds_read_b64_tr_b16 a[204:205], v8 offset:32768\n"
"ds_read_b64_tr_b16 a[206:207], v8 offset:36864\n"
"s_waitcnt lgkmcnt(4)\n"
"v_mfma_f32_32x32x16_bf16 v[128:143], a[192:195], a[88:91], v[128:143]\n"
"buffer_load_dwordx4 v2, s[40:43], s69 offen offset:1024 lds\n"
"ds_read_b64_tr_b16 a[208:209], v5 offset:33024\n"
"ds_read_b64_tr_b16 a[210:211], v5 offset:37120\n"
"v_mfma_f32_32x32x16_bf16 v[144:159], a[196:199], a[88:91], v[144:159]\n"
"ds_read_b64_tr_b16 a[212:213], v6 offset:33024\n"
"ds_read_b64_tr_b16 a[214:215], v6 offset:37120\n"
"s_waitcnt lgkmcnt(4)\n"
"v_mfma_f32_32x32x16_bf16 v[160:175], a[200:203], a[88:91], v[160:175]\n"
"buffer_load_dwordx4 v3, s[40:43], s69 offen offset:2048 lds\n"
"ds_read_b64_tr_b16 a[216:217], v7 offset:33024\n"
"ds_read_b64_tr_b16 a[218:219], v7 offset:37120\n"
"v_mfma_f32_32x32x16_bf16 v[176:191], a[204:207], a[88:91], v[176:191]\n"
"ds_read_b64_tr_b16 a[220:221], v8 offset:33024\n"
"ds_read_b64_tr_b16 a[222:223], v8 offset:37120\n"
"s_waitcnt lgkmcnt(4)\n"
"v_mfma_f32_32x32x16_bf16 v[192:207], a[208:211], a[88:91], v[192:207]\n"
"s_waitcnt vmcnt(11)\n"
"s_barrier\n"
"buffer_load_dwordx4 v4, s[40:43], s69 offen offset:3072 lds\n"
"ds_read_b64_tr_b16 a[192:193], v5 offset:49152\n"
"ds_read_b64_tr_b16 a[194:195], v5 offset:53248\n"
"v_mfma_f32_32x32x16_bf16 v[208:223], a[212:215], a[88:91], v[208:223]\n"
"ds_read_b64_tr_b16 a[196:197], v6 offset:49152\n"
"ds_read_b64_tr_b16 a[198:199], v6 offset:53248\n"
"s_waitcnt lgkmcnt(4)\n"
"v_mfma_f32_32x32x16_bf16 v[224:239], a[216:219], a[88:91], v[224:239]\n"
"s_add_u32 m0, s68, 49152\n"
"s_mov_b32 s69, 1441792\n"
"buffer_load_dwordx4 v1, s[40:43], s69 offen offset:0 lds\n"
"ds_read_b64_tr_b16 a[200:201], v7 offset:49152\n"
"ds_read_b64_tr_b16 a[202:203], v7 offset:53248\n"
"v_mfma_f32_32x32x16_bf16 v[240:255], a[220:223], a[88:91], v[240:255]\n"
"ds_read_b64_tr_b16 a[204:205], v8 offset:49152\n"
"ds_read_b64_tr_b16 a[206:207], v8 offset:53248\n"
"s_waitcnt lgkmcnt(4)\n"
"v_mfma_f32_32x32x16_bf16 v[128:143], a[192:195], a[92:95], v[128:143]\n"
"buffer_load_dwordx4 v2, s[40:43], s69 offen offset:1024 lds\n"
"ds_read_b64_tr_b16 a[208:209], v5 offset:49408\n"
"ds_read_b64_tr_b16 a[210:211], v5 offset:53504\n"
"v_mfma_f32_32x32x16_bf16 v[144:159], a[196:199], a[92:95], v[144:159]\n"
"ds_read_b64_tr_b16 a[212:213], v6 offset:49408\n"
"ds_read_b64_tr_b16 a[214:215], v6 offset:53504\n"
"s_waitcnt lgkmcnt(4)\n"
"v_mfma_f32_32x32x16_bf16 v[160:175], a[200:203], a[92:95], v[160:175]\n"
"buffer_load_dwordx4 v3, s[40:43], s69 offen offset:2048 lds\n"
"ds_read_b64_tr_b16 a[216:217], v7 offset:49408\n"
"ds_read_b64_tr_b16 a[218:219], v7 offset:53504\n"
"v_mfma_f32_32x32x16_bf16 v[176:191], a[204:207], a[92:95], v[176:191]\n"
"ds_read_b64_tr_b16 a[220:221], v8 offset:49408\n"
"ds_read_b64_tr_b16 a[222:223], v8 offset:53504\n"
"s_waitcnt lgkmcnt(4)\n"
"v_mfma_f32_32x32x16_bf16 v[192:207], a[208:211], a[92:95], v[192:207]\n"
"s_waitcnt vmcnt(11)\n"
"s_barrier\n"
"buffer_load_dwordx4 v4, s[40:43], s69 offen offset:3072 lds\n"
"v_xor_b32 v5, 0x10000, v5\n"
"ds_read_b64_tr_b16 a[192:193], v5 offset:0\n"
"ds_read_b64_tr_b16 a[194:195], v5 offset:4096\n"
"v_mfma_f32_32x32x16_bf16 v[208:223], a[212:215], a[92:95], v[208:223]\n"
"v_xor_b32 v6, 0x10000, v6\n"
"ds_read_b64_tr_b16 a[196:197], v6 offset:0\n"
"ds_read_b64_tr_b16 a[198:199], v6 offset:4096\n"
"s_waitcnt lgkmcnt(4)\n"
"v_mfma_f32_32x32x16_bf16 v[224:239], a[216:219], a[92:95], v[224:239]\n"
"s_add_u32 m0, s68, 65536\n"
"s_mov_b32 s69, 1572864\n"
"buffer_load_dwordx4 v1, s[40:43], s69 offen offset:0 lds\n"
"v_xor_b32 v7, 0x10000, v7\n"
"ds_read_b64_tr_b16 a[200:201], v7 offset:0\n"
"ds_read_b64_tr_b16 a[202:203], v7 offset:4096\n"
"v_mfma_f32_32x32x16_bf16 v[240:255], a[220:223], a[92:95], v[240:255]\n"
"v_xor_b32 v8, 0x10000, v8\n"
"ds_read_b64_tr_b16 a[204:205], v8 offset:0\n"
"ds_read_b64_tr_b16 a[206:207], v8 offset:4096\n"
"s_waitcnt lgkmcnt(4)\n"
"v_mfma_f32_32x32x16_bf16 v[128:143], a[192:195], a[96:99], v[128:143]\n"
"buffer_load_dwordx4 v2, s[40:43], s69 offen offset:1024 lds\n"
"ds_read_b64_tr_b16 a[208:209], v5 offset:256\n"
"ds_read_b64_tr_b16 a[210:211], v5 offset:4352\n"
"v_mfma_f32_32x32x16_bf16 v[144:159], a[196:199], a[96:99], v[144:159]\n"
"ds_read_b64_tr_b16 a[212:213], v6 offset:256\n"
"ds_read_b64_tr_b16 a[214:215], v6 offset:4352\n"
"s_waitcnt lgkmcnt(4)\n"
"v_mfma_f32_32x32x16_bf16 v[160:175], a[200:203], a[96:99], v[160:175]\n"
"buffer_load_dwordx4 v3, s[40:43], s69 offen offset:2048 lds\n"
"ds_read_b64_tr_b16 a[216:217], v7 offset:256\n"
"ds_read_b64_tr_b16 a[218:219], v7 offset:4352\n"
"v_mfma_f32_32x32x16_bf16 v[176:191], a[204:207], a[96:99], v[176:191]\n"
"ds_read_b64_tr_b16 a[220:221], v8 offset:256\n"
"ds_read_b64_tr_b16 a[222:223], v8 offset:4352\n"
"s_waitcnt lgkmcnt(4)\n"
"v_mfma_f32_32x32x16_bf16 v[192:207], a[208:211], a[96:99], v[192:207]\n"
"s_waitcnt vmcnt(11)\n"
"s_barrier\n"
"buffer_load_dwordx4 v4, s[40:43], s69 offen offset:3072 lds\n"
"ds_read_b64_tr_b16 a[192:193], v5 offset:16384\n"
"ds_read_b64_tr_b16 a[194:195], v5 offset:20480\n"
"v_mfma_f32_32x32x16_bf16 v[208:223], a[212:215], a[96:99], v[208:223]\n"
"ds_read_b64_tr_b16 a[196:197], v6 offset:16384\n"
"ds_read_b64_tr_b16 a[198:199], v6 offset:20480\n"
"s_waitcnt lgkmcnt(4)\n"
"v_mfma_f32_32x32x16_bf16 v[224:239], a[216:219], a[96:99], v[224:239]\n"
"s_add_u32 m0, s68, 81920\n"
"s_mov_b32 s69, 1703936\n"
"buffer_load_dwordx4 v1, s[40:43], s69 offen offset:0 lds\n"
"ds_read_b64_tr_b16 a[200:201], v7 offset:16384\n"
"ds_read_b64_tr_b16 a[202:203], v7 offset:20480\n"
"v_mfma_f32_32x32x16_bf16 v[240:255], a[220:223], a[96:99], v[240:255]\n"
"ds_read_b64_tr_b16 a[204:205], v8 offset:16384\n"
"ds_read_b64_tr_b16 a[206:207], v8 offset:20480\n"
"s_waitcnt lgkmcnt(4)\n"
"v_mfma_f32_32x32x16_bf16 v[128:143], a[192:195], a[100:103], v[128:143]\n"
"buffer_load_dwordx4 v2, s[40:43], s69 offen offset:1024 lds\n"
"ds_read_b64_tr_b16 a[208:209], v5 offset:16640\n"
"ds_read_b64_tr_b16 a[210:211], v5 offset:20736\n"
"v_mfma_f32_32x32x16_bf16 v[144:159], a[196:199], a[100:103], v[144:159]\n"
"ds_read_b64_tr_b16 a[212:213], v6 offset:16640\n"
"ds_read_b64_tr_b16 a[214:215], v6 offset:20736\n"
"s_waitcnt lgkmcnt(4)\n"
"v_mfma_f32_32x32x16_bf16 v[160:175], a[200:203], a[100:103], v[160:175]\n"
"buffer_load_dwordx4 v3, s[40:43], s69 offen offset:2048 lds\n"
"ds_read_b64_tr_b16 a[216:217], v7 offset:16640\n"
"ds_read_b64_tr_b16 a[218:219], v7 offset:20736\n"
"v_mfma_f32_32x32x16_bf16 v[176:191], a[204:207], a[100:103], v[176:191]\n"
"ds_read_b64_tr_b16 a[220:221], v8 offset:16640\n"
"ds_read_b64_tr_b16 a[222:223], v8 offset:20736\n"
"s_waitcnt lgkmcnt(4)\n"
"v_mfma_f32_32x32x16_bf16 v[192:207], a[208:211], a[100:103], v[192:207]\n"
"s_waitcnt vmcnt(11)\n"
"s_barrier\n"
"buffer_load_dwordx4 v4, s[40:43], s69 offen offset:3072 lds\n"
"ds_read_b64_tr_b16 a[192:193], v5 offset:32768\n"
"ds_read_b64_tr_b16 a[194:195], v5 offset:36864\n"
"v_mfma_f32_32x32x16_bf16 v[208:223], a[212:215], a[100:103], v[208:223]\n"
"ds_read_b64_tr_b16 a[196:197], v6 offset:32768\n"
"ds_read_b64_tr_b16 a[198:199], v6 offset:36864\n"
"s_waitcnt lgkmcnt(4)\n"
"v_mfma_f32_32x32x16_bf16 v[224:239], a[216:219], a[100:103], v[224:239]\n"
"s_add_u32 m0, s68, 98304\n"
"s_mov_b32 s69, 1835008\n"
"buffer_load_dwordx4 v1, s[40:43], s69 offen offset:0 lds\n"
"ds_read_b64_tr_b16 a[200:201], v7 offset:32768\n"
"ds_read_b64_tr_b16 a[202:203], v7 offset:36864\n"
"v_mfma_f32_32x32x16_bf16 v[240:255], a[220:223], a[100:103], v[240:255]\n"
"ds_read_b64_tr_b16 a[204:205], v8 offset:32768\n"
"ds_read_b64_tr_b16 a[206:207], v8 offset:36864\n"
"s_waitcnt lgkmcnt(4)\n"
"v_mfma_f32_32x32x16_bf16 v[128:143], a[192:195], a[104:107], v[128:143]\n"
"buffer_load_dwordx4 v2, s[40:43], s69 offen offset:1024 lds\n"
"ds_read_b64_tr_b16 a[208:209], v5 offset:33024\n"
"ds_read_b64_tr_b16 a[210:211], v5 offset:37120\n"
"v_mfma_f32_32x32x16_bf16 v[144:159], a[196:199], a[104:107], v[144:159]\n"
"ds_read_b64_tr_b16 a[212:213], v6 offset:33024\n"
"ds_read_b64_tr_b16 a[214:215], v6 offset:37120\n"
"s_waitcnt lgkmcnt(4)\n"
"v_mfma_f32_32x32x16_bf16 v[160:175], a[200:203], a[104:107], v[160:175]\n"
"buffer_load_dwordx4 v3, s[40:43], s69 offen offset:2048 lds\n"
"ds_read_b64_tr_b16 a[216:217], v7 offset:33024\n"
"ds_read_b64_tr_b16 a[218:219], v7 offset:37120\n"
"v_mfma_f32_32x32x16_bf16 v[176:191], a[204:207], a[104:107], v[176:191]\n"
"ds_read_b64_tr_b16 a[220:221], v8 offset:33024\n"
"ds_read_b64_tr_b16 a[222:223], v8 offset:37120\n"
"s_waitcnt lgkmcnt(4)\n"
"v_mfma_f32_32x32x16_bf16 v[192:207], a[208:211], a[104:107], v[192:207]\n"
"s_waitcnt vmcnt(11)\n"
"s_barrier\n"
"buffer_load_dwordx4 v4, s[40:43], s69 offen offset:3072 lds\n"
"ds_read_b64_tr_b16 a[192:193], v5 offset:49152\n"
"ds_read_b64_tr_b16 a[194:195], v5 offset:53248\n"
"v_mfma_f32_32x32x16_bf16 v[208:223], a[212:215], a[104:107], v[208:223]\n"
"ds_read_b64_tr_b16 a[196:197], v6 offset:49152\n"
"ds_read_b64_tr_b16 a[198:199], v6 offset:53248\n"
"s_waitcnt lgkmcnt(4)\n"
"v_mfma_f32_32x32x16_bf16 v[224:239], a[216:219], a[104:107], v[224:239]\n"
"s_add_u32 m0, s68, 114688\n"
"s_mov_b32 s69, 1966080\n"
"buffer_load_dwordx4 v1, s[40:43], s69 offen offset:0 lds\n"
"ds_read_b64_tr_b16 a[200:201], v7 offset:49152\n"
"ds_read_b64_tr_b16 a[202:203], v7 offset:53248\n"
"v_mfma_f32_32x32x16_bf16 v[240:255], a[220:223], a[104:107], v[240:255]\n"
"ds_read_b64_tr_b16 a[204:205], v8 offset:49152\n"
"ds_read_b64_tr_b16 a[206:207], v8 offset:53248\n"
"s_waitcnt lgkmcnt(4)\n"
"v_mfma_f32_32x32x16_bf16 v[128:143], a[192:195], a[108:111], v[128:143]\n"
"buffer_load_dwordx4 v2, s[40:43], s69 offen offset:1024 lds\n"
"ds_read_b64_tr_b16 a[208:209], v5 offset:49408\n"
"ds_read_b64_tr_b16 a[210:211], v5 offset:53504\n"
"v_mfma_f32_32x32x16_bf16 v[144:159], a[196:199], a[108:111], v[144:159]\n"
"ds_read_b64_tr_b16 a[212:213], v6 offset:49408\n"
"ds_read_b64_tr_b16 a[214:215], v6 offset:53504\n"
"s_waitcnt lgkmcnt(4)\n"
"v_mfma_f32_32x32x16_bf16 v[160:175], a[200:203], a[108:111], v[160:175]\n"
"buffer_load_dwordx4 v3, s[40:43], s69 offen offset:2048 lds\n"
"ds_read_b64_tr_b16 a[216:217], v7 offset:49408\n"
"ds_read_b64_tr_b16 a[218:219], v7 offset:53504\n"
"v_mfma_f32_32x32x16_bf16 v[176:191], a[204:207], a[108:111], v[176:191]\n"
"ds_read_b64_tr_b16 a[220:221], v8 offset:49408\n"
"ds_read_b64_tr_b16 a[222:223], v8 offset:53504\n"
"s_waitcnt lgkmcnt(4)\n"
"v_mfma_f32_32x32x16_bf16 v[192:207], a[208:211], a[108:111], v[192:207]\n"
"s_waitcnt vmcnt(11)\n"
"s_barrier\n"
"buffer_load_dwordx4 v4, s[40:43], s69 offen offset:3072 lds\n"
"v_xor_b32 v5, 0x10000, v5\n"
"ds_read_b64_tr_b16 a[192:193], v5 offset:0\n"
"ds_read_b64_tr_b16 a[194:195], v5 offset:4096\n"
"v_mfma_f32_32x32x16_bf16 v[208:223], a[212:215], a[108:111], v[208:223]\n"
"v_xor_b32 v6, 0x10000, v6\n"
"ds_read_b64_tr_b16 a[196:197], v6 offset:0\n"
"ds_read_b64_tr_b16 a[198:199], v6 offset:4096\n"
"s_waitcnt lgkmcnt(4)\n"
"v_mfma_f32_32x32x16_bf16 v[224:239], a[216:219], a[108:111], v[224:239]\n"
"s_add_u32 m0, s68, 0\n"
"s_mov_b32 s69, 2097152\n"
"buffer_load_dwordx4 v1, s[40:43], s69 offen offset:0 lds\n"
"v_xor_b32 v7, 0x10000, v7\n"
"ds_read_b64_tr_b16 a[200:201], v7 offset:0\n"
"ds_read_b64_tr_b16 a[202:203], v7 offset:4096\n"
"v_mfma_f32_32x32x16_bf16 v[240:255], a[220:223], a[108:111], v[240:255]\n"
"v_xor_b32 v8, 0x10000, v8\n"
"ds_read_b64_tr_b16 a[204:205], v8 offset:0\n"
"ds_read_b64_tr_b16 a[206:207], v8 offset:4096\n"
"s_waitcnt lgkmcnt(4)\n"
"v_mfma_f32_32x32x16_bf16 v[128:143], a[192:195], a[112:115], v[128:143]\n"
"buffer_load_dwordx4 v2, s[40:43], s69 offen offset:1024 lds\n"
"ds_read_b64_tr_b16 a[208:209], v5 offset:256\n"
"ds_read_b64_tr_b16 a[210:211], v5 offset:4352\n"
"v_mfma_f32_32x32x16_bf16 v[144:159], a[196:199], a[112:115], v[144:159]\n"
"ds_read_b64_tr_b16 a[212:213], v6 offset:256\n"
"ds_read_b64_tr_b16 a[214:215], v6 offset:4352\n"
"s_waitcnt lgkmcnt(4)\n"
"v_mfma_f32_32x32x16_bf16 v[160:175], a[200:203], a[112:115], v[160:175]\n"
"buffer_load_dwordx4 v3, s[40:43], s69 offen offset:2048 lds\n"
"ds_read_b64_tr_b16 a[216:217], v7 offset:256\n"
"ds_read_b64_tr_b16 a[218:219], v7 offset:4352\n"
"v_mfma_f32_32x32x16_bf16 v[176:191], a[204:207], a[112:115], v[176:191]\n"
"ds_read_b64_tr_b16 a[220:221], v8 offset:256\n"
"ds_read_b64_tr_b16 a[222:223], v8 offset:4352\n"
"s_waitcnt lgkmcnt(4)\n"
"v_mfma_f32_32x32x16_bf16 v[192:207], a[208:211], a[112:115], v[192:207]\n"
"s_waitcnt vmcnt(11)\n"
"s_barrier\n"
"buffer_load_dwordx4 v4, s[40:43], s69 offen offset:3072 lds\n"
"ds_read_b64_tr_b16 a[192:193], v5 offset:16384\n"
"ds_read_b64_tr_b16 a[194:195], v5 offset:20480\n"
"v_mfma_f32_32x32x16_bf16 v[208:223], a[212:215], a[112:115], v[208:223]\n"
"ds_read_b64_tr_b16 a[196:197], v6 offset:16384\n"
"ds_read_b64_tr_b16 a[198:199], v6 offset:20480\n"
"s_waitcnt lgkmcnt(4)\n"
"v_mfma_f32_32x32x16_bf16 v[224:239], a[216:219], a[112:115], v[224:239]\n"
"s_add_u32 m0, s68, 16384\n"
"s_mov_b32 s69, 2228224\n"
"buffer_load_dwordx4 v1, s[40:43], s69 offen offset:0 lds\n"
"ds_read_b64_tr_b16 a[200:201], v7 offset:16384\n"
"ds_read_b64_tr_b16 a[202:203], v7 offset:20480\n"
"v_mfma_f32_32x32x16_bf16 v[240:255], a[220:223], a[112:115], v[240:255]\n"
"ds_read_b64_tr_b16 a[204:205], v8 offset:16384\n"
"ds_read_b64_tr_b16 a[206:207], v8 offset:20480\n"
"s_waitcnt lgkmcnt(4)\n"
"v_mfma_f32_32x32x16_bf16 v[128:143], a[192:195], a[116:119], v[128:143]\n"
"buffer_load_dwordx4 v2, s[40:43], s69 offen offset:1024 lds\n"
"ds_read_b64_tr_b16 a[208:209], v5 offset:16640\n"
"ds_read_b64_tr_b16 a[210:211], v5 offset:20736\n"
"v_mfma_f32_32x32x16_bf16 v[144:159], a[196:199], a[116:119], v[144:159]\n"
"ds_read_b64_tr_b16 a[212:213], v6 offset:16640\n"
"ds_read_b64_tr_b16 a[214:215], v6 offset:20736\n"
"s_waitcnt lgkmcnt(4)\n"
"v_mfma_f32_32x32x16_bf16 v[160:175], a[200:203], a[116:119], v[160:175]\n"
"buffer_load_dwordx4 v3, s[40:43], s69 offen offset:2048 lds\n"
"ds_read_b64_tr_b16 a[216:217], v7 offset:16640\n"
"ds_read_b64_tr_b16 a[218:219], v7 offset:20736\n"
"v_mfma_f32_32x32x16_bf16 v[176:191], a[204:207], a[116:119], v[176:191]\n"
"ds_read_b64_tr_b16 a[220:221], v8 offset:16640\n"
"ds_read_b64_tr_b16 a[222:223], v8 offset:20736\n"
"s_waitcnt lgkmcnt(4)\n"
"v_mfma_f32_32x32x16_bf16 v[192:207], a[208:211], a[116:119], v[192:207]\n"
"s_waitcnt vmcnt(11)\n"
"s_barrier\n"
"buffer_load_dwordx4 v4, s[40:43], s69 offen offset:3072 lds\n"
"ds_read_b64_tr_b16 a[192:193], v5 offset:32768\n"
"ds_read_b64_tr_b16 a[194:195], v5 offset:36864\n"
"v_mfma_f32_32x32x16_bf16 v[208:223], a[212:215], a[116:119], v[208:223]\n"
"ds_read_b64_tr_b16 a[196:197], v6 offset:32768\n"
"ds_read_b64_tr_b16 a[198:199], v6 offset:36864\n"
"s_waitcnt lgkmcnt(4)\n"
"v_mfma_f32_32x32x16_bf16 v[224:239], a[216:219], a[116:119], v[224:239]\n"
"s_add_u32 m0, s68, 32768\n"
"s_mov_b32 s69, 2359296\n"
"buffer_load_dwordx4 v1, s[40:43], s69 offen offset:0 lds\n"
"ds_read_b64_tr_b16 a[200:201], v7 offset:32768\n"
"ds_read_b64_tr_b16 a[202:203], v7 offset:36864\n"
"v_mfma_f32_32x32x16_bf16 v[240:255], a[220:223], a[116:119], v[240:255]\n"
"ds_read_b64_tr_b16 a[204:205], v8 offset:32768\n"
"ds_read_b64_tr_b16 a[206:207], v8 offset:36864\n"
"s_waitcnt lgkmcnt(4)\n"
"v_mfma_f32_32x32x16_bf16 v[128:143], a[192:195], a[120:123], v[128:143]\n"
"buffer_load_dwordx4 v2, s[40:43], s69 offen offset:1024 lds\n"
"ds_read_b64_tr_b16 a[208:209], v5 offset:33024\n"
"ds_read_b64_tr_b16 a[210:211], v5 offset:37120\n"
"v_mfma_f32_32x32x16_bf16 v[144:159], a[196:199], a[120:123], v[144:159]\n"
"ds_read_b64_tr_b16 a[212:213], v6 offset:33024\n"
"ds_read_b64_tr_b16 a[214:215], v6 offset:37120\n"
"s_waitcnt lgkmcnt(4)\n"
"v_mfma_f32_32x32x16_bf16 v[160:175], a[200:203], a[120:123], v[160:175]\n"
"buffer_load_dwordx4 v3, s[40:43], s69 offen offset:2048 lds\n"
"ds_read_b64_tr_b16 a[216:217], v7 offset:33024\n"
"ds_read_b64_tr_b16 a[218:219], v7 offset:37120\n"
"v_mfma_f32_32x32x16_bf16 v[176:191], a[204:207], a[120:123], v[176:191]\n"
"ds_read_b64_tr_b16 a[220:221], v8 offset:33024\n"
"ds_read_b64_tr_b16 a[222:223], v8 offset:37120\n"
"s_waitcnt lgkmcnt(4)\n"
"v_mfma_f32_32x32x16_bf16 v[192:207], a[208:211], a[120:123], v[192:207]\n"
"s_waitcnt vmcnt(11)\n"
"s_barrier\n"
"buffer_load_dwordx4 v4, s[40:43], s69 offen offset:3072 lds\n"
"ds_read_b64_tr_b16 a[192:193], v5 offset:49152\n"
"ds_read_b64_tr_b16 a[194:195], v5 offset:53248\n"
"v_mfma_f32_32x32x16_bf16 v[208:223], a[212:215], a[120:123], v[208:223]\n"
"ds_read_b64_tr_b16 a[196:197], v6 offset:49152\n"
"ds_read_b64_tr_b16 a[198:199], v6 offset:53248\n"
"s_waitcnt lgkmcnt(4)\n"
"v_mfma_f32_32x32x16_bf16 v[224:239], a[216:219], a[120:123], v[224:239]\n"
"s_add_u32 m0, s68, 49152\n"
"s_mov_b32 s69, 2490368\n"
"buffer_load_dwordx4 v1, s[40:43], s69 offen offset:0 lds\n"
"ds_read_b64_tr_b16 a[200:201], v7 offset:49152\n"
"ds_read_b64_tr_b16 a[202:203], v7 offset:53248\n"
"v_mfma_f32_32x32x16_bf16 v[240:255], a[220:223], a[120:123], v[240:255]\n"
"ds_read_b64_tr_b16 a[204:205], v8 offset:49152\n"
"ds_read_b64_tr_b16 a[206:207], v8 offset:53248\n"
"s_waitcnt lgkmcnt(4)\n"
"v_mfma_f32_32x32x16_bf16 v[128:143], a[192:195], a[124:127], v[128:143]\n"
"buffer_load_dwordx4 v2, s[40:43], s69 offen offset:1024 lds\n"
"ds_read_b64_tr_b16 a[208:209], v5 offset:49408\n"
"ds_read_b64_tr_b16 a[210:211], v5 offset:53504\n"
"v_mfma_f32_32x32x16_bf16 v[144:159], a[196:199], a[124:127], v[144:159]\n"
"ds_read_b64_tr_b16 a[212:213], v6 offset:49408\n"
"ds_read_b64_tr_b16 a[214:215], v6 offset:53504\n"
"s_waitcnt lgkmcnt(4)\n"
"v_mfma_f32_32x32x16_bf16 v[160:175], a[200:203], a[124:127], v[160:175]\n"
"buffer_load_dwordx4 v3, s[40:43], s69 offen offset:2048 lds\n"
"ds_read_b64_tr_b16 a[216:217], v7 offset:49408\n"
"ds_read_b64_tr_b16 a[218:219], v7 offset:53504\n"
"v_mfma_f32_32x32x16_bf16 v[176:191], a[204:207], a[124:127], v[176:191]\n"
"ds_read_b64_tr_b16 a[220:221], v8 offset:49408\n"
"ds_read_b64_tr_b16 a[222:223], v8 offset:53504\n"
"s_waitcnt lgkmcnt(4)\n"
"v_mfma_f32_32x32x16_bf16 v[192:207], a[208:211], a[124:127], v[192:207]\n"
"s_waitcnt vmcnt(11)\n"
"s_barrier\n"
"buffer_load_dwordx4 v4, s[40:43], s69 offen offset:3072 lds\n"
"v_xor_b32 v5, 0x10000, v5\n"
"ds_read_b64_tr_b16 a[192:193], v5 offset:0\n"
"ds_read_b64_tr_b16 a[194:195], v5 offset:4096\n"
"v_mfma_f32_32x32x16_bf16 v[208:223], a[212:215], a[124:127], v[208:223]\n"
"v_xor_b32 v6, 0x10000, v6\n"
"ds_read_b64_tr_b16 a[196:197], v6 offset:0\n"
"ds_read_b64_tr_b16 a[198:199], v6 offset:4096\n"
"s_waitcnt lgkmcnt(4)\n"
"v_mfma_f32_32x32x16_bf16 v[224:239], a[216:219], a[124:127], v[224:239]\n"
"s_add_u32 m0, s68, 65536\n"
"s_mov_b32 s69, 2621440\n"
"buffer_load_dwordx4 v1, s[40:43], s69 offen offset:0 lds\n"
"v_xor_b32 v7, 0x10000, v7\n"
"ds_read_b64_tr_b16 a[200:201], v7 offset:0\n"
"ds_read_b64_tr_b16 a[202:203], v7 offset:4096\n"
"v_mfma_f32_32x32x16_bf16 v[240:255], a[220:223], a[124:127], v[240:255]\n"
"v_xor_b32 v8, 0x10000, v8\n"
"ds_read_b64_tr_b16 a[204:205], v8 offset:0\n"
"ds_read_b64_tr_b16 a[206:207], v8 offset:4096\n"
"s_waitcnt lgkmcnt(4)\n"
"v_mfma_f32_32x32x16_bf16 v[128:143], a[192:195], a[128:131], v[128:143]\n"
"buffer_load_dwordx4 v2, s[40:43], s69 offen offset:1024 lds\n"
"ds_read_b64_tr_b16 a[208:209], v5 offset:256\n"
"ds_read_b64_tr_b16 a[210:211], v5 offset:4352\n"
"v_mfma_f32_32x32x16_bf16 v[144:159], a[196:199], a[128:131], v[144:159]\n"
"ds_read_b64_tr_b16 a[212:213], v6 offset:256\n"
"ds_read_b64_tr_b16 a[214:215], v6 offset:4352\n"
"s_waitcnt lgkmcnt(4)\n"
"v_mfma_f32_32x32x16_bf16 v[160:175], a[200:203], a[128:131], v[160:175]\n"
"buffer_load_dwordx4 v3, s[40:43], s69 offen offset:2048 lds\n"
"ds_read_b64_tr_b16 a[216:217], v7 offset:256\n"
"ds_read_b64_tr_b16 a[218:219], v7 offset:4352\n"
"v_mfma_f32_32x32x16_bf16 v[176:191], a[204:207], a[128:131], v[176:191]\n"
"ds_read_b64_tr_b16 a[220:221], v8 offset:256\n"
"ds_read_b64_tr_b16 a[222:223], v8 offset:4352\n"
"s_waitcnt lgkmcnt(4)\n"
"v_mfma_f32_32x32x16_bf16 v[192:207], a[208:211], a[128:131], v[192:207]\n"
"s_waitcnt vmcnt(11)\n"
"s_barrier\n"
"buffer_load_dwordx4 v4, s[40:43], s69 offen offset:3072 lds\n"
"ds_read_b64_tr_b16 a[192:193], v5 offset:16384\n"
"ds_read_b64_tr_b16 a[194:195], v5 offset:20480\n"
"v_mfma_f32_32x32x16_bf16 v[208:223], a[212:215], a[128:131], v[208:223]\n"
"ds_read_b64_tr_b16 a[196:197], v6 offset:16384\n"
"ds_read_b64_tr_b16 a[198:199], v6 offset:20480\n"
"s_waitcnt lgkmcnt(4)\n"
"v_mfma_f32_32x32x16_bf16 v[224:239], a[216:219], a[128:131], v[224:239]\n"
"s_add_u32 m0, s68, 81920\n"
"s_mov_b32 s69, 2752512\n"
"buffer_load_dwordx4 v1, s[40:43], s69 offen offset:0 lds\n"
"ds_read_b64_tr_b16 a[200:201], v7 offset:16384\n"
"ds_read_b64_tr_b16 a[202:203], v7 offset:20480\n"
"v_mfma_f32_32x32x16_bf16 v[240:255], a[220:223], a[128:131], v[240:255]\n"
"ds_read_b64_tr_b16 a[204:205], v8 offset:16384\n"
"ds_read_b64_tr_b16 a[206:207], v8 offset:20480\n"
"s_waitcnt lgkmcnt(4)\n"
"v_mfma_f32_32x32x16_bf16 v[128:143], a[192:195], a[132:135], v[128:143]\n"
"buffer_load_dwordx4 v2, s[40:43], s69 offen offset:1024 lds\n"
"ds_read_b64_tr_b16 a[208:209], v5 offset:16640\n"
"ds_read_b64_tr_b16 a[210:211], v5 offset:20736\n"
"v_mfma_f32_32x32x16_bf16 v[144:159], a[196:199], a[132:135], v[144:159]\n"
"ds_read_b64_tr_b16 a[212:213], v6 offset:16640\n"
"ds_read_b64_tr_b16 a[214:215], v6 offset:20736\n"
"s_waitcnt lgkmcnt(4)\n"
"v_mfma_f32_32x32x16_bf16 v[160:175], a[200:203], a[132:135], v[160:175]\n"
"buffer_load_dwordx4 v3, s[40:43], s69 offen offset:2048 lds\n"
"ds_read_b64_tr_b16 a[216:217], v7 offset:16640\n"
"ds_read_b64_tr_b16 a[218:219], v7 offset:20736\n"
"v_mfma_f32_32x32x16_bf16 v[176:191], a[204:207], a[132:135], v[176:191]\n"
"ds_read_b64_tr_b16 a[220:221], v8 offset:16640\n"
"ds_read_b64_tr_b16 a[222:223], v8 offset:20736\n"
"s_waitcnt lgkmcnt(4)\n"
"v_mfma_f32_32x32x16_bf16 v[192:207], a[208:211], a[132:135], v[192:207]\n"
"s_waitcnt vmcnt(11)\n"
"s_barrier\n"
"buffer_load_dwordx4 v4, s[40:43], s69 offen offset:3072 lds\n"
"ds_read_b64_tr_b16 a[192:193], v5 offset:32768\n"
"ds_read_b64_tr_b16 a[194:195], v5 offset:36864\n"
"v_mfma_f32_32x32x16_bf16 v[208:223], a[212:215], a[132:135], v[208:223]\n"
"ds_read_b64_tr_b16 a[196:197], v6 offset:32768\n"
"ds_read_b64_tr_b16 a[198:199], v6 offset:36864\n"
"s_waitcnt lgkmcnt(4)\n"
"v_mfma_f32_32x32x16_bf16 v[224:239], a[216:219], a[132:135], v[224:239]\n"
"s_add_u32 m0, s68, 98304\n"
"s_mov_b32 s69, 2883584\n"
"buffer_load_dwordx4 v1, s[40:43], s69 offen offset:0 lds\n"
"ds_read_b64_tr_b16 a[200:201], v7 offset:32768\n"
"ds_read_b64_tr_b16 a[202:203], v7 offset:36864\n"
"v_mfma_f32_32x32x16_bf16 v[240:255], a[220:223], a[132:135], v[240:255]\n"
"ds_read_b64_tr_b16 a[204:205], v8 offset:32768\n"
"ds_read_b64_tr_b16 a[206:207], v8 offset:36864\n"
"s_waitcnt lgkmcnt(4)\n"
"v_mfma_f32_32x32x16_bf16 v[128:143], a[192:195], a[136:139], v[128:143]\n"
"buffer_load_dwordx4 v2, s[40:43], s69 offen offset:1024 lds\n"
"ds_read_b64_tr_b16 a[208:209], v5 offset:33024\n"
"ds_read_b64_tr_b16 a[210:211], v5 offset:37120\n"
"v_mfma_f32_32x32x16_bf16 v[144:159], a[196:199], a[136:139], v[144:159]\n"
"ds_read_b64_tr_b16 a[212:213], v6 offset:33024\n"
"ds_read_b64_tr_b16 a[214:215], v6 offset:37120\n"
"s_waitcnt lgkmcnt(4)\n"
"v_mfma_f32_32x32x16_bf16 v[160:175], a[200:203], a[136:139], v[160:175]\n"
"buffer_load_dwordx4 v3, s[40:43], s69 offen offset:2048 lds\n"
"ds_read_b64_tr_b16 a[216:217], v7 offset:33024\n"
"ds_read_b64_tr_b16 a[218:219], v7 offset:37120\n"
"v_mfma_f32_32x32x16_bf16 v[176:191], a[204:207], a[136:139], v[176:191]\n"
"ds_read_b64_tr_b16 a[220:221], v8 offset:33024\n"
"ds_read_b64_tr_b16 a[222:223], v8 offset:37120\n"
"s_waitcnt lgkmcnt(4)\n"
"v_mfma_f32_32x32x16_bf16 v[192:207], a[208:211], a[136:139], v[192:207]\n"
"s_waitcnt vmcnt(11)\n"
"s_barrier\n"
"buffer_load_dwordx4 v4, s[40:43], s69 offen offset:3072 lds\n"
"ds_read_b64_tr_b16 a[192:193], v5 offset:49152\n"
"ds_read_b64_tr_b16 a[194:195], v5 offset:53248\n"
"v_mfma_f32_32x32x16_bf16 v[208:223], a[212:215], a[136:139], v[208:223]\n"
"ds_read_b64_tr_b16 a[196:197], v6 offset:49152\n"
"ds_read_b64_tr_b16 a[198:199], v6 offset:53248\n"
"s_waitcnt lgkmcnt(4)\n"
"v_mfma_f32_32x32x16_bf16 v[224:239], a[216:219], a[136:139], v[224:239]\n"
"s_add_u32 m0, s68, 114688\n"
"s_mov_b32 s69, 3014656\n"
"buffer_load_dwordx4 v1, s[40:43], s69 offen offset:0 lds\n"
"ds_read_b64_tr_b16 a[200:201], v7 offset:49152\n"
"ds_read_b64_tr_b16 a[202:203], v7 offset:53248\n"
"v_mfma_f32_32x32x16_bf16 v[240:255], a[220:223], a[136:139], v[240:255]\n"
"ds_read_b64_tr_b16 a[204:205], v8 offset:49152\n"
"ds_read_b64_tr_b16 a[206:207], v8 offset:53248\n"
"s_waitcnt lgkmcnt(4)\n"
"v_mfma_f32_32x32x16_bf16 v[128:143], a[192:195], a[140:143], v[128:143]\n"
"buffer_load_dwordx4 v2, s[40:43], s69 offen offset:1024 lds\n"
"ds_read_b64_tr_b16 a[208:209], v5 offset:49408\n"
"ds_read_b64_tr_b16 a[210:211], v5 offset:53504\n"
"v_mfma_f32_32x32x16_bf16 v[144:159], a[196:199], a[140:143], v[144:159]\n"
"ds_read_b64_tr_b16 a[212:213], v6 offset:49408\n"
"ds_read_b64_tr_b16 a[214:215], v6 offset:53504\n"
"s_waitcnt lgkmcnt(4)\n"
"v_mfma_f32_32x32x16_bf16 v[160:175], a[200:203], a[140:143], v[160:175]\n"
"buffer_load_dwordx4 v3, s[40:43], s69 offen offset:2048 lds\n"
"ds_read_b64_tr_b16 a[216:217], v7 offset:49408\n"
"ds_read_b64_tr_b16 a[218:219], v7 offset:53504\n"
"v_mfma_f32_32x32x16_bf16 v[176:191], a[204:207], a[140:143], v[176:191]\n"
"ds_read_b64_tr_b16 a[220:221], v8 offset:49408\n"
"ds_read_b64_tr_b16 a[222:223], v8 offset:53504\n"
"s_waitcnt lgkmcnt(4)\n"
"v_mfma_f32_32x32x16_bf16 v[192:207], a[208:211], a[140:143], v[192:207]\n"
"s_waitcnt vmcnt(11)\n"
"s_barrier\n"
"buffer_load_dwordx4 v4, s[40:43], s69 offen offset:3072 lds\n"
"v_xor_b32 v5, 0x10000, v5\n"
"ds_read_b64_tr_b16 a[192:193], v5 offset:0\n"
"ds_read_b64_tr_b16 a[194:195], v5 offset:4096\n"
"v_mfma_f32_32x32x16_bf16 v[208:223], a[212:215], a[140:143], v[208:223]\n"
"v_xor_b32 v6, 0x10000, v6\n"
"ds_read_b64_tr_b16 a[196:197], v6 offset:0\n"
"ds_read_b64_tr_b16 a[198:199], v6 offset:4096\n"
"s_waitcnt lgkmcnt(4)\n"
"v_mfma_f32_32x32x16_bf16 v[224:239], a[216:219], a[140:143], v[224:239]\n"
"s_add_u32 m0, s68, 0\n"
"s_mov_b32 s69, 3145728\n"
"buffer_load_dwordx4 v1, s[40:43], s69 offen offset:0 lds\n"
"v_xor_b32 v7, 0x10000, v7\n"
"ds_read_b64_tr_b16 a[200:201], v7 offset:0\n"
"ds_read_b64_tr_b16 a[202:203], v7 offset:4096\n"
"v_mfma_f32_32x32x16_bf16 v[240:255], a[220:223], a[140:143], v[240:255]\n"
"v_xor_b32 v8, 0x10000, v8\n"
"ds_read_b64_tr_b16 a[204:205], v8 offset:0\n"
"ds_read_b64_tr_b16 a[206:207], v8 offset:4096\n"
"s_waitcnt lgkmcnt(4)\n"
"v_mfma_f32_32x32x16_bf16 v[128:143], a[192:195], a[144:147], v[128:143]\n"
"buffer_load_dwordx4 v2, s[40:43], s69 offen offset:1024 lds\n"
"ds_read_b64_tr_b16 a[208:209], v5 offset:256\n"
"ds_read_b64_tr_b16 a[210:211], v5 offset:4352\n"
"v_mfma_f32_32x32x16_bf16 v[144:159], a[196:199], a[144:147], v[144:159]\n"
"ds_read_b64_tr_b16 a[212:213], v6 offset:256\n"
"ds_read_b64_tr_b16 a[214:215], v6 offset:4352\n"
"s_waitcnt lgkmcnt(4)\n"
"v_mfma_f32_32x32x16_bf16 v[160:175], a[200:203], a[144:147], v[160:175]\n"
"buffer_load_dwordx4 v3, s[40:43], s69 offen offset:2048 lds\n"
"ds_read_b64_tr_b16 a[216:217], v7 offset:256\n"
"ds_read_b64_tr_b16 a[218:219], v7 offset:4352\n"
"v_mfma_f32_32x32x16_bf16 v[176:191], a[204:207], a[144:147], v[176:191]\n"
"ds_read_b64_tr_b16 a[220:221], v8 offset:256\n"
"ds_read_b64_tr_b16 a[222:223], v8 offset:4352\n"
"s_waitcnt lgkmcnt(4)\n"
"v_mfma_f32_32x32x16_bf16 v[192:207], a[208:211], a[144:147], v[192:207]\n"
"s_waitcnt vmcnt(11)\n"
"s_barrier\n"
"buffer_load_dwordx4 v4, s[40:43], s69 offen offset:3072 lds\n"
"ds_read_b64_tr_b16 a[192:193], v5 offset:16384\n"
"ds_read_b64_tr_b16 a[194:195], v5 offset:20480\n"
"v_mfma_f32_32x32x16_bf16 v[208:223], a[212:215], a[144:147], v[208:223]\n"
"ds_read_b64_tr_b16 a[196:197], v6 offset:16384\n"
"ds_read_b64_tr_b16 a[198:199], v6 offset:20480\n"
"s_waitcnt lgkmcnt(4)\n"
"v_mfma_f32_32x32x16_bf16 v[224:239], a[216:219], a[144:147], v[224:239]\n"
"s_add_u32 m0, s68, 16384\n"
"s_mov_b32 s69, 3276800\n"
"buffer_load_dwordx4 v1, s[40:43], s69 offen offset:0 lds\n"
"ds_read_b64_tr_b16 a[200:201], v7 offset:16384\n"
"ds_read_b64_tr_b16 a[202:203], v7 offset:20480\n"
"v_mfma_f32_32x32x16_bf16 v[240:255], a[220:223], a[144:147], v[240:255]\n"
"ds_read_b64_tr_b16 a[204:205], v8 offset:16384\n"
"ds_read_b64_tr_b16 a[206:207], v8 offset:20480\n"
"s_waitcnt lgkmcnt(4)\n"
"v_mfma_f32_32x32x16_bf16 v[128:143], a[192:195], a[148:151], v[128:143]\n"
"buffer_load_dwordx4 v2, s[40:43], s69 offen offset:1024 lds\n"
"ds_read_b64_tr_b16 a[208:209], v5 offset:16640\n"
"ds_read_b64_tr_b16 a[210:211], v5 offset:20736\n"
"v_mfma_f32_32x32x16_bf16 v[144:159], a[196:199], a[148:151], v[144:159]\n"
"ds_read_b64_tr_b16 a[212:213], v6 offset:16640\n"
"ds_read_b64_tr_b16 a[214:215], v6 offset:20736\n"
"s_waitcnt lgkmcnt(4)\n"
"v_mfma_f32_32x32x16_bf16 v[160:175], a[200:203], a[148:151], v[160:175]\n"
"buffer_load_dwordx4 v3, s[40:43], s69 offen offset:2048 lds\n"
"ds_read_b64_tr_b16 a[216:217], v7 offset:16640\n"
"ds_read_b64_tr_b16 a[218:219], v7 offset:20736\n"
"v_mfma_f32_32x32x16_bf16 v[176:191], a[204:207], a[148:151], v[176:191]\n"
"ds_read_b64_tr_b16 a[220:221], v8 offset:16640\n"
"ds_read_b64_tr_b16 a[222:223], v8 offset:20736\n"
"s_waitcnt lgkmcnt(4)\n"
"v_mfma_f32_32x32x16_bf16 v[192:207], a[208:211], a[148:151], v[192:207]\n"
"s_waitcnt vmcnt(11)\n"
"s_barrier\n"
"buffer_load_dwordx4 v4, s[40:43], s69 offen offset:3072 lds\n"
"ds_read_b64_tr_b16 a[192:193], v5 offset:32768\n"
"ds_read_b64_tr_b16 a[194:195], v5 offset:36864\n"
"v_mfma_f32_32x32x16_bf16 v[208:223], a[212:215], a[148:151], v[208:223]\n"
"ds_read_b64_tr_b16 a[196:197], v6 offset:32768\n"
"ds_read_b64_tr_b16 a[198:199], v6 offset:36864\n"
"s_waitcnt lgkmcnt(4)\n"
"v_mfma_f32_32x32x16_bf16 v[224:239], a[216:219], a[148:151], v[224:239]\n"
"s_add_u32 m0, s68, 32768\n"
"s_mov_b32 s69, 3407872\n"
"buffer_load_dwordx4 v1, s[40:43], s69 offen offset:0 lds\n"
"ds_read_b64_tr_b16 a[200:201], v7 offset:32768\n"
"ds_read_b64_tr_b16 a[202:203], v7 offset:36864\n"
"v_mfma_f32_32x32x16_bf16 v[240:255], a[220:223], a[148:151], v[240:255]\n"
"ds_read_b64_tr_b16 a[204:205], v8 offset:32768\n"
"ds_read_b64_tr_b16 a[206:207], v8 offset:36864\n"
"s_waitcnt lgkmcnt(4)\n"
"v_mfma_f32_32x32x16_bf16 v[128:143], a[192:195], a[152:155], v[128:143]\n"
"buffer_load_dwordx4 v2, s[40:43], s69 offen offset:1024 lds\n"
"ds_read_b64_tr_b16 a[208:209], v5 offset:33024\n"
"ds_read_b64_tr_b16 a[210:211], v5 offset:37120\n"
"v_mfma_f32_32x32x16_bf16 v[144:159], a[196:199], a[152:155], v[144:159]\n"
"ds_read_b64_tr_b16 a[212:213], v6 offset:33024\n"
"ds_read_b64_tr_b16 a[214:215], v6 offset:37120\n"
"s_waitcnt lgkmcnt(4)\n"
"v_mfma_f32_32x32x16_bf16 v[160:175], a[200:203], a[152:155], v[160:175]\n"
"buffer_load_dwordx4 v3, s[40:43], s69 offen offset:2048 lds\n"
"ds_read_b64_tr_b16 a[216:217], v7 offset:33024\n"
"ds_read_b64_tr_b16 a[218:219], v7 offset:37120\n"
"v_mfma_f32_32x32x16_bf16 v[176:191], a[204:207], a[152:155], v[176:191]\n"
"ds_read_b64_tr_b16 a[220:221], v8 offset:33024\n"
"ds_read_b64_tr_b16 a[222:223], v8 offset:37120\n"
"s_waitcnt lgkmcnt(4)\n"
"v_mfma_f32_32x32x16_bf16 v[192:207], a[208:211], a[152:155], v[192:207]\n"
"s_waitcnt vmcnt(11)\n"
"s_barrier\n"
"buffer_load_dwordx4 v4, s[40:43], s69 offen offset:3072 lds\n"
"ds_read_b64_tr_b16 a[192:193], v5 offset:49152\n"
"ds_read_b64_tr_b16 a[194:195], v5 offset:53248\n"
"v_mfma_f32_32x32x16_bf16 v[208:223], a[212:215], a[152:155], v[208:223]\n"
"ds_read_b64_tr_b16 a[196:197], v6 offset:49152\n"
"ds_read_b64_tr_b16 a[198:199], v6 offset:53248\n"
"s_waitcnt lgkmcnt(4)\n"
"v_mfma_f32_32x32x16_bf16 v[224:239], a[216:219], a[152:155], v[224:239]\n"
"s_add_u32 m0, s68, 49152\n"
"s_mov_b32 s69, 3538944\n"
"buffer_load_dwordx4 v1, s[40:43], s69 offen offset:0 lds\n"
"ds_read_b64_tr_b16 a[200:201], v7 offset:49152\n"
"ds_read_b64_tr_b16 a[202:203], v7 offset:53248\n"
"v_mfma_f32_32x32x16_bf16 v[240:255], a[220:223], a[152:155], v[240:255]\n"
"ds_read_b64_tr_b16 a[204:205], v8 offset:49152\n"
"ds_read_b64_tr_b16 a[206:207], v8 offset:53248\n"
"s_waitcnt lgkmcnt(4)\n"
"v_mfma_f32_32x32x16_bf16 v[128:143], a[192:195], a[156:159], v[128:143]\n"
"buffer_load_dwordx4 v2, s[40:43], s69 offen offset:1024 lds\n"
"ds_read_b64_tr_b16 a[208:209], v5 offset:49408\n"
"ds_read_b64_tr_b16 a[210:211], v5 offset:53504\n"
"v_mfma_f32_32x32x16_bf16 v[144:159], a[196:199], a[156:159], v[144:159]\n"
"ds_read_b64_tr_b16 a[212:213], v6 offset:49408\n"
"ds_read_b64_tr_b16 a[214:215], v6 offset:53504\n"
"s_waitcnt lgkmcnt(4)\n"
"v_mfma_f32_32x32x16_bf16 v[160:175], a[200:203], a[156:159], v[160:175]\n"
"buffer_load_dwordx4 v3, s[40:43], s69 offen offset:2048 lds\n"
"ds_read_b64_tr_b16 a[216:217], v7 offset:49408\n"
"ds_read_b64_tr_b16 a[218:219], v7 offset:53504\n"
"v_mfma_f32_32x32x16_bf16 v[176:191], a[204:207], a[156:159], v[176:191]\n"
"ds_read_b64_tr_b16 a[220:221], v8 offset:49408\n"
"ds_read_b64_tr_b16 a[222:223], v8 offset:53504\n"
"s_waitcnt lgkmcnt(4)\n"
"v_mfma_f32_32x32x16_bf16 v[192:207], a[208:211], a[156:159], v[192:207]\n"
"s_waitcnt vmcnt(11)\n"
"s_barrier\n"
"buffer_load_dwordx4 v4, s[40:43], s69 offen offset:3072 lds\n"
"v_xor_b32 v5, 0x10000, v5\n"
"ds_read_b64_tr_b16 a[192:193], v5 offset:0\n"
"ds_read_b64_tr_b16 a[194:195], v5 offset:4096\n"
"v_mfma_f32_32x32x16_bf16 v[208:223], a[212:215], a[156:159], v[208:223]\n"
"v_lshrrev_b32 v28, 5, v0\n"
"s_lshl_b32 s75, s71, 3\n"
"v_add_u32 v28, s75, v28\n"
"v_and_b32 v29, 31, v0\n"
"v_add_u32 v26, 0, v28\n"
"v_and_b32 v27, 15, v26\n"
"v_xor_b32 v27, v27, v29\n"
"v_lshlrev_b32 v27, 4, v27\n"
"v_mul_u32_u24 v26, 512, v26\n"
"v_add_u32 v27, v26, v27\n"
"v_subrev_u32 v1, 0, v27\n"
"v_add_u32 v26, 2, v28\n"
"v_and_b32 v27, 15, v26\n"
"v_xor_b32 v27, v27, v29\n"
"v_lshlrev_b32 v27, 4, v27\n"
"v_mul_u32_u24 v26, 512, v26\n"
"v_xor_b32 v6, 0x10000, v6\n"
"ds_read_b64_tr_b16 a[196:197], v6 offset:0\n"
"ds_read_b64_tr_b16 a[198:199], v6 offset:4096\n"
"s_waitcnt lgkmcnt(4)\n"
"v_mfma_f32_32x32x16_bf16 v[224:239], a[216:219], a[156:159], v[224:239]\n"
"v_add_u32 v27, v26, v27\n"
"v_subrev_u32 v2, 1024, v27\n"
"v_add_u32 v26, 4, v28\n"
"v_and_b32 v27, 15, v26\n"
"v_xor_b32 v27, v27, v29\n"
"v_lshlrev_b32 v27, 4, v27\n"
"v_mul_u32_u24 v26, 512, v26\n"
"v_add_u32 v27, v26, v27\n"
"v_subrev_u32 v3, 2048, v27\n"
"v_add_u32 v26, 6, v28\n"
"v_and_b32 v27, 15, v26\n"
"v_xor_b32 v27, v27, v29\n"
"v_lshlrev_b32 v27, 4, v27\n"
"v_mul_u32_u24 v26, 512, v26\n"
"v_add_u32 v27, v26, v27\n"
"v_subrev_u32 v4, 3072, v27\n"
"s_add_u32 m0, s68, 65536\n"
"s_mov_b32 s69, 0\n"
"buffer_load_dwordx4 v1, s[44:47], s69 offen offset:0 lds\n"
"v_xor_b32 v7, 0x10000, v7\n"
"ds_read_b64_tr_b16 a[200:201], v7 offset:0\n"
"ds_read_b64_tr_b16 a[202:203], v7 offset:4096\n"
"v_mfma_f32_32x32x16_bf16 v[240:255], a[220:223], a[156:159], v[240:255]\n"
"v_xor_b32 v8, 0x10000, v8\n"
"ds_read_b64_tr_b16 a[204:205], v8 offset:0\n"
"ds_read_b64_tr_b16 a[206:207], v8 offset:4096\n"
"s_waitcnt lgkmcnt(4)\n"
"v_mfma_f32_32x32x16_bf16 v[128:143], a[192:195], a[160:163], v[128:143]\n"
"buffer_load_dwordx4 v2, s[44:47], s69 offen offset:1024 lds\n"
"ds_read_b64_tr_b16 a[208:209], v5 offset:256\n"
"ds_read_b64_tr_b16 a[210:211], v5 offset:4352\n"
"v_mfma_f32_32x32x16_bf16 v[144:159], a[196:199], a[160:163], v[144:159]\n"
"ds_read_b64_tr_b16 a[212:213], v6 offset:256\n"
"ds_read_b64_tr_b16 a[214:215], v6 offset:4352\n"
"s_waitcnt lgkmcnt(4)\n"
"v_mfma_f32_32x32x16_bf16 v[160:175], a[200:203], a[160:163], v[160:175]\n"
"buffer_load_dwordx4 v3, s[44:47], s69 offen offset:2048 lds\n"
"ds_read_b64_tr_b16 a[216:217], v7 offset:256\n"
"ds_read_b64_tr_b16 a[218:219], v7 offset:4352\n"
"v_mfma_f32_32x32x16_bf16 v[176:191], a[204:207], a[160:163], v[176:191]\n"
"ds_read_b64_tr_b16 a[220:221], v8 offset:256\n"
"ds_read_b64_tr_b16 a[222:223], v8 offset:4352\n"
"s_waitcnt lgkmcnt(4)\n"
"v_mfma_f32_32x32x16_bf16 v[192:207], a[208:211], a[160:163], v[192:207]\n"
"s_waitcnt vmcnt(11)\n"
"s_barrier\n"
"buffer_load_dwordx4 v4, s[44:47], s69 offen offset:3072 lds\n"
"ds_read_b64_tr_b16 a[192:193], v5 offset:16384\n"
"ds_read_b64_tr_b16 a[194:195], v5 offset:20480\n"
"v_mfma_f32_32x32x16_bf16 v[208:223], a[212:215], a[160:163], v[208:223]\n"
"ds_read_b64_tr_b16 a[196:197], v6 offset:16384\n"
"ds_read_b64_tr_b16 a[198:199], v6 offset:20480\n"
"s_waitcnt lgkmcnt(4)\n"
"v_mfma_f32_32x32x16_bf16 v[224:239], a[216:219], a[160:163], v[224:239]\n"
"s_add_u32 m0, s68, 81920\n"
"s_mov_b32 s69, 16384\n"
"buffer_load_dwordx4 v1, s[44:47], s69 offen offset:0 lds\n"
"ds_read_b64_tr_b16 a[200:201], v7 offset:16384\n"
"ds_read_b64_tr_b16 a[202:203], v7 offset:20480\n"
"v_mfma_f32_32x32x16_bf16 v[240:255], a[220:223], a[160:163], v[240:255]\n"
"ds_read_b64_tr_b16 a[204:205], v8 offset:16384\n"
"ds_read_b64_tr_b16 a[206:207], v8 offset:20480\n"
"s_waitcnt lgkmcnt(4)\n"
"v_mfma_f32_32x32x16_bf16 v[128:143], a[192:195], a[164:167], v[128:143]\n"
"buffer_load_dwordx4 v2, s[44:47], s69 offen offset:1024 lds\n"
"ds_read_b64_tr_b16 a[208:209], v5 offset:16640\n"
"ds_read_b64_tr_b16 a[210:211], v5 offset:20736\n"
"v_mfma_f32_32x32x16_bf16 v[144:159], a[196:199], a[164:167], v[144:159]\n"
"ds_read_b64_tr_b16 a[212:213], v6 offset:16640\n"
"ds_read_b64_tr_b16 a[214:215], v6 offset:20736\n"
"s_waitcnt lgkmcnt(4)\n"
"v_mfma_f32_32x32x16_bf16 v[160:175], a[200:203], a[164:167], v[160:175]\n"
"buffer_load_dwordx4 v3, s[44:47], s69 offen offset:2048 lds\n"
"ds_read_b64_tr_b16 a[216:217], v7 offset:16640\n"
"ds_read_b64_tr_b16 a[218:219], v7 offset:20736\n"
"v_mfma_f32_32x32x16_bf16 v[176:191], a[204:207], a[164:167], v[176:191]\n"
"ds_read_b64_tr_b16 a[220:221], v8 offset:16640\n"
"ds_read_b64_tr_b16 a[222:223], v8 offset:20736\n"
"s_waitcnt lgkmcnt(4)\n"
"v_mfma_f32_32x32x16_bf16 v[192:207], a[208:211], a[164:167], v[192:207]\n"
"s_waitcnt vmcnt(11)\n"
"s_barrier\n"
"buffer_load_dwordx4 v4, s[44:47], s69 offen offset:3072 lds\n"
"ds_read_b64_tr_b16 a[192:193], v5 offset:32768\n"
"ds_read_b64_tr_b16 a[194:195], v5 offset:36864\n"
"v_mfma_f32_32x32x16_bf16 v[208:223], a[212:215], a[164:167], v[208:223]\n"
"ds_read_b64_tr_b16 a[196:197], v6 offset:32768\n"
"ds_read_b64_tr_b16 a[198:199], v6 offset:36864\n"
"s_waitcnt lgkmcnt(4)\n"
"v_mfma_f32_32x32x16_bf16 v[224:239], a[216:219], a[164:167], v[224:239]\n"
"s_add_u32 m0, s68, 98304\n"
"s_mov_b32 s69, 32768\n"
"buffer_load_dwordx4 v1, s[44:47], s69 offen offset:0 lds\n"
"ds_read_b64_tr_b16 a[200:201], v7 offset:32768\n"
"ds_read_b64_tr_b16 a[202:203], v7 offset:36864\n"
"v_mfma_f32_32x32x16_bf16 v[240:255], a[220:223], a[164:167], v[240:255]\n"
"ds_read_b64_tr_b16 a[204:205], v8 offset:32768\n"
"ds_read_b64_tr_b16 a[206:207], v8 offset:36864\n"
"s_waitcnt lgkmcnt(4)\n"
"v_mfma_f32_32x32x16_bf16 v[128:143], a[192:195], a[168:171], v[128:143]\n"
"buffer_load_dwordx4 v2, s[44:47], s69 offen offset:1024 lds\n"
"ds_read_b64_tr_b16 a[208:209], v5 offset:33024\n"
"ds_read_b64_tr_b16 a[210:211], v5 offset:37120\n"
"v_mfma_f32_32x32x16_bf16 v[144:159], a[196:199], a[168:171], v[144:159]\n"
"ds_read_b64_tr_b16 a[212:213], v6 offset:33024\n"
"ds_read_b64_tr_b16 a[214:215], v6 offset:37120\n"
"s_waitcnt lgkmcnt(4)\n"
"v_mfma_f32_32x32x16_bf16 v[160:175], a[200:203], a[168:171], v[160:175]\n"
"buffer_load_dwordx4 v3, s[44:47], s69 offen offset:2048 lds\n"
"ds_read_b64_tr_b16 a[216:217], v7 offset:33024\n"
"ds_read_b64_tr_b16 a[218:219], v7 offset:37120\n"
"v_mfma_f32_32x32x16_bf16 v[176:191], a[204:207], a[168:171], v[176:191]\n"
"ds_read_b64_tr_b16 a[220:221], v8 offset:33024\n"
"ds_read_b64_tr_b16 a[222:223], v8 offset:37120\n"
"s_waitcnt lgkmcnt(4)\n"
"v_mfma_f32_32x32x16_bf16 v[192:207], a[208:211], a[168:171], v[192:207]\n"
"s_waitcnt vmcnt(11)\n"
"s_barrier\n"
"buffer_load_dwordx4 v4, s[44:47], s69 offen offset:3072 lds\n"
"ds_read_b64_tr_b16 a[192:193], v5 offset:49152\n"
"ds_read_b64_tr_b16 a[194:195], v5 offset:53248\n"
"v_mfma_f32_32x32x16_bf16 v[208:223], a[212:215], a[168:171], v[208:223]\n"
"ds_read_b64_tr_b16 a[196:197], v6 offset:49152\n"
"ds_read_b64_tr_b16 a[198:199], v6 offset:53248\n"
"s_waitcnt lgkmcnt(4)\n"
"v_mfma_f32_32x32x16_bf16 v[224:239], a[216:219], a[168:171], v[224:239]\n"
"s_add_u32 m0, s68, 114688\n"
"s_mov_b32 s69, 49152\n"
"buffer_load_dwordx4 v1, s[44:47], s69 offen offset:0 lds\n"
"ds_read_b64_tr_b16 a[200:201], v7 offset:49152\n"
"ds_read_b64_tr_b16 a[202:203], v7 offset:53248\n"
"v_mfma_f32_32x32x16_bf16 v[240:255], a[220:223], a[168:171], v[240:255]\n"
"ds_read_b64_tr_b16 a[204:205], v8 offset:49152\n"
"ds_read_b64_tr_b16 a[206:207], v8 offset:53248\n"
"s_waitcnt lgkmcnt(4)\n"
"v_mfma_f32_32x32x16_bf16 v[128:143], a[192:195], a[172:175], v[128:143]\n"
"buffer_load_dwordx4 v2, s[44:47], s69 offen offset:1024 lds\n"
"ds_read_b64_tr_b16 a[208:209], v5 offset:49408\n"
"ds_read_b64_tr_b16 a[210:211], v5 offset:53504\n"
"v_mfma_f32_32x32x16_bf16 v[144:159], a[196:199], a[172:175], v[144:159]\n"
"ds_read_b64_tr_b16 a[212:213], v6 offset:49408\n"
"ds_read_b64_tr_b16 a[214:215], v6 offset:53504\n"
"s_waitcnt lgkmcnt(4)\n"
"v_mfma_f32_32x32x16_bf16 v[160:175], a[200:203], a[172:175], v[160:175]\n"
"buffer_load_dwordx4 v3, s[44:47], s69 offen offset:2048 lds\n"
"ds_read_b64_tr_b16 a[216:217], v7 offset:49408\n"
"ds_read_b64_tr_b16 a[218:219], v7 offset:53504\n"
"v_mfma_f32_32x32x16_bf16 v[176:191], a[204:207], a[172:175], v[176:191]\n"
"ds_read_b64_tr_b16 a[220:221], v8 offset:49408\n"
"ds_read_b64_tr_b16 a[222:223], v8 offset:53504\n"
"s_waitcnt lgkmcnt(4)\n"
"v_mfma_f32_32x32x16_bf16 v[192:207], a[208:211], a[172:175], v[192:207]\n"
"s_waitcnt vmcnt(11)\n"
"s_barrier\n"
"buffer_load_dwordx4 v4, s[44:47], s69 offen offset:3072 lds\n"
"v_xor_b32 v9, 0x10000, v9\n"
"ds_read_b128 a[192:195], v9 offset:0\n"
"v_cvt_pk_bf16_f32 v32, v128, v129\n"
"v_cvt_pk_bf16_f32 v33, v130, v131\n"
"v_cvt_pk_bf16_f32 v34, v132, v133\n"
"v_cvt_pk_bf16_f32 v35, v134, v135\n"
"v_mfma_f32_32x32x16_bf16 v[208:223], a[212:215], a[172:175], v[208:223]\n"
"v_xor_b32 v10, 0x10000, v10\n"
"ds_read_b128 a[196:199], v10 offset:0\n"
"v_cvt_pk_bf16_f32 v36, v136, v137\n"
"v_cvt_pk_bf16_f32 v37, v138, v139\n"
"v_cvt_pk_bf16_f32 v38, v140, v141\n"
"v_cvt_pk_bf16_f32 v39, v142, v143\n"
"s_waitcnt lgkmcnt(2)\n"
"v_mfma_f32_32x32x16_bf16 v[224:239], a[216:219], a[172:175], v[224:239]\n"
"s_add_u32 m0, s68, 0\n"
"s_mov_b32 s69, 65536\n"
"buffer_load_dwordx4 v1, s[44:47], s69 offen offset:0 lds\n"
"v_xor_b32 v11, 0x10000, v11\n"
"ds_read_b128 a[200:203], v11 offset:0\n"
"v_cvt_pk_bf16_f32 v40, v144, v145\n"
"v_cvt_pk_bf16_f32 v41, v146, v147\n"
"v_cvt_pk_bf16_f32 v42, v148, v149\n"
"v_cvt_pk_bf16_f32 v43, v150, v151\n"
"v_mfma_f32_32x32x16_bf16 v[240:255], a[220:223], a[172:175], v[240:255]\n"
"v_xor_b32 v12, 0x10000, v12\n"
"ds_read_b128 a[204:207], v12 offset:0\n"
"v_cvt_pk_bf16_f32 v44, v152, v153\n"
"v_cvt_pk_bf16_f32 v45, v154, v155\n"
"v_cvt_pk_bf16_f32 v46, v156, v157\n"
"v_cvt_pk_bf16_f32 v47, v158, v159\n"
"s_waitcnt lgkmcnt(2)\n"
"v_mfma_f32_32x32x16_bf16 v[96:111], a[192:195], v[32:35], 0\n"
"buffer_load_dwordx4 v2, s[44:47], s69 offen offset:1024 lds\n"
"v_xor_b32 v13, 0x10000, v13\n"
"ds_read_b128 a[208:211], v13 offset:0\n"
"v_cvt_pk_bf16_f32 v48, v160, v161\n"
"v_cvt_pk_bf16_f32 v49, v162, v163\n"
"v_cvt_pk_bf16_f32 v50, v164, v165\n"
"v_cvt_pk_bf16_f32 v51, v166, v167\n"
"v_mfma_f32_32x32x16_bf16 v[96:111], a[196:199], v[36:39], v[96:111]\n"
"v_xor_b32 v14, 0x10000, v14\n"
"ds_read_b128 a[212:215], v14 offset:0\n"
"v_cvt_pk_bf16_f32 v52, v168, v169\n"
"v_cvt_pk_bf16_f32 v53, v170, v171\n"
"v_cvt_pk_bf16_f32 v54, v172, v173\n"
"v_cvt_pk_bf16_f32 v55, v174, v175\n"
"v_lshrrev_b32 v30, 5, v0\n"
"s_waitcnt lgkmcnt(2)\n"
"v_mfma_f32_32x32x16_bf16 v[96:111], a[200:203], v[40:43], v[96:111]\n"
"buffer_load_dwordx4 v3, s[44:47], s69 offen offset:2048 lds\n"
"v_xor_b32 v15, 0x10000, v15\n"
"ds_read_b128 a[216:219], v15 offset:0\n"
"v_cvt_pk_bf16_f32 v56, v176, v177\n"
"v_cvt_pk_bf16_f32 v57, v178, v179\n"
"v_cvt_pk_bf16_f32 v58, v180, v181\n"
"v_cvt_pk_bf16_f32 v59, v182, v183\n"
"v_bfe_u32 v29, v0, 2, 2\n"
"v_bfe_u32 v26, v0, 4, 1\n"
"v_mfma_f32_32x32x16_bf16 v[96:111], a[204:207], v[44:47], v[96:111]\n"
"v_xor_b32 v16, 0x10000, v16\n"
"ds_read_b128 a[220:223], v16 offset:0\n"
"v_cvt_pk_bf16_f32 v60, v184, v185\n"
"v_cvt_pk_bf16_f32 v61, v186, v187\n"
"v_cvt_pk_bf16_f32 v62, v188, v189\n"
"v_cvt_pk_bf16_f32 v63, v190, v191\n"
"v_lshlrev_b32 v26, 5, v26\n"
"s_waitcnt lgkmcnt(2)\n"
"v_mfma_f32_32x32x16_bf16 v[96:111], a[208:211], v[48:51], v[96:111]\n"
"buffer_load_dwordx4 v4, s[44:47], s69 offen offset:3072 lds\n"
"ds_read_b128 a[192:195], v9 offset:256\n"
"v_cvt_pk_bf16_f32 v64, v192, v193\n"
"v_cvt_pk_bf16_f32 v65, v194, v195\n"
"v_cvt_pk_bf16_f32 v66, v196, v197\n"
"v_cvt_pk_bf16_f32 v67, v198, v199\n"
"v_and_b32 v27, 1, v0\n"
"v_mfma_f32_32x32x16_bf16 v[96:111], a[212:215], v[52:55], v[96:111]\n"
"ds_read_b128 a[196:199], v10 offset:256\n"
"v_cvt_pk_bf16_f32 v68, v200, v201\n"
"v_cvt_pk_bf16_f32 v69, v202, v203\n"
"v_cvt_pk_bf16_f32 v70, v204, v205\n"
"v_cvt_pk_bf16_f32 v71, v206, v207\n"
"v_lshl_or_b32 v26, v27, 4, v26\n"
"s_waitcnt lgkmcnt(2)\n"
"v_mfma_f32_32x32x16_bf16 v[96:111], a[216:219], v[56:59], v[96:111]\n"
"s_add_u32 m0, s68, 16384\n"
"s_mov_b32 s69, 81920\n"
"buffer_load_dwordx4 v1, s[44:47], s69 offen offset:0 lds\n"
"ds_read_b128 a[200:203], v11 offset:256\n"
"v_cvt_pk_bf16_f32 v72, v208, v209\n"
"v_cvt_pk_bf16_f32 v73, v210, v211\n"
"v_cvt_pk_bf16_f32 v74, v212, v213\n"
"v_cvt_pk_bf16_f32 v75, v214, v215\n"
"v_bfe_u32 v27, v0, 1, 1\n"
"v_lshl_or_b32 v26, v27, 3, v26\n"
"s_mov_b64 s[40:41], %[w1]\n"
"s_mov_b32 s42, %[n1]\n"
"s_mov_b32 s43, 0x00020000\n"
"v_mfma_f32_32x32x16_bf16 v[96:111], a[220:223], v[60:63], v[96:111]\n"
"ds_read_b128 a[204:207], v12 offset:256\n"
"v_cvt_pk_bf16_f32 v76, v216, v217\n"
"v_cvt_pk_bf16_f32 v77, v218, v219\n"
"v_cvt_pk_bf16_f32 v78, v220, v221\n"
"v_cvt_pk_bf16_f32 v79, v222, v223\n"
"v_lshl_add_u32 v27, v30, 3, v29\n"
"s_waitcnt lgkmcnt(2)\n"
"v_mfma_f32_32x32x16_bf16 v[96:111], a[192:195], v[64:67], v[96:111]\n"
"buffer_load_dwordx4 v2, s[44:47], s69 offen offset:1024 lds\n"
"ds_read_b128 a[208:211], v13 offset:256\n"
"v_cvt_pk_bf16_f32 v80, v224, v225\n"
"v_cvt_pk_bf16_f32 v81, v226, v227\n"
"v_cvt_pk_bf16_f32 v82, v228, v229\n"
"v_cvt_pk_bf16_f32 v83, v230, v231\n"
"v_lshl_add_u32 v28, v27, 9, v26\n"
"v_mfma_f32_32x32x16_bf16 v[96:111], a[196:199], v[68:71], v[96:111]\n"
"ds_read_b128 a[212:215], v14 offset:256\n"
"v_cvt_pk_bf16_f32 v84, v232, v233\n"
"v_cvt_pk_bf16_f32 v85, v234, v235\n"
"v_cvt_pk_bf16_f32 v86, v236, v237\n"
"v_cvt_pk_bf16_f32 v87, v238, v239\n"
"v_xor_b32 v27, 0, v29\n"
"v_lshl_add_u32 v5, v27, 6, v28\n"
"s_waitcnt lgkmcnt(2)\n"
"v_mfma_f32_32x32x16_bf16 v[96:111], a[200:203], v[72:75], v[96:111]\n"
"buffer_load_dwordx4 v3, s[44:47], s69 offen offset:2048 lds\n"
"ds_read_b128 a[216:219], v15 offset:256\n"
"v_cvt_pk_bf16_f32 v88, v240, v241\n"
"v_cvt_pk_bf16_f32 v89, v242, v243\n"
"v_cvt_pk_bf16_f32 v90, v244, v245\n"
"v_cvt_pk_bf16_f32 v91, v246, v247\n"
"v_xor_b32 v27, 1, v29\n"
"v_mfma_f32_32x32x16_bf16 v[96:111], a[204:207], v[76:79], v[96:111]\n"
"ds_read_b128 a[220:223], v16 offset:256\n"
"v_cvt_pk_bf16_f32 v92, v248, v249\n"
"v_cvt_pk_bf16_f32 v93, v250, v251\n"
"v_cvt_pk_bf16_f32 v94, v252, v253\n"
"v_cvt_pk_bf16_f32 v95, v254, v255\n"
"v_lshl_add_u32 v6, v27, 6, v28\n"
"s_waitcnt lgkmcnt(2)\n"
"v_mfma_f32_32x32x16_bf16 v[96:111], a[208:211], v[80:83], v[96:111]\n"
"s_waitcnt vmcnt(15)\n"
"s_barrier\n"
"buffer_load_dwordx4 v4, s[44:47], s69 offen offset:3072 lds\n"
"ds_read_b128 a[192:195], v9 offset:16384\n"
"v_xor_b32 v27, 2, v29\n"
"v_mfma_f32_32x32x16_bf16 v[96:111], a[212:215], v[84:87], v[96:111]\n"
"ds_read_b128 a[196:199], v10 offset:16384\n"
"v_lshl_add_u32 v7, v27, 6, v28\n"
"v_xor_b32 v27, 3, v29\n"
"s_waitcnt lgkmcnt(2)\n"
"v_mfma_f32_32x32x16_bf16 v[96:111], a[216:219], v[88:91], v[96:111]\n"
"s_add_u32 m0, s68, 32768\n"
"s_mov_b32 s69, 98304\n"
"buffer_load_dwordx4 v1, s[44:47], s69 offen offset:0 lds\n"
"ds_read_b128 a[200:203], v11 offset:16384\n"
"v_lshl_add_u32 v8, v27, 6, v28\n"
"v_mfma_f32_32x32x16_bf16 v[96:111], a[220:223], v[92:95], v[96:111]\n"
"ds_read_b128 a[204:207], v12 offset:16384\n"
"s_waitcnt lgkmcnt(2)\n"
"v_mfma_f32_32x32x16_bf16 v[112:127], a[192:195], v[32:35], 0\n"
"buffer_load_dwordx4 v2, s[44:47], s69 offen offset:1024 lds\n"
"ds_read_b128 a[208:211], v13 offset:16384\n"
"v_mfma_f32_32x32x16_bf16 v[112:127], a[196:199], v[36:39], v[112:127]\n"
"ds_read_b128 a[212:215], v14 offset:16384\n"
"v_max3_f32 v18, v96, v97, v97\n"
"v_max3_f32 v18, v98, v99, v18\n"
"v_max3_f32 v18, v100, v101, v18\n"
"v_max3_f32 v18, v102, v103, v18\n"
"s_waitcnt lgkmcnt(2)\n"
"v_mfma_f32_32x32x16_bf16 v[112:127], a[200:203], v[40:43], v[112:127]\n"
"buffer_load_dwordx4 v3, s[44:47], s69 offen offset:2048 lds\n"
"ds_read_b128 a[216:219], v15 offset:16384\n"
"v_max3_f32 v18, v104, v105, v18\n"
"v_max3_f32 v18, v106, v107, v18\n"
"v_max3_f32 v18, v108, v109, v18\n"
"v_max3_f32 v18, v110, v111, v18\n"
"v_mul_f32 v19, s73, v18\n"
"v_mfma_f32_32x32x16_bf16 v[112:127], a[204:207], v[44:47], v[112:127]\n"
"ds_read_b128 a[220:223], v16 offset:16384\n"
"v_floor_f32 v19, v19\n"
"v_accvgpr_write_b32 a48, v19\n"
"v_fmamk_f32 v96, v96, 0x3db8aa3b, v19\n"
"v_fmamk_f32 v97, v97, 0x3db8aa3b, v19\n"
"s_lshl_b32 s75, %[T], 2\n"
"s_add_u32 s75, s75, s71\n"
"s_mul_i32 s75, s75, 16\n"
"v_min_u32 v30, 15, v0\n"
"v_add_u32 v30, s75, v30\n"
"v_lshrrev_b32 v31, 0, v30\n"
"v_mul_u32_u24 v31, 0, v31\n"
"v_lshl_add_u32 v30, v30, 7, v31\n"
"buffer_load_dword a226, v30, s[48:51], 0 offen\n"
"s_waitcnt lgkmcnt(2)\n"
"v_mfma_f32_32x32x16_bf16 v[112:127], a[208:211], v[48:51], v[112:127]\n"
"buffer_load_dwordx4 v4, s[44:47], s69 offen offset:3072 lds\n"
"ds_read_b128 a[192:195], v9 offset:16640\n"
"v_exp_f32 v96, v96\n"
"v_fmamk_f32 v98, v98, 0x3db8aa3b, v19\n"
"v_exp_f32 v97, v97\n"
"v_mfma_f32_32x32x16_bf16 v[112:127], a[212:215], v[52:55], v[112:127]\n"
"ds_read_b128 a[196:199], v10 offset:16640\n"
"v_fmamk_f32 v99, v99, 0x3db8aa3b, v19\n"
"v_exp_f32 v98, v98\n"
"v_fmamk_f32 v100, v100, 0x3db8aa3b, v19\n"
"s_waitcnt lgkmcnt(2)\n"
"v_mfma_f32_32x32x16_bf16 v[112:127], a[216:219], v[56:59], v[112:127]\n"
"s_add_u32 m0, s68, 49152\n"
"s_mov_b32 s69, 114688\n"
"buffer_load_dwordx4 v1, s[44:47], s69 offen offset:0 lds\n"
"ds_read_b128 a[200:203], v11 offset:16640\n"
"v_exp_f32 v99, v99\n"
"v_cvt_pk_bf16_f32 v128, v96, v97\n"
"v_fmamk_f32 v101, v101, 0x3db8aa3b, v19\n"
"v_mfma_f32_32x32x16_bf16 v[112:127], a[220:223], v[60:63], v[112:127]\n"
"ds_read_b128 a[204:207], v12 offset:16640\n"
"v_exp_f32 v100, v100\n"
"v_fmamk_f32 v102, v102, 0x3db8aa3b, v19\n"
"v_exp_f32 v101, v101\n"
"s_waitcnt lgkmcnt(2)\n"
"v_mfma_f32_32x32x16_bf16 v[112:127], a[192:195], v[64:67], v[112:127]\n"
"buffer_load_dwordx4 v2, s[44:47], s69 offen offset:1024 lds\n"
"ds_read_b128 a[208:211], v13 offset:16640\n"
"v_cvt_pk_bf16_f32 v129, v98, v99\n"
"v_fmamk_f32 v103, v103, 0x3db8aa3b, v19\n"
"v_exp_f32 v102, v102\n"
"v_fmamk_f32 v104, v104, 0x3db8aa3b, v19\n"
"v_mfma_f32_32x32x16_bf16 v[112:127], a[196:199], v[68:71], v[112:127]\n"
"ds_read_b128 a[212:215], v14 offset:16640\n"
"v_exp_f32 v103, v103\n"
"v_cvt_pk_bf16_f32 v130, v100, v101\n"
"v_fmamk_f32 v105, v105, 0x3db8aa3b, v19\n"
"s_waitcnt lgkmcnt(2)\n"
"v_mfma_f32_32x32x16_bf16 v[112:127], a[200:203], v[72:75], v[112:127]\n"
"buffer_load_dwordx4 v3, s[44:47], s69 offen offset:2048 lds\n"
"ds_read_b128 a[216:219], v15 offset:16640\n"
"v_exp_f32 v104, v104\n"
"v_fmamk_f32 v106, v106, 0x3db8aa3b, v19\n"
"v_exp_f32 v105, v105\n"
"s_mov_b32 s74, 16384\n"
"buffer_store_dwordx4 a[248:251], v23, s[64:67], s74 offen offset:0 sc1\n"
"v_mfma_f32_32x32x16_bf16 v[112:127], a[204:207], v[76:79], v[112:127]\n"
"ds_read_b128 a[220:223], v16 offset:16640\n"
"v_cvt_pk_bf16_f32 v131, v102, v103\n"
"v_fmamk_f32 v107, v107, 0x3db8aa3b, v19\n"
"v_exp_f32 v106, v106\n"
"s_waitcnt lgkmcnt(2)\n"
"v_mfma_f32_32x32x16_bf16 v[112:127], a[208:211], v[80:83], v[112:127]\n"
"s_waitcnt vmcnt(21)\n"
"s_barrier\n"
"buffer_load_dwordx4 v4, s[44:47], s69 offen offset:3072 lds\n"
"ds_read_b128 a[192:195], v9 offset:32768\n"
"v_fmamk_f32 v108, v108, 0x3db8aa3b, v19\n"
"v_exp_f32 v107, v107\n"
"v_cvt_pk_bf16_f32 v132, v104, v105\n"
"v_fmamk_f32 v109, v109, 0x3db8aa3b, v19\n"
"v_mfma_f32_32x32x16_bf16 v[112:127], a[212:215], v[84:87], v[112:127]\n"
"ds_read_b128 a[196:199], v10 offset:32768\n"
"v_exp_f32 v108, v108\n"
"v_fmamk_f32 v110, v110, 0x3db8aa3b, v19\n"
"v_exp_f32 v109, v109\n"
"s_waitcnt lgkmcnt(2)\n"
"v_mfma_f32_32x32x16_bf16 v[112:127], a[216:219], v[88:91], v[112:127]\n"
"ds_read_b128 a[200:203], v11 offset:32768\n"
"v_cvt_pk_bf16_f32 v133, v106, v107\n"
"v_fmamk_f32 v111, v111, 0x3db8aa3b, v19\n"
"v_exp_f32 v110, v110\n"
"v_mfma_f32_32x32x16_bf16 v[112:127], a[220:223], v[92:95], v[112:127]\n"
"ds_read_b128 a[204:207], v12 offset:32768\n"
"v_exp_f32 v111, v111\n"
"v_cvt_pk_bf16_f32 v134, v108, v109\n"
"v_cvt_pk_bf16_f32 v135, v110, v111\n"
"s_waitcnt lgkmcnt(2)\n"
"v_mfma_f32_32x32x16_bf16 v[96:111], a[192:195], v[32:35], 0\n"
"s_add_u32 m0, s68, 65536\n"
"s_mov_b32 s69, 131072\n"
"buffer_load_dwordx4 v1, s[44:47], s69 offen offset:0 lds\n"
"ds_read_b128 a[208:211], v13 offset:32768\n"
"v_mfma_f32_32x32x16_bf16 v[96:111], a[196:199], v[36:39], v[96:111]\n"
"ds_read_b128 a[212:215], v14 offset:32768\n"
"v_max3_f32 v18, v112, v113, v18\n"
"v_max3_f32 v18, v114, v115, v18\n"
"v_max3_f32 v18, v116, v117, v18\n"
"v_max3_f32 v18, v118, v119, v18\n"
"s_waitcnt lgkmcnt(2)\n"
"v_mfma_f32_32x32x16_bf16 v[96:111], a[200:203], v[40:43], v[96:111]\n"
"ds_read_b128 a[216:219], v15 offset:32768\n"
"v_max3_f32 v18, v120, v121, v18\n"
"v_max3_f32 v18, v122, v123, v18\n"
"v_max3_f32 v18, v124, v125, v18\n"
"v_max3_f32 v18, v126, v127, v18\n"
"v_mul_f32 v19, s73, v18\n"
"v_mfma_f32_32x32x16_bf16 v[96:111], a[204:207], v[44:47], v[96:111]\n"
"ds_read_b128 a[220:223], v16 offset:32768\n"
"v_floor_f32 v19, v19\n"
"v_accvgpr_write_b32 a49, v19\n"
"v_fmamk_f32 v112, v112, 0x3db8aa3b, v19\n"
"v_fmamk_f32 v113, v113, 0x3db8aa3b, v19\n"
"s_waitcnt lgkmcnt(2)\n"
"v_mfma_f32_32x32x16_bf16 v[96:111], a[208:211], v[48:51], v[96:111]\n"
"buffer_load_dwordx4 v2, s[44:47], s69 offen offset:1024 lds\n"
"ds_read_b128 a[192:195], v9 offset:33024\n"
"v_exp_f32 v112, v112\n"
"v_fmamk_f32 v114, v114, 0x3db8aa3b, v19\n"
"v_exp_f32 v113, v113\n"
"v_mfma_f32_32x32x16_bf16 v[96:111], a[212:215], v[52:55], v[96:111]\n"
"ds_read_b128 a[196:199], v10 offset:33024\n"
"v_fmamk_f32 v115, v115, 0x3db8aa3b, v19\n"
"v_exp_f32 v114, v114\n"
"v_fmamk_f32 v116, v116, 0x3db8aa3b, v19\n"
"s_waitcnt lgkmcnt(2)\n"
"v_mfma_f32_32x32x16_bf16 v[96:111], a[216:219], v[56:59], v[96:111]\n"
"ds_read_b128 a[200:203], v11 offset:33024\n"
"v_exp_f32 v115, v115\n"
"v_cvt_pk_bf16_f32 v136, v112, v113\n"
"v_fmamk_f32 v117, v117, 0x3db8aa3b, v19\n"
"v_mfma_f32_32x32x16_bf16 v[96:111], a[220:223], v[60:63], v[96:111]\n"
"ds_read_b128 a[204:207], v12 offset:33024\n"
"v_exp_f32 v116, v116\n"
"v_fmamk_f32 v118, v118, 0x3db8aa3b, v19\n"
"v_exp_f32 v117, v117\n"
"s_waitcnt lgkmcnt(2)\n"
"v_mfma_f32_32x32x16_bf16 v[96:111], a[192:195], v[64:67], v[96:111]\n"
"buffer_load_dwordx4 v3, s[44:47], s69 offen offset:2048 lds\n"
"ds_read_b128 a[208:211], v13 offset:33024\n"
"v_cvt_pk_bf16_f32 v137, v114, v115\n"
"v_fmamk_f32 v119, v119, 0x3db8aa3b, v19\n"
"v_exp_f32 v118, v118\n"
"v_fmamk_f32 v120, v120, 0x3db8aa3b, v19\n"
"v_mfma_f32_32x32x16_bf16 v[96:111], a[196:199], v[68:71], v[96:111]\n"
"ds_read_b128 a[212:215], v14 offset:33024\n"
"v_exp_f32 v119, v119\n"
"v_cvt_pk_bf16_f32 v138, v116, v117\n"
"v_fmamk_f32 v121, v121, 0x3db8aa3b, v19\n"
"s_waitcnt lgkmcnt(2)\n"
"v_mfma_f32_32x32x16_bf16 v[96:111], a[200:203], v[72:75], v[96:111]\n"
"ds_read_b128 a[216:219], v15 offset:33024\n"
"v_exp_f32 v120, v120\n"
"v_fmamk_f32 v122, v122, 0x3db8aa3b, v19\n"
"v_exp_f32 v121, v121\n"
"v_mfma_f32_32x32x16_bf16 v[96:111], a[204:207], v[76:79], v[96:111]\n"
"ds_read_b128 a[220:223], v16 offset:33024\n"
"v_cvt_pk_bf16_f32 v139, v118, v119\n"
"v_fmamk_f32 v123, v123, 0x3db8aa3b, v19\n"
"v_exp_f32 v122, v122\n"
"s_waitcnt lgkmcnt(2)\n"
"v_mfma_f32_32x32x16_bf16 v[96:111], a[208:211], v[80:83], v[96:111]\n"
"s_waitcnt vmcnt(21)\n"
"s_barrier\n"
"buffer_load_dwordx4 v4, s[44:47], s69 offen offset:3072 lds\n"
"ds_read_b128 a[192:195], v9 offset:49152\n"
"v_fmamk_f32 v124, v124, 0x3db8aa3b, v19\n"
"v_exp_f32 v123, v123\n"
"v_cvt_pk_bf16_f32 v140, v120, v121\n"
"v_fmamk_f32 v125, v125, 0x3db8aa3b, v19\n"
"v_mfma_f32_32x32x16_bf16 v[96:111], a[212:215], v[84:87], v[96:111]\n"
"ds_read_b128 a[196:199], v10 offset:49152\n"
"v_exp_f32 v124, v124\n"
"v_fmamk_f32 v126, v126, 0x3db8aa3b, v19\n"
"v_exp_f32 v125, v125\n"
"s_waitcnt lgkmcnt(2)\n"
"v_mfma_f32_32x32x16_bf16 v[96:111], a[216:219], v[88:91], v[96:111]\n"
"ds_read_b128 a[200:203], v11 offset:49152\n"
"v_cvt_pk_bf16_f32 v141, v122, v123\n"
"v_fmamk_f32 v127, v127, 0x3db8aa3b, v19\n"
"v_exp_f32 v126, v126\n"
"v_mfma_f32_32x32x16_bf16 v[96:111], a[220:223], v[92:95], v[96:111]\n"
"ds_read_b128 a[204:207], v12 offset:49152\n"
"v_exp_f32 v127, v127\n"
"v_cvt_pk_bf16_f32 v142, v124, v125\n"
"v_cvt_pk_bf16_f32 v143, v126, v127\n"
"s_waitcnt lgkmcnt(2)\n"
"v_mfma_f32_32x32x16_bf16 v[112:127], a[192:195], v[32:35], 0\n"
"s_add_u32 m0, s68, 81920\n"
"s_mov_b32 s69, 147456\n"
"buffer_load_dwordx4 v1, s[44:47], s69 offen offset:0 lds\n"
"ds_read_b128 a[208:211], v13 offset:49152\n"
"v_mfma_f32_32x32x16_bf16 v[112:127], a[196:199], v[36:39], v[112:127]\n"
"ds_read_b128 a[212:215], v14 offset:49152\n"
"v_max3_f32 v18, v96, v97, v18\n"
"v_max3_f32 v18, v98, v99, v18\n"
"v_max3_f32 v18, v100, v101, v18\n"
"v_max3_f32 v18, v102, v103, v18\n"
"s_waitcnt lgkmcnt(2)\n"
"v_mfma_f32_32x32x16_bf16 v[112:127], a[200:203], v[40:43], v[112:127]\n"
"ds_read_b128 a[216:219], v15 offset:49152\n"
"v_max3_f32 v18, v104, v105, v18\n"
"v_max3_f32 v18, v106, v107, v18\n"
"v_max3_f32 v18, v108, v109, v18\n"
"v_max3_f32 v18, v110, v111, v18\n"
"v_mul_f32 v19, s73, v18\n"
"v_mfma_f32_32x32x16_bf16 v[112:127], a[204:207], v[44:47], v[112:127]\n"
"ds_read_b128 a[220:223], v16 offset:49152\n"
"v_floor_f32 v19, v19\n"
"v_accvgpr_write_b32 a50, v19\n"
"v_fmamk_f32 v96, v96, 0x3db8aa3b, v19\n"
"v_fmamk_f32 v97, v97, 0x3db8aa3b, v19\n"
"s_waitcnt lgkmcnt(2)\n"
"v_mfma_f32_32x32x16_bf16 v[112:127], a[208:211], v[48:51], v[112:127]\n"
"buffer_load_dwordx4 v2, s[44:47], s69 offen offset:1024 lds\n"
"ds_read_b128 a[192:195], v9 offset:49408\n"
"v_exp_f32 v96, v96\n"
"v_fmamk_f32 v98, v98, 0x3db8aa3b, v19\n"
"v_exp_f32 v97, v97\n"
"v_mfma_f32_32x32x16_bf16 v[112:127], a[212:215], v[52:55], v[112:127]\n"
"ds_read_b128 a[196:199], v10 offset:49408\n"
"v_fmamk_f32 v99, v99, 0x3db8aa3b, v19\n"
"v_exp_f32 v98, v98\n"
"v_fmamk_f32 v100, v100, 0x3db8aa3b, v19\n"
"s_waitcnt lgkmcnt(2)\n"
"v_mfma_f32_32x32x16_bf16 v[112:127], a[216:219], v[56:59], v[112:127]\n"
"ds_read_b128 a[200:203], v11 offset:49408\n"
"v_exp_f32 v99, v99\n"
"v_cvt_pk_bf16_f32 v144, v96, v97\n"
"v_fmamk_f32 v101, v101, 0x3db8aa3b, v19\n"
"v_mfma_f32_32x32x16_bf16 v[112:127], a[220:223], v[60:63], v[112:127]\n"
"ds_read_b128 a[204:207], v12 offset:49408\n"
"v_exp_f32 v100, v100\n"
"v_fmamk_f32 v102, v102, 0x3db8aa3b, v19\n"
"v_exp_f32 v101, v101\n"
"s_waitcnt lgkmcnt(2)\n"
"v_mfma_f32_32x32x16_bf16 v[112:127], a[192:195], v[64:67], v[112:127]\n"
"buffer_load_dwordx4 v3, s[44:47], s69 offen offset:2048 lds\n"
"ds_read_b128 a[208:211], v13 offset:49408\n"
"v_cvt_pk_bf16_f32 v145, v98, v99\n"
"v_fmamk_f32 v103, v103, 0x3db8aa3b, v19\n"
"v_exp_f32 v102, v102\n"
"v_fmamk_f32 v104, v104, 0x3db8aa3b, v19\n"
"v_mfma_f32_32x32x16_bf16 v[112:127], a[196:199], v[68:71], v[112:127]\n"
"ds_read_b128 a[212:215], v14 offset:49408\n"
"v_exp_f32 v103, v103\n"
"v_cvt_pk_bf16_f32 v146, v100, v101\n"
"v_fmamk_f32 v105, v105, 0x3db8aa3b, v19\n"
"s_waitcnt lgkmcnt(2)\n"
"v_mfma_f32_32x32x16_bf16 v[112:127], a[200:203], v[72:75], v[112:127]\n"
"ds_read_b128 a[216:219], v15 offset:49408\n"
"v_exp_f32 v104, v104\n"
"v_fmamk_f32 v106, v106, 0x3db8aa3b, v19\n"
"v_exp_f32 v105, v105\n"
"v_mfma_f32_32x32x16_bf16 v[112:127], a[204:207], v[76:79], v[112:127]\n"
"ds_read_b128 a[220:223], v16 offset:49408\n"
"v_cvt_pk_bf16_f32 v147, v102, v103\n"
"v_fmamk_f32 v107, v107, 0x3db8aa3b, v19\n"
"v_exp_f32 v106, v106\n"
"s_waitcnt lgkmcnt(2)\n"
"v_mfma_f32_32x32x16_bf16 v[112:127], a[208:211], v[80:83], v[112:127]\n"
"s_waitcnt vmcnt(21)\n"
"s_barrier\n"
"buffer_load_dwordx4 v4, s[44:47], s69 offen offset:3072 lds\n"
"v_xor_b32 v9, 0x10000, v9\n"
"ds_read_b128 a[192:195], v9 offset:0\n"
"v_fmamk_f32 v108, v108, 0x3db8aa3b, v19\n"
"v_exp_f32 v107, v107\n"
"v_cvt_pk_bf16_f32 v148, v104, v105\n"
"v_fmamk_f32 v109, v109, 0x3db8aa3b, v19\n"
"s_mov_b32 s74, 16384\n"
"buffer_store_dwordx4 a[248:251], v23, s[64:67], s74 offen offset:1024 sc1\n"
"v_mfma_f32_32x32x16_bf16 v[112:127], a[212:215], v[84:87], v[112:127]\n"
"v_xor_b32 v10, 0x10000, v10\n"
"ds_read_b128 a[196:199], v10 offset:0\n"
"v_exp_f32 v108, v108\n"
"v_fmamk_f32 v110, v110, 0x3db8aa3b, v19\n"
"v_exp_f32 v109, v109\n"
"s_waitcnt lgkmcnt(2)\n"
"v_mfma_f32_32x32x16_bf16 v[112:127], a[216:219], v[88:91], v[112:127]\n"
"v_xor_b32 v11, 0x10000, v11\n"
"ds_read_b128 a[200:203], v11 offset:0\n"
"v_cvt_pk_bf16_f32 v149, v106, v107\n"
"v_fmamk_f32 v111, v111, 0x3db8aa3b, v19\n"
"v_exp_f32 v110, v110\n"
"v_mfma_f32_32x32x16_bf16 v[112:127], a[220:223], v[92:95], v[112:127]\n"
"v_xor_b32 v12, 0x10000, v12\n"
"ds_read_b128 a[204:207], v12 offset:0\n"
"v_exp_f32 v111, v111\n"
"v_cvt_pk_bf16_f32 v150, v108, v109\n"
"v_cvt_pk_bf16_f32 v151, v110, v111\n"
"s_waitcnt lgkmcnt(2)\n"
"v_mfma_f32_32x32x16_bf16 v[96:111], a[192:195], v[32:35], 0\n"
"s_add_u32 m0, s68, 98304\n"
"s_mov_b32 s69, 163840\n"
"buffer_load_dwordx4 v1, s[44:47], s69 offen offset:0 lds\n"
"v_xor_b32 v13, 0x10000, v13\n"
"ds_read_b128 a[208:211], v13 offset:0\n"
"v_mfma_f32_32x32x16_bf16 v[96:111], a[196:199], v[36:39], v[96:111]\n"
"v_xor_b32 v14, 0x10000, v14\n"
"ds_read_b128 a[212:215], v14 offset:0\n"
"v_max3_f32 v18, v112, v113, v18\n"
"v_max3_f32 v18, v114, v115, v18\n"
"v_max3_f32 v18, v116, v117, v18\n"
"v_max3_f32 v18, v118, v119, v18\n"
"s_waitcnt lgkmcnt(2)\n"
"v_mfma_f32_32x32x16_bf16 v[96:111], a[200:203], v[40:43], v[96:111]\n"
"v_xor_b32 v15, 0x10000, v15\n"
"ds_read_b128 a[216:219], v15 offset:0\n"
"v_max3_f32 v18, v120, v121, v18\n"
"v_max3_f32 v18, v122, v123, v18\n"
"v_max3_f32 v18, v124, v125, v18\n"
"v_max3_f32 v18, v126, v127, v18\n"
"v_mul_f32 v19, s73, v18\n"
"v_mfma_f32_32x32x16_bf16 v[96:111], a[204:207], v[44:47], v[96:111]\n"
"v_xor_b32 v16, 0x10000, v16\n"
"ds_read_b128 a[220:223], v16 offset:0\n"
"v_floor_f32 v19, v19\n"
"v_accvgpr_write_b32 a51, v19\n"
"v_fmamk_f32 v112, v112, 0x3db8aa3b, v19\n"
"v_fmamk_f32 v113, v113, 0x3db8aa3b, v19\n"
"s_waitcnt lgkmcnt(2)\n"
"v_mfma_f32_32x32x16_bf16 v[96:111], a[208:211], v[48:51], v[96:111]\n"
"buffer_load_dwordx4 v2, s[44:47], s69 offen offset:1024 lds\n"
"ds_read_b128 a[192:195], v9 offset:256\n"
"v_exp_f32 v112, v112\n"
"v_fmamk_f32 v114, v114, 0x3db8aa3b, v19\n"
"v_exp_f32 v113, v113\n"
"v_mfma_f32_32x32x16_bf16 v[96:111], a[212:215], v[52:55], v[96:111]\n"
"ds_read_b128 a[196:199], v10 offset:256\n"
"v_fmamk_f32 v115, v115, 0x3db8aa3b, v19\n"
"v_exp_f32 v114, v114\n"
"v_fmamk_f32 v116, v116, 0x3db8aa3b, v19\n"
"s_waitcnt lgkmcnt(2)\n"
"v_mfma_f32_32x32x16_bf16 v[96:111], a[216:219], v[56:59], v[96:111]\n"
"ds_read_b128 a[200:203], v11 offset:256\n"
"v_exp_f32 v115, v115\n"
"v_cvt_pk_bf16_f32 v152, v112, v113\n"
"v_fmamk_f32 v117, v117, 0x3db8aa3b, v19\n"
"v_mfma_f32_32x32x16_bf16 v[96:111], a[220:223], v[60:63], v[96:111]\n"
"ds_read_b128 a[204:207], v12 offset:256\n"
"v_exp_f32 v116, v116\n"
"v_fmamk_f32 v118, v118, 0x3db8aa3b, v19\n"
"v_exp_f32 v117, v117\n"
"s_waitcnt lgkmcnt(2)\n"
"v_mfma_f32_32x32x16_bf16 v[96:111], a[192:195], v[64:67], v[96:111]\n"
"buffer_load_dwordx4 v3, s[44:47], s69 offen offset:2048 lds\n"
"ds_read_b128 a[208:211], v13 offset:256\n"
"v_cvt_pk_bf16_f32 v153, v114, v115\n"
"v_fmamk_f32 v119, v119, 0x3db8aa3b, v19\n"
"v_exp_f32 v118, v118\n"
"v_fmamk_f32 v120, v120, 0x3db8aa3b, v19\n"
"v_mfma_f32_32x32x16_bf16 v[96:111], a[196:199], v[68:71], v[96:111]\n"
"ds_read_b128 a[212:215], v14 offset:256\n"
"v_exp_f32 v119, v119\n"
"v_cvt_pk_bf16_f32 v154, v116, v117\n"
"v_fmamk_f32 v121, v121, 0x3db8aa3b, v19\n"
"s_waitcnt lgkmcnt(2)\n"
"v_mfma_f32_32x32x16_bf16 v[96:111], a[200:203], v[72:75], v[96:111]\n"
"ds_read_b128 a[216:219], v15 offset:256\n"
"v_exp_f32 v120, v120\n"
"v_fmamk_f32 v122, v122, 0x3db8aa3b, v19\n"
"v_exp_f32 v121, v121\n"
"v_mfma_f32_32x32x16_bf16 v[96:111], a[204:207], v[76:79], v[96:111]\n"
"ds_read_b128 a[220:223], v16 offset:256\n"
"v_cvt_pk_bf16_f32 v155, v118, v119\n"
"v_fmamk_f32 v123, v123, 0x3db8aa3b, v19\n"
"v_exp_f32 v122, v122\n"
"s_waitcnt lgkmcnt(2)\n"
"v_mfma_f32_32x32x16_bf16 v[96:111], a[208:211], v[80:83], v[96:111]\n"
"s_waitcnt vmcnt(22)\n"
"s_barrier\n"
"buffer_load_dwordx4 v4, s[44:47], s69 offen offset:3072 lds\n"
"ds_read_b128 a[192:195], v9 offset:16384\n"
"v_fmamk_f32 v124, v124, 0x3db8aa3b, v19\n"
"v_exp_f32 v123, v123\n"
"v_cvt_pk_bf16_f32 v156, v120, v121\n"
"v_fmamk_f32 v125, v125, 0x3db8aa3b, v19\n"
"v_mfma_f32_32x32x16_bf16 v[96:111], a[212:215], v[84:87], v[96:111]\n"
"ds_read_b128 a[196:199], v10 offset:16384\n"
"v_exp_f32 v124, v124\n"
"v_fmamk_f32 v126, v126, 0x3db8aa3b, v19\n"
"v_exp_f32 v125, v125\n"
"s_waitcnt lgkmcnt(2)\n"
"v_mfma_f32_32x32x16_bf16 v[96:111], a[216:219], v[88:91], v[96:111]\n"
"ds_read_b128 a[200:203], v11 offset:16384\n"
"v_cvt_pk_bf16_f32 v157, v122, v123\n"
"v_fmamk_f32 v127, v127, 0x3db8aa3b, v19\n"
"v_exp_f32 v126, v126\n"
"v_mfma_f32_32x32x16_bf16 v[96:111], a[220:223], v[92:95], v[96:111]\n"
"ds_read_b128 a[204:207], v12 offset:16384\n"
"v_exp_f32 v127, v127\n"
"v_cvt_pk_bf16_f32 v158, v124, v125\n"
"v_cvt_pk_bf16_f32 v159, v126, v127\n"
"s_waitcnt lgkmcnt(2)\n"
"v_mfma_f32_32x32x16_bf16 v[112:127], a[192:195], v[32:35], 0\n"
"s_add_u32 m0, s68, 114688\n"
"s_mov_b32 s69, 180224\n"
"buffer_load_dwordx4 v1, s[44:47], s69 offen offset:0 lds\n"
"ds_read_b128 a[208:211], v13 offset:16384\n"
"v_mfma_f32_32x32x16_bf16 v[112:127], a[196:199], v[36:39], v[112:127]\n"
"ds_read_b128 a[212:215], v14 offset:16384\n"
"v_max3_f32 v18, v96, v97, v18\n"
"v_max3_f32 v18, v98, v99, v18\n"
"v_max3_f32 v18, v100, v101, v18\n"
"v_max3_f32 v18, v102, v103, v18\n"
"s_waitcnt lgkmcnt(2)\n"
"v_mfma_f32_32x32x16_bf16 v[112:127], a[200:203], v[40:43], v[112:127]\n"
"ds_read_b128 a[216:219], v15 offset:16384\n"
"v_max3_f32 v18, v104, v105, v18\n"
"v_max3_f32 v18, v106, v107, v18\n"
"v_max3_f32 v18, v108, v109, v18\n"
"v_max3_f32 v18, v110, v111, v18\n"
"v_mul_f32 v19, s73, v18\n"
"v_mfma_f32_32x32x16_bf16 v[112:127], a[204:207], v[44:47], v[112:127]\n"
"ds_read_b128 a[220:223], v16 offset:16384\n"
"v_floor_f32 v19, v19\n"
"v_accvgpr_write_b32 a52, v19\n"
"v_fmamk_f32 v96, v96, 0x3db8aa3b, v19\n"
"v_fmamk_f32 v97, v97, 0x3db8aa3b, v19\n"
"s_lshl_b32 s75, %[T], 2\n"
"s_add_u32 s75, s75, s71\n"
"s_mul_i32 s75, s75, 28\n"
"v_min_u32 v30, 27, v0\n"
"v_add_u32 v30, s75, v30\n"
"v_lshrrev_b32 v31, 0, v30\n"
"v_mul_u32_u24 v31, 0, v31\n"
"v_lshl_add_u32 v30, v30, 7, v31\n"
"buffer_load_dword a227, v30, s[52:55], 0 offen\n"
"s_waitcnt lgkmcnt(2)\n"
"v_mfma_f32_32x32x16_bf16 v[112:127], a[208:211], v[48:51], v[112:127]\n"
"buffer_load_dwordx4 v2, s[44:47], s69 offen offset:1024 lds\n"
"ds_read_b128 a[192:195], v9 offset:16640\n"
"v_exp_f32 v96, v96\n"
"v_fmamk_f32 v98, v98, 0x3db8aa3b, v19\n"
"v_exp_f32 v97, v97\n"
"v_mfma_f32_32x32x16_bf16 v[112:127], a[212:215], v[52:55], v[112:127]\n"
"ds_read_b128 a[196:199], v10 offset:16640\n"
"v_fmamk_f32 v99, v99, 0x3db8aa3b, v19\n"
"v_exp_f32 v98, v98\n"
"v_fmamk_f32 v100, v100, 0x3db8aa3b, v19\n"
"s_waitcnt lgkmcnt(2)\n"
"v_mfma_f32_32x32x16_bf16 v[112:127], a[216:219], v[56:59], v[112:127]\n"
"ds_read_b128 a[200:203], v11 offset:16640\n"
"v_exp_f32 v99, v99\n"
"v_cvt_pk_bf16_f32 v160, v96, v97\n"
"v_fmamk_f32 v101, v101, 0x3db8aa3b, v19\n"
"v_mfma_f32_32x32x16_bf16 v[112:127], a[220:223], v[60:63], v[112:127]\n"
"ds_read_b128 a[204:207], v12 offset:16640\n"
"v_exp_f32 v100, v100\n"
"v_fmamk_f32 v102, v102, 0x3db8aa3b, v19\n"
"v_exp_f32 v101, v101\n"
"s_waitcnt lgkmcnt(2)\n"
"v_mfma_f32_32x32x16_bf16 v[112:127], a[192:195], v[64:67], v[112:127]\n"
"buffer_load_dwordx4 v3, s[44:47], s69 offen offset:2048 lds\n"
"ds_read_b128 a[208:211], v13 offset:16640\n"
"v_cvt_pk_bf16_f32 v161, v98, v99\n"
"v_fmamk_f32 v103, v103, 0x3db8aa3b, v19\n"
"v_exp_f32 v102, v102\n"
"v_fmamk_f32 v104, v104, 0x3db8aa3b, v19\n"
"v_mfma_f32_32x32x16_bf16 v[112:127], a[196:199], v[68:71], v[112:127]\n"
"ds_read_b128 a[212:215], v14 offset:16640\n"
"v_exp_f32 v103, v103\n"
"v_cvt_pk_bf16_f32 v162, v100, v101\n"
"v_fmamk_f32 v105, v105, 0x3db8aa3b, v19\n"
"s_waitcnt lgkmcnt(2)\n"
"v_mfma_f32_32x32x16_bf16 v[112:127], a[200:203], v[72:75], v[112:127]\n"
"ds_read_b128 a[216:219], v15 offset:16640\n"
"v_exp_f32 v104, v104\n"
"v_fmamk_f32 v106, v106, 0x3db8aa3b, v19\n"
"v_exp_f32 v105, v105\n"
"v_mfma_f32_32x32x16_bf16 v[112:127], a[204:207], v[76:79], v[112:127]\n"
"ds_read_b128 a[220:223], v16 offset:16640\n"
"v_cvt_pk_bf16_f32 v163, v102, v103\n"
"v_fmamk_f32 v107, v107, 0x3db8aa3b, v19\n"
"v_exp_f32 v106, v106\n"
"s_waitcnt lgkmcnt(2)\n"
"v_mfma_f32_32x32x16_bf16 v[112:127], a[208:211], v[80:83], v[112:127]\n"
"s_waitcnt vmcnt(22)\n"
"s_barrier\n"
"buffer_load_dwordx4 v4, s[44:47], s69 offen offset:3072 lds\n"
"ds_read_b128 a[192:195], v9 offset:32768\n"
"v_fmamk_f32 v108, v108, 0x3db8aa3b, v19\n"
"v_exp_f32 v107, v107\n"
"v_cvt_pk_bf16_f32 v164, v104, v105\n"
"v_fmamk_f32 v109, v109, 0x3db8aa3b, v19\n"
"v_mfma_f32_32x32x16_bf16 v[112:127], a[212:215], v[84:87], v[112:127]\n"
"ds_read_b128 a[196:199], v10 offset:32768\n"
"v_exp_f32 v108, v108\n"
"v_fmamk_f32 v110, v110, 0x3db8aa3b, v19\n"
"v_exp_f32 v109, v109\n"
"s_waitcnt lgkmcnt(2)\n"
"v_mfma_f32_32x32x16_bf16 v[112:127], a[216:219], v[88:91], v[112:127]\n"
"ds_read_b128 a[200:203], v11 offset:32768\n"
"v_cvt_pk_bf16_f32 v165, v106, v107\n"
"v_fmamk_f32 v111, v111, 0x3db8aa3b, v19\n"
"v_exp_f32 v110, v110\n"
"s_mov_b32 s74, 20480\n"
"buffer_store_dwordx4 a[248:251], v23, s[64:67], s74 offen offset:0 sc1\n"
"v_mfma_f32_32x32x16_bf16 v[112:127], a[220:223], v[92:95], v[112:127]\n"
"ds_read_b128 a[204:207], v12 offset:32768\n"
"v_exp_f32 v111, v111\n"
"v_cvt_pk_bf16_f32 v166, v108, v109\n"
"v_cvt_pk_bf16_f32 v167, v110, v111\n"
"s_waitcnt lgkmcnt(2)\n"
"v_mfma_f32_32x32x16_bf16 v[96:111], a[192:195], v[32:35], 0\n"
"s_add_u32 m0, s68, 0\n"
"s_mov_b32 s69, 196608\n"
"buffer_load_dwordx4 v1, s[44:47], s69 offen offset:0 lds\n"
"ds_read_b128 a[208:211], v13 offset:32768\n"
"v_mfma_f32_32x32x16_bf16 v[96:111], a[196:199], v[36:39], v[96:111]\n"
"ds_read_b128 a[212:215], v14 offset:32768\n"
"v_max3_f32 v18, v112, v113, v18\n"
"v_max3_f32 v18, v114, v115, v18\n"
"v_max3_f32 v18, v116, v117, v18\n"
"v_max3_f32 v18, v118, v119, v18\n"
"s_waitcnt lgkmcnt(2)\n"
"v_mfma_f32_32x32x16_bf16 v[96:111], a[200:203], v[40:43], v[96:111]\n"
"ds_read_b128 a[216:219], v15 offset:32768\n"
"v_max3_f32 v18, v120, v121, v18\n"
"v_max3_f32 v18, v122, v123, v18\n"
"v_max3_f32 v18, v124, v125, v18\n"
"v_max3_f32 v18, v126, v127, v18\n"
"v_mul_f32 v19, s73, v18\n"
"v_mfma_f32_32x32x16_bf16 v[96:111], a[204:207], v[44:47], v[96:111]\n"
"ds_read_b128 a[220:223], v16 offset:32768\n"
"v_floor_f32 v19, v19\n"
"v_accvgpr_write_b32 a53, v19\n"
"v_fmamk_f32 v112, v112, 0x3db8aa3b, v19\n"
"v_fmamk_f32 v113, v113, 0x3db8aa3b, v19\n"
"s_waitcnt lgkmcnt(2)\n"
"v_mfma_f32_32x32x16_bf16 v[96:111], a[208:211], v[48:51], v[96:111]\n"
"buffer_load_dwordx4 v2, s[44:47], s69 offen offset:1024 lds\n"
"ds_read_b128 a[192:195], v9 offset:33024\n"
"v_exp_f32 v112, v112\n"
"v_fmamk_f32 v114, v114, 0x3db8aa3b, v19\n"
"v_exp_f32 v113, v113\n"
"v_mfma_f32_32x32x16_bf16 v[96:111], a[212:215], v[52:55], v[96:111]\n"
"ds_read_b128 a[196:199], v10 offset:33024\n"
"v_fmamk_f32 v115, v115, 0x3db8aa3b, v19\n"
"v_exp_f32 v114, v114\n"
"v_fmamk_f32 v116, v116, 0x3db8aa3b, v19\n"
"s_waitcnt lgkmcnt(2)\n"
"v_mfma_f32_32x32x16_bf16 v[96:111], a[216:219], v[56:59], v[96:111]\n"
"ds_read_b128 a[200:203], v11 offset:33024\n"
"v_exp_f32 v115, v115\n"
"v_cvt_pk_bf16_f32 v168, v112, v113\n"
"v_fmamk_f32 v117, v117, 0x3db8aa3b, v19\n"
"v_mfma_f32_32x32x16_bf16 v[96:111], a[220:223], v[60:63], v[96:111]\n"
"ds_read_b128 a[204:207], v12 offset:33024\n"
"v_exp_f32 v116, v116\n"
"v_fmamk_f32 v118, v118, 0x3db8aa3b, v19\n"
"v_exp_f32 v117, v117\n"
"s_waitcnt lgkmcnt(2)\n"
"v_mfma_f32_32x32x16_bf16 v[96:111], a[192:195], v[64:67], v[96:111]\n"
"buffer_load_dwordx4 v3, s[44:47], s69 offen offset:2048 lds\n"
"ds_read_b128 a[208:211], v13 offset:33024\n"
"v_cvt_pk_bf16_f32 v169, v114, v115\n"
"v_fmamk_f32 v119, v119, 0x3db8aa3b, v19\n"
"v_exp_f32 v118, v118\n"
"v_fmamk_f32 v120, v120, 0x3db8aa3b, v19\n"
"v_mfma_f32_32x32x16_bf16 v[96:111], a[196:199], v[68:71], v[96:111]\n"
"ds_read_b128 a[212:215], v14 offset:33024\n"
"v_exp_f32 v119, v119\n"
"v_cvt_pk_bf16_f32 v170, v116, v117\n"
"v_fmamk_f32 v121, v121, 0x3db8aa3b, v19\n"
"s_waitcnt lgkmcnt(2)\n"
"v_mfma_f32_32x32x16_bf16 v[96:111], a[200:203], v[72:75], v[96:111]\n"
"ds_read_b128 a[216:219], v15 offset:33024\n"
"v_exp_f32 v120, v120\n"
"v_fmamk_f32 v122, v122, 0x3db8aa3b, v19\n"
"v_exp_f32 v121, v121\n"
"v_mfma_f32_32x32x16_bf16 v[96:111], a[204:207], v[76:79], v[96:111]\n"
"ds_read_b128 a[220:223], v16 offset:33024\n"
"v_cvt_pk_bf16_f32 v171, v118, v119\n"
"v_fmamk_f32 v123, v123, 0x3db8aa3b, v19\n"
"v_exp_f32 v122, v122\n"
"s_waitcnt lgkmcnt(2)\n"
"v_mfma_f32_32x32x16_bf16 v[96:111], a[208:211], v[80:83], v[96:111]\n"
"s_waitcnt vmcnt(22)\n"
"s_barrier\n"
"buffer_load_dwordx4 v4, s[44:47], s69 offen offset:3072 lds\n"
"ds_read_b128 a[192:195], v9 offset:49152\n"
"v_fmamk_f32 v124, v124, 0x3db8aa3b, v19\n"
"v_exp_f32 v123, v123\n"
"v_cvt_pk_bf16_f32 v172, v120, v121\n"
"v_fmamk_f32 v125, v125, 0x3db8aa3b, v19\n"
"v_mfma_f32_32x32x16_bf16 v[96:111], a[212:215], v[84:87], v[96:111]\n"
"ds_read_b128 a[196:199], v10 offset:49152\n"
"v_exp_f32 v124, v124\n"
"v_fmamk_f32 v126, v126, 0x3db8aa3b, v19\n"
"v_exp_f32 v125, v125\n"
"s_waitcnt lgkmcnt(2)\n"
"v_mfma_f32_32x32x16_bf16 v[96:111], a[216:219], v[88:91], v[96:111]\n"
"ds_read_b128 a[200:203], v11 offset:49152\n"
"v_cvt_pk_bf16_f32 v173, v122, v123\n"
"v_fmamk_f32 v127, v127, 0x3db8aa3b, v19\n"
"v_exp_f32 v126, v126\n"
"v_mfma_f32_32x32x16_bf16 v[96:111], a[220:223], v[92:95], v[96:111]\n"
"ds_read_b128 a[204:207], v12 offset:49152\n"
"v_exp_f32 v127, v127\n"
"v_cvt_pk_bf16_f32 v174, v124, v125\n"
"v_cvt_pk_bf16_f32 v175, v126, v127\n"
"s_waitcnt lgkmcnt(2)\n"
"v_mfma_f32_32x32x16_bf16 v[112:127], a[192:195], v[32:35], 0\n"
"s_add_u32 m0, s68, 16384\n"
"s_mov_b32 s69, 212992\n"
"buffer_load_dwordx4 v1, s[44:47], s69 offen offset:0 lds\n"
"ds_read_b128 a[208:211], v13 offset:49152\n"
"v_mfma_f32_32x32x16_bf16 v[112:127], a[196:199], v[36:39], v[112:127]\n"
"ds_read_b128 a[212:215], v14 offset:49152\n"
"v_max3_f32 v18, v96, v97, v18\n"
"v_max3_f32 v18, v98, v99, v18\n"
"v_max3_f32 v18, v100, v101, v18\n"
"v_max3_f32 v18, v102, v103, v18\n"
"s_waitcnt lgkmcnt(2)\n"
"v_mfma_f32_32x32x16_bf16 v[112:127], a[200:203], v[40:43], v[112:127]\n"
"ds_read_b128 a[216:219], v15 offset:49152\n"
"v_max3_f32 v18, v104, v105, v18\n"
"v_max3_f32 v18, v106, v107, v18\n"
"v_max3_f32 v18, v108, v109, v18\n"
"v_max3_f32 v18, v110, v111, v18\n"
"v_mul_f32 v19, s73, v18\n"
"v_mfma_f32_32x32x16_bf16 v[112:127], a[204:207], v[44:47], v[112:127]\n"
"ds_read_b128 a[220:223], v16 offset:49152\n"
"v_floor_f32 v19, v19\n"
"v_accvgpr_write_b32 a54, v19\n"
"v_fmamk_f32 v96, v96, 0x3db8aa3b, v19\n"
"v_fmamk_f32 v97, v97, 0x3db8aa3b, v19\n"
"s_waitcnt lgkmcnt(2)\n"
"v_mfma_f32_32x32x16_bf16 v[112:127], a[208:211], v[48:51], v[112:127]\n"
"buffer_load_dwordx4 v2, s[44:47], s69 offen offset:1024 lds\n"
"ds_read_b128 a[192:195], v9 offset:49408\n"
"v_exp_f32 v96, v96\n"
"v_fmamk_f32 v98, v98, 0x3db8aa3b, v19\n"
"v_exp_f32 v97, v97\n"
"v_mfma_f32_32x32x16_bf16 v[112:127], a[212:215], v[52:55], v[112:127]\n"
"ds_read_b128 a[196:199], v10 offset:49408\n"
"v_fmamk_f32 v99, v99, 0x3db8aa3b, v19\n"
"v_exp_f32 v98, v98\n"
"v_fmamk_f32 v100, v100, 0x3db8aa3b, v19\n"
"s_waitcnt lgkmcnt(2)\n"
"v_mfma_f32_32x32x16_bf16 v[112:127], a[216:219], v[56:59], v[112:127]\n"
"ds_read_b128 a[200:203], v11 offset:49408\n"
"v_exp_f32 v99, v99\n"
"v_cvt_pk_bf16_f32 v176, v96, v97\n"
"v_fmamk_f32 v101, v101, 0x3db8aa3b, v19\n"
"v_mfma_f32_32x32x16_bf16 v[112:127], a[220:223], v[60:63], v[112:127]\n"
"ds_read_b128 a[204:207], v12 offset:49408\n"
"v_exp_f32 v100, v100\n"
"v_fmamk_f32 v102, v102, 0x3db8aa3b, v19\n"
"v_exp_f32 v101, v101\n"
"s_waitcnt lgkmcnt(2)\n"
"v_mfma_f32_32x32x16_bf16 v[112:127], a[192:195], v[64:67], v[112:127]\n"
"buffer_load_dwordx4 v3, s[44:47], s69 offen offset:2048 lds\n"
"ds_read_b128 a[208:211], v13 offset:49408\n"
"v_cvt_pk_bf16_f32 v177, v98, v99\n"
"v_fmamk_f32 v103, v103, 0x3db8aa3b, v19\n"
"v_exp_f32 v102, v102\n"
"v_fmamk_f32 v104, v104, 0x3db8aa3b, v19\n"
"v_mfma_f32_32x32x16_bf16 v[112:127], a[196:199], v[68:71], v[112:127]\n"
"ds_read_b128 a[212:215], v14 offset:49408\n"
"v_exp_f32 v103, v103\n"
"v_cvt_pk_bf16_f32 v178, v100, v101\n"
"v_fmamk_f32 v105, v105, 0x3db8aa3b, v19\n"
"s_waitcnt lgkmcnt(2)\n"
"v_mfma_f32_32x32x16_bf16 v[112:127], a[200:203], v[72:75], v[112:127]\n"
"ds_read_b128 a[216:219], v15 offset:49408\n"
"v_exp_f32 v104, v104\n"
"v_fmamk_f32 v106, v106, 0x3db8aa3b, v19\n"
"v_exp_f32 v105, v105\n"
"v_mfma_f32_32x32x16_bf16 v[112:127], a[204:207], v[76:79], v[112:127]\n"
"ds_read_b128 a[220:223], v16 offset:49408\n"
"v_cvt_pk_bf16_f32 v179, v102, v103\n"
"v_fmamk_f32 v107, v107, 0x3db8aa3b, v19\n"
"v_exp_f32 v106, v106\n"
"s_waitcnt lgkmcnt(2)\n"
"v_mfma_f32_32x32x16_bf16 v[112:127], a[208:211], v[80:83], v[112:127]\n"
"s_waitcnt vmcnt(22)\n"
"s_barrier\n"
"buffer_load_dwordx4 v4, s[44:47], s69 offen offset:3072 lds\n"
"v_xor_b32 v9, 0x10000, v9\n"
"ds_read_b128 a[192:195], v9 offset:0\n"
"v_fmamk_f32 v108, v108, 0x3db8aa3b, v19\n"
"v_exp_f32 v107, v107\n"
"v_cvt_pk_bf16_f32 v180, v104, v105\n"
"v_fmamk_f32 v109, v109, 0x3db8aa3b, v19\n"
"v_mfma_f32_32x32x16_bf16 v[112:127], a[212:215], v[84:87], v[112:127]\n"
"v_xor_b32 v10, 0x10000, v10\n"
"ds_read_b128 a[196:199], v10 offset:0\n"
"v_exp_f32 v108, v108\n"
"v_fmamk_f32 v110, v110, 0x3db8aa3b, v19\n"
"v_exp_f32 v109, v109\n"
"s_waitcnt lgkmcnt(2)\n"
"v_mfma_f32_32x32x16_bf16 v[112:127], a[216:219], v[88:91], v[112:127]\n"
"v_xor_b32 v11, 0x10000, v11\n"
"ds_read_b128 a[200:203], v11 offset:0\n"
"v_cvt_pk_bf16_f32 v181, v106, v107\n"
"v_fmamk_f32 v111, v111, 0x3db8aa3b, v19\n"
"v_exp_f32 v110, v110\n"
"v_mfma_f32_32x32x16_bf16 v[112:127], a[220:223], v[92:95], v[112:127]\n"
"v_xor_b32 v12, 0x10000, v12\n"
"ds_read_b128 a[204:207], v12 offset:0\n"
"v_exp_f32 v111, v111\n"
"v_cvt_pk_bf16_f32 v182, v108, v109\n"
"v_cvt_pk_bf16_f32 v183, v110, v111\n"
"s_waitcnt lgkmcnt(2)\n"
"v_mfma_f32_32x32x16_bf16 v[96:111], a[192:195], v[32:35], 0\n"
"s_add_u32 m0, s68, 32768\n"
"s_mov_b32 s69, 229376\n"
"buffer_load_dwordx4 v1, s[44:47], s69 offen offset:0 lds\n"
"v_xor_b32 v13, 0x10000, v13\n"
"ds_read_b128 a[208:211], v13 offset:0\n"
"s_mov_b32 s74, 20480\n"
"buffer_store_dwordx4 a[248:251], v23, s[64:67], s74 offen offset:1024 sc1\n"
"v_mfma_f32_32x32x16_bf16 v[96:111], a[196:199], v[36:39], v[96:111]\n"
"v_xor_b32 v14, 0x10000, v14\n"
"ds_read_b128 a[212:215], v14 offset:0\n"
"v_max3_f32 v18, v112, v113, v18\n"
"v_max3_f32 v18, v114, v115, v18\n"
"v_max3_f32 v18, v116, v117, v18\n"
"v_max3_f32 v18, v118, v119, v18\n"
"s_waitcnt lgkmcnt(2)\n"
"v_mfma_f32_32x32x16_bf16 v[96:111], a[200:203], v[40:43], v[96:111]\n"
"v_xor_b32 v15, 0x10000, v15\n"
"ds_read_b128 a[216:219], v15 offset:0\n"
"v_max3_f32 v18, v120, v121, v18\n"
"v_max3_f32 v18, v122, v123, v18\n"
"v_max3_f32 v18, v124, v125, v18\n"
"v_max3_f32 v18, v126, v127, v18\n"
"v_mul_f32 v19, s73, v18\n"
"v_mfma_f32_32x32x16_bf16 v[96:111], a[204:207], v[44:47], v[96:111]\n"
"v_xor_b32 v16, 0x10000, v16\n"
"ds_read_b128 a[220:223], v16 offset:0\n"
"v_floor_f32 v19, v19\n"
"v_accvgpr_write_b32 a55, v19\n"
"v_fmamk_f32 v112, v112, 0x3db8aa3b, v19\n"
"v_fmamk_f32 v113, v113, 0x3db8aa3b, v19\n"
"s_waitcnt lgkmcnt(2)\n"
"v_mfma_f32_32x32x16_bf16 v[96:111], a[208:211], v[48:51], v[96:111]\n"
"buffer_load_dwordx4 v2, s[44:47], s69 offen offset:1024 lds\n"
"ds_read_b128 a[192:195], v9 offset:256\n"
"v_exp_f32 v112, v112\n"
"v_fmamk_f32 v114, v114, 0x3db8aa3b, v19\n"
"v_exp_f32 v113, v113\n"
"v_mfma_f32_32x32x16_bf16 v[96:111], a[212:215], v[52:55], v[96:111]\n"
"ds_read_b128 a[196:199], v10 offset:256\n"
"v_fmamk_f32 v115, v115, 0x3db8aa3b, v19\n"
"v_exp_f32 v114, v114\n"
"v_fmamk_f32 v116, v116, 0x3db8aa3b, v19\n"
"s_waitcnt lgkmcnt(2)\n"
"v_mfma_f32_32x32x16_bf16 v[96:111], a[216:219], v[56:59], v[96:111]\n"
"ds_read_b128 a[200:203], v11 offset:256\n"
"v_exp_f32 v115, v115\n"
"v_cvt_pk_bf16_f32 v184, v112, v113\n"
"v_fmamk_f32 v117, v117, 0x3db8aa3b, v19\n"
"v_mfma_f32_32x32x16_bf16 v[96:111], a[220:223], v[60:63], v[96:111]\n"
"ds_read_b128 a[204:207], v12 offset:256\n"
"v_exp_f32 v116, v116\n"
"v_fmamk_f32 v118, v118, 0x3db8aa3b, v19\n"
"v_exp_f32 v117, v117\n"
"s_waitcnt lgkmcnt(2)\n"
"v_mfma_f32_32x32x16_bf16 v[96:111], a[192:195], v[64:67], v[96:111]\n"
"buffer_load_dwordx4 v3, s[44:47], s69 offen offset:2048 lds\n"
"ds_read_b128 a[208:211], v13 offset:256\n"
"v_cvt_pk_bf16_f32 v185, v114, v115\n"
"v_fmamk_f32 v119, v119, 0x3db8aa3b, v19\n"
"v_exp_f32 v118, v118\n"
"v_fmamk_f32 v120, v120, 0x3db8aa3b, v19\n"
"v_mfma_f32_32x32x16_bf16 v[96:111], a[196:199], v[68:71], v[96:111]\n"
"ds_read_b128 a[212:215], v14 offset:256\n"
"v_exp_f32 v119, v119\n"
"v_cvt_pk_bf16_f32 v186, v116, v117\n"
"v_fmamk_f32 v121, v121, 0x3db8aa3b, v19\n"
"s_waitcnt lgkmcnt(2)\n"
"v_mfma_f32_32x32x16_bf16 v[96:111], a[200:203], v[72:75], v[96:111]\n"
"ds_read_b128 a[216:219], v15 offset:256\n"
"v_exp_f32 v120, v120\n"
"v_fmamk_f32 v122, v122, 0x3db8aa3b, v19\n"
"v_exp_f32 v121, v121\n"
"v_mfma_f32_32x32x16_bf16 v[96:111], a[204:207], v[76:79], v[96:111]\n"
"ds_read_b128 a[220:223], v16 offset:256\n"
"v_cvt_pk_bf16_f32 v187, v118, v119\n"
"v_fmamk_f32 v123, v123, 0x3db8aa3b, v19\n"
"v_exp_f32 v122, v122\n"
"s_waitcnt lgkmcnt(2)\n"
"v_mfma_f32_32x32x16_bf16 v[96:111], a[208:211], v[80:83], v[96:111]\n"
"s_waitcnt vmcnt(23)\n"
"s_barrier\n"
"buffer_load_dwordx4 v4, s[44:47], s69 offen offset:3072 lds\n"
"ds_read_b128 a[192:195], v9 offset:16384\n"
"v_fmamk_f32 v124, v124, 0x3db8aa3b, v19\n"
"v_exp_f32 v123, v123\n"
"v_cvt_pk_bf16_f32 v188, v120, v121\n"
"v_fmamk_f32 v125, v125, 0x3db8aa3b, v19\n"
"v_mfma_f32_32x32x16_bf16 v[96:111], a[212:215], v[84:87], v[96:111]\n"
"ds_read_b128 a[196:199], v10 offset:16384\n"
"v_exp_f32 v124, v124\n"
"v_fmamk_f32 v126, v126, 0x3db8aa3b, v19\n"
"v_exp_f32 v125, v125\n"
"s_waitcnt lgkmcnt(2)\n"
"v_mfma_f32_32x32x16_bf16 v[96:111], a[216:219], v[88:91], v[96:111]\n"
"ds_read_b128 a[200:203], v11 offset:16384\n"
"v_cvt_pk_bf16_f32 v189, v122, v123\n"
"v_fmamk_f32 v127, v127, 0x3db8aa3b, v19\n"
"v_exp_f32 v126, v126\n"
"v_mfma_f32_32x32x16_bf16 v[96:111], a[220:223], v[92:95], v[96:111]\n"
"ds_read_b128 a[204:207], v12 offset:16384\n"
"v_exp_f32 v127, v127\n"
"v_cvt_pk_bf16_f32 v190, v124, v125\n"
"v_cvt_pk_bf16_f32 v191, v126, v127\n"
"s_waitcnt lgkmcnt(2)\n"
"v_mfma_f32_32x32x16_bf16 v[112:127], a[192:195], v[32:35], 0\n"
"s_add_u32 m0, s68, 49152\n"
"s_mov_b32 s69, 245760\n"
"buffer_load_dwordx4 v1, s[44:47], s69 offen offset:0 lds\n"
"ds_read_b128 a[208:211], v13 offset:16384\n"
"v_mfma_f32_32x32x16_bf16 v[112:127], a[196:199], v[36:39], v[112:127]\n"
"ds_read_b128 a[212:215], v14 offset:16384\n"
"v_max3_f32 v18, v96, v97, v18\n"
"v_max3_f32 v18, v98, v99, v18\n"
"v_max3_f32 v18, v100, v101, v18\n"
"v_max3_f32 v18, v102, v103, v18\n"
"s_waitcnt lgkmcnt(2)\n"
"v_mfma_f32_32x32x16_bf16 v[112:127], a[200:203], v[40:43], v[112:127]\n"
"ds_read_b128 a[216:219], v15 offset:16384\n"
"v_max3_f32 v18, v104, v105, v18\n"
"v_max3_f32 v18, v106, v107, v18\n"
"v_max3_f32 v18, v108, v109, v18\n"
"v_max3_f32 v18, v110, v111, v18\n"
"v_mul_f32 v19, s73, v18\n"
"v_mfma_f32_32x32x16_bf16 v[112:127], a[204:207], v[44:47], v[112:127]\n"
"ds_read_b128 a[220:223], v16 offset:16384\n"
"v_floor_f32 v19, v19\n"
"v_accvgpr_write_b32 a56, v19\n"
"v_fmamk_f32 v96, v96, 0x3db8aa3b, v19\n"
"v_fmamk_f32 v97, v97, 0x3db8aa3b, v19\n"
"s_waitcnt lgkmcnt(2)\n"
"v_mfma_f32_32x32x16_bf16 v[112:127], a[208:211], v[48:51], v[112:127]\n"
"buffer_load_dwordx4 v2, s[44:47], s69 offen offset:1024 lds\n"
"ds_read_b128 a[192:195], v9 offset:16640\n"
"v_exp_f32 v96, v96\n"
"v_fmamk_f32 v98, v98, 0x3db8aa3b, v19\n"
"v_exp_f32 v97, v97\n"
"v_mfma_f32_32x32x16_bf16 v[112:127], a[212:215], v[52:55], v[112:127]\n"
"ds_read_b128 a[196:199], v10 offset:16640\n"
"v_fmamk_f32 v99, v99, 0x3db8aa3b, v19\n"
"v_exp_f32 v98, v98\n"
"v_fmamk_f32 v100, v100, 0x3db8aa3b, v19\n"
"s_waitcnt lgkmcnt(2)\n"
"v_mfma_f32_32x32x16_bf16 v[112:127], a[216:219], v[56:59], v[112:127]\n"
"ds_read_b128 a[200:203], v11 offset:16640\n"
"v_exp_f32 v99, v99\n"
"v_cvt_pk_bf16_f32 v192, v96, v97\n"
"v_fmamk_f32 v101, v101, 0x3db8aa3b, v19\n"
"v_mfma_f32_32x32x16_bf16 v[112:127], a[220:223], v[60:63], v[112:127]\n"
"ds_read_b128 a[204:207], v12 offset:16640\n"
"v_exp_f32 v100, v100\n"
"v_fmamk_f32 v102, v102, 0x3db8aa3b, v19\n"
"v_exp_f32 v101, v101\n"
"s_waitcnt lgkmcnt(2)\n"
"v_mfma_f32_32x32x16_bf16 v[112:127], a[192:195], v[64:67], v[112:127]\n"
"buffer_load_dwordx4 v3, s[44:47], s69 offen offset:2048 lds\n"
"ds_read_b128 a[208:211], v13 offset:16640\n"
"v_cvt_pk_bf16_f32 v193, v98, v99\n"
"v_fmamk_f32 v103, v103, 0x3db8aa3b, v19\n"
"v_exp_f32 v102, v102\n"
"v_fmamk_f32 v104, v104, 0x3db8aa3b, v19\n"
"v_mfma_f32_32x32x16_bf16 v[112:127], a[196:199], v[68:71], v[112:127]\n"
"ds_read_b128 a[212:215], v14 offset:16640\n"
"v_exp_f32 v103, v103\n"
"v_cvt_pk_bf16_f32 v194, v100, v101\n"
"v_fmamk_f32 v105, v105, 0x3db8aa3b, v19\n"
"s_waitcnt lgkmcnt(2)\n"
"v_mfma_f32_32x32x16_bf16 v[112:127], a[200:203], v[72:75], v[112:127]\n"
"ds_read_b128 a[216:219], v15 offset:16640\n"
"v_exp_f32 v104, v104\n"
"v_fmamk_f32 v106, v106, 0x3db8aa3b, v19\n"
"v_exp_f32 v105, v105\n"
"v_mfma_f32_32x32x16_bf16 v[112:127], a[204:207], v[76:79], v[112:127]\n"
"ds_read_b128 a[220:223], v16 offset:16640\n"
"v_cvt_pk_bf16_f32 v195, v102, v103\n"
"v_fmamk_f32 v107, v107, 0x3db8aa3b, v19\n"
"v_exp_f32 v106, v106\n"
"s_waitcnt lgkmcnt(2)\n"
"v_mfma_f32_32x32x16_bf16 v[112:127], a[208:211], v[80:83], v[112:127]\n"
"s_waitcnt vmcnt(22)\n"
"s_barrier\n"
"buffer_load_dwordx4 v4, s[44:47], s69 offen offset:3072 lds\n"
"ds_read_b128 a[192:195], v9 offset:32768\n"
"v_fmamk_f32 v108, v108, 0x3db8aa3b, v19\n"
"v_exp_f32 v107, v107\n"
"v_cvt_pk_bf16_f32 v196, v104, v105\n"
"v_fmamk_f32 v109, v109, 0x3db8aa3b, v19\n"
"v_mfma_f32_32x32x16_bf16 v[112:127], a[212:215], v[84:87], v[112:127]\n"
"ds_read_b128 a[196:199], v10 offset:32768\n"
"v_exp_f32 v108, v108\n"
"v_fmamk_f32 v110, v110, 0x3db8aa3b, v19\n"
"v_exp_f32 v109, v109\n"
"s_waitcnt lgkmcnt(2)\n"
"v_mfma_f32_32x32x16_bf16 v[112:127], a[216:219], v[88:91], v[112:127]\n"
"ds_read_b128 a[200:203], v11 offset:32768\n"
"v_cvt_pk_bf16_f32 v197, v106, v107\n"
"v_fmamk_f32 v111, v111, 0x3db8aa3b, v19\n"
"v_exp_f32 v110, v110\n"
"v_mfma_f32_32x32x16_bf16 v[112:127], a[220:223], v[92:95], v[112:127]\n"
"v_lshrrev_b32 v28, 5, v0\n"
"s_lshl_b32 s75, s71, 3\n"
"v_add_u32 v28, s75, v28\n"
"v_and_b32 v29, 31, v0\n"
"v_add_u32 v26, 0, v28\n"
"v_and_b32 v27, 3, v26\n"
"v_lshlrev_b32 v27, 2, v27\n"
"v_xor_b32 v27, v27, v29\n"
"v_lshlrev_b32 v27, 4, v27\n"
"v_mul_u32_u24 v26, 512, v26\n"
"v_add_u32 v27, v26, v27\n"
"v_subrev_u32 v1, 0, v27\n"
"v_add_u32 v26, 2, v28\n"
"v_and_b32 v27, 3, v26\n"
"v_lshlrev_b32 v27, 2, v27\n"
"v_xor_b32 v27, v27, v29\n"
"v_lshlrev_b32 v27, 4, v27\n"
"v_mul_u32_u24 v26, 512, v26\n"
"ds_read_b128 a[204:207], v12 offset:32768\n"
"v_exp_f32 v111, v111\n"
"v_cvt_pk_bf16_f32 v198, v108, v109\n"
"v_cvt_pk_bf16_f32 v199, v110, v111\n"
"s_waitcnt lgkmcnt(2)\n"
"v_mfma_f32_32x32x16_bf16 v[96:111], a[192:195], v[32:35], 0\n"
"v_add_u32 v27, v26, v27\n"
"v_subrev_u32 v2, 1024, v27\n"
"v_add_u32 v26, 4, v28\n"
"v_and_b32 v27, 3, v26\n"
"v_lshlrev_b32 v27, 2, v27\n"
"v_xor_b32 v27, v27, v29\n"
"v_lshlrev_b32 v27, 4, v27\n"
"v_mul_u32_u24 v26, 512, v26\n"
"v_add_u32 v27, v26, v27\n"
"v_subrev_u32 v3, 2048, v27\n"
"v_add_u32 v26, 6, v28\n"
"v_and_b32 v27, 3, v26\n"
"v_lshlrev_b32 v27, 2, v27\n"
"v_xor_b32 v27, v27, v29\n"
"v_lshlrev_b32 v27, 4, v27\n"
"v_mul_u32_u24 v26, 512, v26\n"
"v_add_u32 v27, v26, v27\n"
"v_subrev_u32 v4, 3072, v27\n"
"s_add_u32 m0, s68, 65536\n"
"s_mov_b32 s69, 0\n"
"buffer_load_dwordx4 v1, s[48:51], s69 offen offset:0 lds\n"
"ds_read_b128 a[208:211], v13 offset:32768\n"
"v_mfma_f32_32x32x16_bf16 v[96:111], a[196:199], v[36:39], v[96:111]\n"
"ds_read_b128 a[212:215], v14 offset:32768\n"
"v_max3_f32 v18, v112, v113, v18\n"
"v_max3_f32 v18, v114, v115, v18\n"
"v_max3_f32 v18, v116, v117, v18\n"
"v_max3_f32 v18, v118, v119, v18\n"
"s_waitcnt lgkmcnt(2)\n"
"v_mfma_f32_32x32x16_bf16 v[96:111], a[200:203], v[40:43], v[96:111]\n"
"ds_read_b128 a[216:219], v15 offset:32768\n"
"v_max3_f32 v18, v120, v121, v18\n"
"v_max3_f32 v18, v122, v123, v18\n"
"v_max3_f32 v18, v124, v125, v18\n"
"v_max3_f32 v18, v126, v127, v18\n"
"v_mul_f32 v19, s73, v18\n"
"s_mov_b32 s74, 24576\n"
"buffer_store_dwordx4 a[248:251], v23, s[64:67], s74 offen offset:0 sc1\n"
"v_mfma_f32_32x32x16_bf16 v[96:111], a[204:207], v[44:47], v[96:111]\n"
"ds_read_b128 a[220:223], v16 offset:32768\n"
"v_floor_f32 v19, v19\n"
"v_accvgpr_write_b32 a57, v19\n"
"v_fmamk_f32 v112, v112, 0x3db8aa3b, v19\n"
"v_fmamk_f32 v113, v113, 0x3db8aa3b, v19\n"
"s_waitcnt lgkmcnt(2)\n"
"v_mfma_f32_32x32x16_bf16 v[96:111], a[208:211], v[48:51], v[96:111]\n"
"buffer_load_dwordx4 v2, s[48:51], s69 offen offset:1024 lds\n"
"ds_read_b128 a[192:195], v9 offset:33024\n"
"v_exp_f32 v112, v112\n"
"v_fmamk_f32 v114, v114, 0x3db8aa3b, v19\n"
"v_exp_f32 v113, v113\n"
"v_mfma_f32_32x32x16_bf16 v[96:111], a[212:215], v[52:55], v[96:111]\n"
"ds_read_b128 a[196:199], v10 offset:33024\n"
"v_fmamk_f32 v115, v115, 0x3db8aa3b, v19\n"
"v_exp_f32 v114, v114\n"
"v_fmamk_f32 v116, v116, 0x3db8aa3b, v19\n"
"s_waitcnt lgkmcnt(2)\n"
"v_mfma_f32_32x32x16_bf16 v[96:111], a[216:219], v[56:59], v[96:111]\n"
"ds_read_b128 a[200:203], v11 offset:33024\n"
"v_exp_f32 v115, v115\n"
"v_cvt_pk_bf16_f32 v200, v112, v113\n"
"v_fmamk_f32 v117, v117, 0x3db8aa3b, v19\n"
"v_mfma_f32_32x32x16_bf16 v[96:111], a[220:223], v[60:63], v[96:111]\n"
"ds_read_b128 a[204:207], v12 offset:33024\n"
"v_exp_f32 v116, v116\n"
"v_fmamk_f32 v118, v118, 0x3db8aa3b, v19\n"
"v_exp_f32 v117, v117\n"
"s_waitcnt lgkmcnt(2)\n"
"v_mfma_f32_32x32x16_bf16 v[96:111], a[192:195], v[64:67], v[96:111]\n"
"buffer_load_dwordx4 v3, s[48:51], s69 offen offset:2048 lds\n"
"ds_read_b128 a[208:211], v13 offset:33024\n"
"v_cvt_pk_bf16_f32 v201, v114, v115\n"
"v_fmamk_f32 v119, v119, 0x3db8aa3b, v19\n"
"v_exp_f32 v118, v118\n"
"v_fmamk_f32 v120, v120, 0x3db8aa3b, v19\n"
"v_mfma_f32_32x32x16_bf16 v[96:111], a[196:199], v[68:71], v[96:111]\n"
"ds_read_b128 a[212:215], v14 offset:33024\n"
"v_exp_f32 v119, v119\n"
"v_cvt_pk_bf16_f32 v202, v116, v117\n"
"v_fmamk_f32 v121, v121, 0x3db8aa3b, v19\n"
"s_waitcnt lgkmcnt(2)\n"
"v_mfma_f32_32x32x16_bf16 v[96:111], a[200:203], v[72:75], v[96:111]\n"
"ds_read_b128 a[216:219], v15 offset:33024\n"
"v_exp_f32 v120, v120\n"
"v_fmamk_f32 v122, v122, 0x3db8aa3b, v19\n"
"v_exp_f32 v121, v121\n"
"v_mfma_f32_32x32x16_bf16 v[96:111], a[204:207], v[76:79], v[96:111]\n"
"ds_read_b128 a[220:223], v16 offset:33024\n"
"v_cvt_pk_bf16_f32 v203, v118, v119\n"
"v_fmamk_f32 v123, v123, 0x3db8aa3b, v19\n"
"v_exp_f32 v122, v122\n"
"s_waitcnt lgkmcnt(2)\n"
"v_mfma_f32_32x32x16_bf16 v[96:111], a[208:211], v[80:83], v[96:111]\n"
"s_waitcnt vmcnt(22)\n"
"s_barrier\n"
"buffer_load_dwordx4 v4, s[48:51], s69 offen offset:3072 lds\n"
"ds_read_b128 a[192:195], v9 offset:49152\n"
"v_fmamk_f32 v124, v124, 0x3db8aa3b, v19\n"
"v_exp_f32 v123, v123\n"
"v_cvt_pk_bf16_f32 v204, v120, v121\n"
"v_fmamk_f32 v125, v125, 0x3db8aa3b, v19\n"
"v_mfma_f32_32x32x16_bf16 v[96:111], a[212:215], v[84:87], v[96:111]\n"
"ds_read_b128 a[196:199], v10 offset:49152\n"
"v_exp_f32 v124, v124\n"
"v_fmamk_f32 v126, v126, 0x3db8aa3b, v19\n"
"v_exp_f32 v125, v125\n"
"s_waitcnt lgkmcnt(2)\n"
"v_mfma_f32_32x32x16_bf16 v[96:111], a[216:219], v[88:91], v[96:111]\n"
"ds_read_b128 a[200:203], v11 offset:49152\n"
"v_cvt_pk_bf16_f32 v205, v122, v123\n"
"v_fmamk_f32 v127, v127, 0x3db8aa3b, v19\n"
"v_exp_f32 v126, v126\n"
"v_mfma_f32_32x32x16_bf16 v[96:111], a[220:223], v[92:95], v[96:111]\n"
"ds_read_b128 a[204:207], v12 offset:49152\n"
"v_exp_f32 v127, v127\n"
"v_cvt_pk_bf16_f32 v206, v124, v125\n"
"v_cvt_pk_bf16_f32 v207, v126, v127\n"
"s_waitcnt lgkmcnt(2)\n"
"v_mfma_f32_32x32x16_bf16 v[112:127], a[192:195], v[32:35], 0\n"
"s_add_u32 m0, s68, 81920\n"
"s_mov_b32 s69, 16384\n"
"buffer_load_dwordx4 v1, s[48:51], s69 offen offset:0 lds\n"
"ds_read_b128 a[208:211], v13 offset:49152\n"
"v_mfma_f32_32x32x16_bf16 v[112:127], a[196:199], v[36:39], v[112:127]\n"
"ds_read_b128 a[212:215], v14 offset:49152\n"
"v_max3_f32 v18, v96, v97, v18\n"
"v_max3_f32 v18, v98, v99, v18\n"
"v_max3_f32 v18, v100, v101, v18\n"
"v_max3_f32 v18, v102, v103, v18\n"
"s_waitcnt lgkmcnt(2)\n"
"v_mfma_f32_32x32x16_bf16 v[112:127], a[200:203], v[40:43], v[112:127]\n"
"ds_read_b128 a[216:219], v15 offset:49152\n"
"v_max3_f32 v18, v104, v105, v18\n"
"v_max3_f32 v18, v106, v107, v18\n"
"v_max3_f32 v18, v108, v109, v18\n"
"v_max3_f32 v18, v110, v111, v18\n"
"v_mul_f32 v19, s73, v18\n"
"v_mfma_f32_32x32x16_bf16 v[112:127], a[204:207], v[44:47], v[112:127]\n"
"ds_read_b128 a[220:223], v16 offset:49152\n"
"v_floor_f32 v19, v19\n"
"v_accvgpr_write_b32 a58, v19\n"
"v_fmamk_f32 v96, v96, 0x3db8aa3b, v19\n"
"v_fmamk_f32 v97, v97, 0x3db8aa3b, v19\n"
"s_lshl_b32 s75, %[T], 2\n"
"s_add_u32 s75, s75, s71\n"
"s_mul_i32 s75, s75, 14\n"
"v_min_u32 v30, 13, v0\n"
"v_add_u32 v30, s75, v30\n"
"v_lshrrev_b32 v31, 2, v30\n"
"v_mul_u32_u24 v31, 3072, v31\n"
"v_lshl_add_u32 v30, v30, 7, v31\n"
"buffer_load_dword a228, v30, s[40:43], 0 offen\n"
"s_waitcnt lgkmcnt(2)\n"
"v_mfma_f32_32x32x16_bf16 v[112:127], a[208:211], v[48:51], v[112:127]\n"
"buffer_load_dwordx4 v2, s[48:51], s69 offen offset:1024 lds\n"
"ds_read_b128 a[192:195], v9 offset:49408\n"
"v_exp_f32 v96, v96\n"
"v_fmamk_f32 v98, v98, 0x3db8aa3b, v19\n"
"v_exp_f32 v97, v97\n"
"v_mfma_f32_32x32x16_bf16 v[112:127], a[212:215], v[52:55], v[112:127]\n"
"ds_read_b128 a[196:199], v10 offset:49408\n"
"v_fmamk_f32 v99, v99, 0x3db8aa3b, v19\n"
"v_exp_f32 v98, v98\n"
"v_fmamk_f32 v100, v100, 0x3db8aa3b, v19\n"
"s_waitcnt lgkmcnt(2)\n"
"v_mfma_f32_32x32x16_bf16 v[112:127], a[216:219], v[56:59], v[112:127]\n"
"ds_read_b128 a[200:203], v11 offset:49408\n"
"v_exp_f32 v99, v99\n"
"v_cvt_pk_bf16_f32 v208, v96, v97\n"
"v_fmamk_f32 v101, v101, 0x3db8aa3b, v19\n"
"v_mfma_f32_32x32x16_bf16 v[112:127], a[220:223], v[60:63], v[112:127]\n"
"ds_read_b128 a[204:207], v12 offset:49408\n"
"v_exp_f32 v100, v100\n"
"v_fmamk_f32 v102, v102, 0x3db8aa3b, v19\n"
"v_exp_f32 v101, v101\n"
"s_waitcnt lgkmcnt(2)\n"
"v_mfma_f32_32x32x16_bf16 v[112:127], a[192:195], v[64:67], v[112:127]\n"
"buffer_load_dwordx4 v3, s[48:51], s69 offen offset:2048 lds\n"
"ds_read_b128 a[208:211], v13 offset:49408\n"
"v_cvt_pk_bf16_f32 v209, v98, v99\n"
"v_fmamk_f32 v103, v103, 0x3db8aa3b, v19\n"
"v_exp_f32 v102, v102\n"
"v_fmamk_f32 v104, v104, 0x3db8aa3b, v19\n"
"v_mfma_f32_32x32x16_bf16 v[112:127], a[196:199], v[68:71], v[112:127]\n"
"ds_read_b128 a[212:215], v14 offset:49408\n"
"v_exp_f32 v103, v103\n"
"v_cvt_pk_bf16_f32 v210, v100, v101\n"
"v_fmamk_f32 v105, v105, 0x3db8aa3b, v19\n"
"s_waitcnt lgkmcnt(2)\n"
"v_mfma_f32_32x32x16_bf16 v[112:127], a[200:203], v[72:75], v[112:127]\n"
"ds_read_b128 a[216:219], v15 offset:49408\n"
"v_exp_f32 v104, v104\n"
"v_fmamk_f32 v106, v106, 0x3db8aa3b, v19\n"
"v_exp_f32 v105, v105\n"
"v_mfma_f32_32x32x16_bf16 v[112:127], a[204:207], v[76:79], v[112:127]\n"
"ds_read_b128 a[220:223], v16 offset:49408\n"
"v_cvt_pk_bf16_f32 v211, v102, v103\n"
"v_fmamk_f32 v107, v107, 0x3db8aa3b, v19\n"
"v_exp_f32 v106, v106\n"
"s_waitcnt lgkmcnt(2)\n"
"v_mfma_f32_32x32x16_bf16 v[112:127], a[208:211], v[80:83], v[112:127]\n"
"s_waitcnt vmcnt(22)\n"
"s_barrier\n"
"buffer_load_dwordx4 v4, s[48:51], s69 offen offset:3072 lds\n"
"v_xor_b32 v9, 0x10000, v9\n"
"ds_read_b128 a[192:195], v9 offset:0\n"
"v_fmamk_f32 v108, v108, 0x3db8aa3b, v19\n"
"v_exp_f32 v107, v107\n"
"v_cvt_pk_bf16_f32 v212, v104, v105\n"
"v_fmamk_f32 v109, v109, 0x3db8aa3b, v19\n"
"v_mfma_f32_32x32x16_bf16 v[112:127], a[212:215], v[84:87], v[112:127]\n"
"v_xor_b32 v10, 0x10000, v10\n"
"ds_read_b128 a[196:199], v10 offset:0\n"
"v_exp_f32 v108, v108\n"
"v_fmamk_f32 v110, v110, 0x3db8aa3b, v19\n"
"v_exp_f32 v109, v109\n"
"s_waitcnt lgkmcnt(2)\n"
"v_mfma_f32_32x32x16_bf16 v[112:127], a[216:219], v[88:91], v[112:127]\n"
"v_xor_b32 v11, 0x10000, v11\n"
"ds_read_b128 a[200:203], v11 offset:0\n"
"v_cvt_pk_bf16_f32 v213, v106, v107\n"
"v_fmamk_f32 v111, v111, 0x3db8aa3b, v19\n"
"v_exp_f32 v110, v110\n"
"v_mfma_f32_32x32x16_bf16 v[112:127], a[220:223], v[92:95], v[112:127]\n"
"v_xor_b32 v12, 0x10000, v12\n"
"ds_read_b128 a[204:207], v12 offset:0\n"
"v_exp_f32 v111, v111\n"
"v_cvt_pk_bf16_f32 v214, v108, v109\n"
"v_cvt_pk_bf16_f32 v215, v110, v111\n"
"s_waitcnt lgkmcnt(2)\n"
"v_mfma_f32_32x32x16_bf16 v[96:111], a[192:195], v[32:35], 0\n"
"s_add_u32 m0, s68, 98304\n"
"s_mov_b32 s69, 32768\n"
"buffer_load_dwordx4 v1, s[48:51], s69 offen offset:0 lds\n"
"v_xor_b32 v13, 0x10000, v13\n"
"ds_read_b128 a[208:211], v13 offset:0\n"
"v_mfma_f32_32x32x16_bf16 v[96:111], a[196:199], v[36:39], v[96:111]\n"
"v_xor_b32 v14, 0x10000, v14\n"
"ds_read_b128 a[212:215], v14 offset:0\n"
"v_max3_f32 v18, v112, v113, v18\n"
"v_max3_f32 v18, v114, v115, v18\n"
"v_max3_f32 v18, v116, v117, v18\n"
"v_max3_f32 v18, v118, v119, v18\n"
"s_waitcnt lgkmcnt(2)\n"
"v_mfma_f32_32x32x16_bf16 v[96:111], a[200:203], v[40:43], v[96:111]\n"
"v_xor_b32 v15, 0x10000, v15\n"
"ds_read_b128 a[216:219], v15 offset:0\n"
"v_max3_f32 v18, v120, v121, v18\n"
"v_max3_f32 v18, v122, v123, v18\n"
"v_max3_f32 v18, v124, v125, v18\n"
"v_max3_f32 v18, v126, v127, v18\n"
"v_mul_f32 v19, s73, v18\n"
"v_mfma_f32_32x32x16_bf16 v[96:111], a[204:207], v[44:47], v[96:111]\n"
"v_xor_b32 v16, 0x10000, v16\n"
"ds_read_b128 a[220:223], v16 offset:0\n"
"v_floor_f32 v19, v19\n"
"v_accvgpr_write_b32 a59, v19\n"
"v_fmamk_f32 v112, v112, 0x3db8aa3b, v19\n"
"v_fmamk_f32 v113, v113, 0x3db8aa3b, v19\n"
"s_waitcnt lgkmcnt(2)\n"
"v_mfma_f32_32x32x16_bf16 v[96:111], a[208:211], v[48:51], v[96:111]\n"
"buffer_load_dwordx4 v2, s[48:51], s69 offen offset:1024 lds\n"
"ds_read_b128 a[192:195], v9 offset:256\n"
"v_exp_f32 v112, v112\n"
"v_fmamk_f32 v114, v114, 0x3db8aa3b, v19\n"
"v_exp_f32 v113, v113\n"
"s_mov_b32 s74, 24576\n"
"buffer_store_dwordx4 a[248:251], v23, s[64:67], s74 offen offset:1024 sc1\n"
"v_mfma_f32_32x32x16_bf16 v[96:111], a[212:215], v[52:55], v[96:111]\n"
"ds_read_b128 a[196:199], v10 offset:256\n"
"v_fmamk_f32 v115, v115, 0x3db8aa3b, v19\n"
"v_exp_f32 v114, v114\n"
"v_fmamk_f32 v116, v116, 0x3db8aa3b, v19\n"
"s_waitcnt lgkmcnt(2)\n"
"v_mfma_f32_32x32x16_bf16 v[96:111], a[216:219], v[56:59], v[96:111]\n"
"ds_read_b128 a[200:203], v11 offset:256\n"
"v_exp_f32 v115, v115\n"
"v_cvt_pk_bf16_f32 v216, v112, v113\n"
"v_fmamk_f32 v117, v117, 0x3db8aa3b, v19\n"
"v_mfma_f32_32x32x16_bf16 v[96:111], a[220:223], v[60:63], v[96:111]\n"
"ds_read_b128 a[204:207], v12 offset:256\n"
"v_exp_f32 v116, v116\n"
"v_fmamk_f32 v118, v118, 0x3db8aa3b, v19\n"
"v_exp_f32 v117, v117\n"
"s_waitcnt lgkmcnt(2)\n"
"v_mfma_f32_32x32x16_bf16 v[96:111], a[192:195], v[64:67], v[96:111]\n"
"buffer_load_dwordx4 v3, s[48:51], s69 offen offset:2048 lds\n"
"ds_read_b128 a[208:211], v13 offset:256\n"
"v_cvt_pk_bf16_f32 v217, v114, v115\n"
"v_fmamk_f32 v119, v119, 0x3db8aa3b, v19\n"
"v_exp_f32 v118, v118\n"
"v_fmamk_f32 v120, v120, 0x3db8aa3b, v19\n"
"v_mfma_f32_32x32x16_bf16 v[96:111], a[196:199], v[68:71], v[96:111]\n"
"ds_read_b128 a[212:215], v14 offset:256\n"
"v_exp_f32 v119, v119\n"
"v_cvt_pk_bf16_f32 v218, v116, v117\n"
"v_fmamk_f32 v121, v121, 0x3db8aa3b, v19\n"
"s_waitcnt lgkmcnt(2)\n"
"v_mfma_f32_32x32x16_bf16 v[96:111], a[200:203], v[72:75], v[96:111]\n"
"ds_read_b128 a[216:219], v15 offset:256\n"
"v_exp_f32 v120, v120\n"
"v_fmamk_f32 v122, v122, 0x3db8aa3b, v19\n"
"v_exp_f32 v121, v121\n"
"v_mfma_f32_32x32x16_bf16 v[96:111], a[204:207], v[76:79], v[96:111]\n"
"ds_read_b128 a[220:223], v16 offset:256\n"
"v_cvt_pk_bf16_f32 v219, v118, v119\n"
"v_fmamk_f32 v123, v123, 0x3db8aa3b, v19\n"
"v_exp_f32 v122, v122\n"
"s_waitcnt lgkmcnt(2)\n"
"v_mfma_f32_32x32x16_bf16 v[96:111], a[208:211], v[80:83], v[96:111]\n"
"s_waitcnt vmcnt(23)\n"
"s_barrier\n"
"buffer_load_dwordx4 v4, s[48:51], s69 offen offset:3072 lds\n"
"ds_read_b128 a[192:195], v9 offset:16384\n"
"v_fmamk_f32 v124, v124, 0x3db8aa3b, v19\n"
"v_exp_f32 v123, v123\n"
"v_cvt_pk_bf16_f32 v220, v120, v121\n"
"v_fmamk_f32 v125, v125, 0x3db8aa3b, v19\n"
"v_mfma_f32_32x32x16_bf16 v[96:111], a[212:215], v[84:87], v[96:111]\n"
"ds_read_b128 a[196:199], v10 offset:16384\n"
"v_exp_f32 v124, v124\n"
"v_fmamk_f32 v126, v126, 0x3db8aa3b, v19\n"
"v_exp_f32 v125, v125\n"
"s_waitcnt lgkmcnt(2)\n"
"v_mfma_f32_32x32x16_bf16 v[96:111], a[216:219], v[88:91], v[96:111]\n"
"ds_read_b128 a[200:203], v11 offset:16384\n"
"v_cvt_pk_bf16_f32 v221, v122, v123\n"
"v_fmamk_f32 v127, v127, 0x3db8aa3b, v19\n"
"v_exp_f32 v126, v126\n"
"v_mfma_f32_32x32x16_bf16 v[96:111], a[220:223], v[92:95], v[96:111]\n"
"ds_read_b128 a[204:207], v12 offset:16384\n"
"v_exp_f32 v127, v127\n"
"v_cvt_pk_bf16_f32 v222, v124, v125\n"
"v_cvt_pk_bf16_f32 v223, v126, v127\n"
"s_waitcnt lgkmcnt(2)\n"
"v_mfma_f32_32x32x16_bf16 v[112:127], a[192:195], v[32:35], 0\n"
"s_add_u32 m0, s68, 114688\n"
"s_mov_b32 s69, 49152\n"
"buffer_load_dwordx4 v1, s[48:51], s69 offen offset:0 lds\n"
"ds_read_b128 a[208:211], v13 offset:16384\n"
"v_mfma_f32_32x32x16_bf16 v[112:127], a[196:199], v[36:39], v[112:127]\n"
"ds_read_b128 a[212:215], v14 offset:16384\n"
"v_max3_f32 v18, v96, v97, v18\n"
"v_max3_f32 v18, v98, v99, v18\n"
"v_max3_f32 v18, v100, v101, v18\n"
"v_max3_f32 v18, v102, v103, v18\n"
"s_waitcnt lgkmcnt(2)\n"
"v_mfma_f32_32x32x16_bf16 v[112:127], a[200:203], v[40:43], v[112:127]\n"
"ds_read_b128 a[216:219], v15 offset:16384\n"
"v_max3_f32 v18, v104, v105, v18\n"
"v_max3_f32 v18, v106, v107, v18\n"
"v_max3_f32 v18, v108, v109, v18\n"
"v_max3_f32 v18, v110, v111, v18\n"
"v_mul_f32 v19, s73, v18\n"
"v_mfma_f32_32x32x16_bf16 v[112:127], a[204:207], v[44:47], v[112:127]\n"
"ds_read_b128 a[220:223], v16 offset:16384\n"
"v_floor_f32 v19, v19\n"
"v_accvgpr_write_b32 a60, v19\n"
"v_fmamk_f32 v96, v96, 0x3db8aa3b, v19\n"
"v_fmamk_f32 v97, v97, 0x3db8aa3b, v19\n"
"s_waitcnt lgkmcnt(2)\n"
"v_mfma_f32_32x32x16_bf16 v[112:127], a[208:211], v[48:51], v[112:127]\n"
"buffer_load_dwordx4 v2, s[48:51], s69 offen offset:1024 lds\n"
"ds_read_b128 a[192:195], v9 offset:16640\n"
"v_exp_f32 v96, v96\n"
"v_fmamk_f32 v98, v98, 0x3db8aa3b, v19\n"
"v_exp_f32 v97, v97\n"
"v_mfma_f32_32x32x16_bf16 v[112:127], a[212:215], v[52:55], v[112:127]\n"
"ds_read_b128 a[196:199], v10 offset:16640\n"
"v_fmamk_f32 v99, v99, 0x3db8aa3b, v19\n"
"v_exp_f32 v98, v98\n"
"v_fmamk_f32 v100, v100, 0x3db8aa3b, v19\n"
"s_waitcnt lgkmcnt(2)\n"
"v_mfma_f32_32x32x16_bf16 v[112:127], a[216:219], v[56:59], v[112:127]\n"
"ds_read_b128 a[200:203], v11 offset:16640\n"
"v_exp_f32 v99, v99\n"
"v_cvt_pk_bf16_f32 v224, v96, v97\n"
"v_fmamk_f32 v101, v101, 0x3db8aa3b, v19\n"
"v_mfma_f32_32x32x16_bf16 v[112:127], a[220:223], v[60:63], v[112:127]\n"
"ds_read_b128 a[204:207], v12 offset:16640\n"
"v_exp_f32 v100, v100\n"
"v_fmamk_f32 v102, v102, 0x3db8aa3b, v19\n"
"v_exp_f32 v101, v101\n"
"s_waitcnt lgkmcnt(2)\n"
"v_mfma_f32_32x32x16_bf16 v[112:127], a[192:195], v[64:67], v[112:127]\n"
"buffer_load_dwordx4 v3, s[48:51], s69 offen offset:2048 lds\n"
"ds_read_b128 a[208:211], v13 offset:16640\n"
"v_cvt_pk_bf16_f32 v225, v98, v99\n"
"v_fmamk_f32 v103, v103, 0x3db8aa3b, v19\n"
"v_exp_f32 v102, v102\n"
"v_fmamk_f32 v104, v104, 0x3db8aa3b, v19\n"
"v_mfma_f32_32x32x16_bf16 v[112:127], a[196:199], v[68:71], v[112:127]\n"
"ds_read_b128 a[212:215], v14 offset:16640\n"
"v_exp_f32 v103, v103\n"
"v_cvt_pk_bf16_f32 v226, v100, v101\n"
"v_fmamk_f32 v105, v105, 0x3db8aa3b, v19\n"
"s_waitcnt lgkmcnt(2)\n"
"v_mfma_f32_32x32x16_bf16 v[112:127], a[200:203], v[72:75], v[112:127]\n"
"ds_read_b128 a[216:219], v15 offset:16640\n"
"v_exp_f32 v104, v104\n"
"v_fmamk_f32 v106, v106, 0x3db8aa3b, v19\n"
"v_exp_f32 v105, v105\n"
"v_mfma_f32_32x32x16_bf16 v[112:127], a[204:207], v[76:79], v[112:127]\n"
"ds_read_b128 a[220:223], v16 offset:16640\n"
"v_cvt_pk_bf16_f32 v227, v102, v103\n"
"v_fmamk_f32 v107, v107, 0x3db8aa3b, v19\n"
"v_exp_f32 v106, v106\n"
"s_waitcnt lgkmcnt(2)\n"
"v_mfma_f32_32x32x16_bf16 v[112:127], a[208:211], v[80:83], v[112:127]\n"
"s_waitcnt vmcnt(22)\n"
"s_barrier\n"
"buffer_load_dwordx4 v4, s[48:51], s69 offen offset:3072 lds\n"
"ds_read_b128 a[192:195], v9 offset:32768\n"
"v_fmamk_f32 v108, v108, 0x3db8aa3b, v19\n"
"v_exp_f32 v107, v107\n"
"v_cvt_pk_bf16_f32 v228, v104, v105\n"
"v_fmamk_f32 v109, v109, 0x3db8aa3b, v19\n"
"v_mfma_f32_32x32x16_bf16 v[112:127], a[212:215], v[84:87], v[112:127]\n"
"ds_read_b128 a[196:199], v10 offset:32768\n"
"v_exp_f32 v108, v108\n"
"v_fmamk_f32 v110, v110, 0x3db8aa3b, v19\n"
"v_exp_f32 v109, v109\n"
"s_waitcnt lgkmcnt(2)\n"
"v_mfma_f32_32x32x16_bf16 v[112:127], a[216:219], v[88:91], v[112:127]\n"
"ds_read_b128 a[200:203], v11 offset:32768\n"
"v_cvt_pk_bf16_f32 v229, v106, v107\n"
"v_fmamk_f32 v111, v111, 0x3db8aa3b, v19\n"
"v_exp_f32 v110, v110\n"
"v_mfma_f32_32x32x16_bf16 v[112:127], a[220:223], v[92:95], v[112:127]\n"
"ds_read_b128 a[204:207], v12 offset:32768\n"
"v_exp_f32 v111, v111\n"
"v_cvt_pk_bf16_f32 v230, v108, v109\n"
"v_cvt_pk_bf16_f32 v231, v110, v111\n"
"s_waitcnt lgkmcnt(2)\n"
"v_mfma_f32_32x32x16_bf16 v[96:111], a[192:195], v[32:35], 0\n"
"s_add_u32 m0, s68, 0\n"
"s_mov_b32 s69, 65536\n"
"buffer_load_dwordx4 v1, s[48:51], s69 offen offset:0 lds\n"
"ds_read_b128 a[208:211], v13 offset:32768\n"
"v_mfma_f32_32x32x16_bf16 v[96:111], a[196:199], v[36:39], v[96:111]\n"
"ds_read_b128 a[212:215], v14 offset:32768\n"
"v_max3_f32 v18, v112, v113, v18\n"
"v_max3_f32 v18, v114, v115, v18\n"
"v_max3_f32 v18, v116, v117, v18\n"
"v_max3_f32 v18, v118, v119, v18\n"
"s_waitcnt lgkmcnt(2)\n"
"v_mfma_f32_32x32x16_bf16 v[96:111], a[200:203], v[40:43], v[96:111]\n"
"ds_read_b128 a[216:219], v15 offset:32768\n"
"v_max3_f32 v18, v120, v121, v18\n"
"v_max3_f32 v18, v122, v123, v18\n"
"v_max3_f32 v18, v124, v125, v18\n"
"v_max3_f32 v18, v126, v127, v18\n"
"v_mul_f32 v19, s73, v18\n"
"v_mfma_f32_32x32x16_bf16 v[96:111], a[204:207], v[44:47], v[96:111]\n"
"ds_read_b128 a[220:223], v16 offset:32768\n"
"v_floor_f32 v19, v19\n"
"v_accvgpr_write_b32 a61, v19\n"
"v_fmamk_f32 v112, v112, 0x3db8aa3b, v19\n"
"v_fmamk_f32 v113, v113, 0x3db8aa3b, v19\n"
"s_waitcnt lgkmcnt(2)\n"
"v_mfma_f32_32x32x16_bf16 v[96:111], a[208:211], v[48:51], v[96:111]\n"
"buffer_load_dwordx4 v2, s[48:51], s69 offen offset:1024 lds\n"
"ds_read_b128 a[192:195], v9 offset:33024\n"
"v_exp_f32 v112, v112\n"
"v_fmamk_f32 v114, v114, 0x3db8aa3b, v19\n"
"v_exp_f32 v113, v113\n"
"v_mfma_f32_32x32x16_bf16 v[96:111], a[212:215], v[52:55], v[96:111]\n"
"ds_read_b128 a[196:199], v10 offset:33024\n"
"v_fmamk_f32 v115, v115, 0x3db8aa3b, v19\n"
"v_exp_f32 v114, v114\n"
"v_fmamk_f32 v116, v116, 0x3db8aa3b, v19\n"
"s_waitcnt lgkmcnt(2)\n"
"v_mfma_f32_32x32x16_bf16 v[96:111], a[216:219], v[56:59], v[96:111]\n"
"ds_read_b128 a[200:203], v11 offset:33024\n"
"v_exp_f32 v115, v115\n"
"v_cvt_pk_bf16_f32 v232, v112, v113\n"
"v_fmamk_f32 v117, v117, 0x3db8aa3b, v19\n"
"s_mov_b32 s74, 14692352\n"
"buffer_store_dwordx4 a[248:251], v23, s[64:67], s74 offen offset:0 sc1\n"
"v_mfma_f32_32x32x16_bf16 v[96:111], a[220:223], v[60:63], v[96:111]\n"
"ds_read_b128 a[204:207], v12 offset:33024\n"
"v_exp_f32 v116, v116\n"
"v_fmamk_f32 v118, v118, 0x3db8aa3b, v19\n"
"v_exp_f32 v117, v117\n"
"s_waitcnt lgkmcnt(2)\n"
"v_mfma_f32_32x32x16_bf16 v[96:111], a[192:195], v[64:67], v[96:111]\n"
"buffer_load_dwordx4 v3, s[48:51], s69 offen offset:2048 lds\n"
"ds_read_b128 a[208:211], v13 offset:33024\n"
"v_cvt_pk_bf16_f32 v233, v114, v115\n"
"v_fmamk_f32 v119, v119, 0x3db8aa3b, v19\n"
"v_exp_f32 v118, v118\n"
"v_fmamk_f32 v120, v120, 0x3db8aa3b, v19\n"
"v_mfma_f32_32x32x16_bf16 v[96:111], a[196:199], v[68:71], v[96:111]\n"
"ds_read_b128 a[212:215], v14 offset:33024\n"
"v_exp_f32 v119, v119\n"
"v_cvt_pk_bf16_f32 v234, v116, v117\n"
"v_fmamk_f32 v121, v121, 0x3db8aa3b, v19\n"
"s_waitcnt lgkmcnt(2)\n"
"v_mfma_f32_32x32x16_bf16 v[96:111], a[200:203], v[72:75], v[96:111]\n"
"ds_read_b128 a[216:219], v15 offset:33024\n"
"v_exp_f32 v120, v120\n"
"v_fmamk_f32 v122, v122, 0x3db8aa3b, v19\n"
"v_exp_f32 v121, v121\n"
"v_mfma_f32_32x32x16_bf16 v[96:111], a[204:207], v[76:79], v[96:111]\n"
"ds_read_b128 a[220:223], v16 offset:33024\n"
"v_cvt_pk_bf16_f32 v235, v118, v119\n"
"v_fmamk_f32 v123, v123, 0x3db8aa3b, v19\n"
"v_exp_f32 v122, v122\n"
"s_waitcnt lgkmcnt(2)\n"
"v_mfma_f32_32x32x16_bf16 v[96:111], a[208:211], v[80:83], v[96:111]\n"
"s_waitcnt vmcnt(23)\n"
"s_barrier\n"
"buffer_load_dwordx4 v4, s[48:51], s69 offen offset:3072 lds\n"
"ds_read_b128 a[192:195], v9 offset:49152\n"
"v_fmamk_f32 v124, v124, 0x3db8aa3b, v19\n"
"v_exp_f32 v123, v123\n"
"v_cvt_pk_bf16_f32 v236, v120, v121\n"
"v_fmamk_f32 v125, v125, 0x3db8aa3b, v19\n"
"v_mfma_f32_32x32x16_bf16 v[96:111], a[212:215], v[84:87], v[96:111]\n"
"ds_read_b128 a[196:199], v10 offset:49152\n"
"v_exp_f32 v124, v124\n"
"v_fmamk_f32 v126, v126, 0x3db8aa3b, v19\n"
"v_exp_f32 v125, v125\n"
"s_waitcnt lgkmcnt(2)\n"
"v_mfma_f32_32x32x16_bf16 v[96:111], a[216:219], v[88:91], v[96:111]\n"
"ds_read_b128 a[200:203], v11 offset:49152\n"
"v_cvt_pk_bf16_f32 v237, v122, v123\n"
"v_fmamk_f32 v127, v127, 0x3db8aa3b, v19\n"
"v_exp_f32 v126, v126\n"
"v_mfma_f32_32x32x16_bf16 v[96:111], a[220:223], v[92:95], v[96:111]\n"
"ds_read_b128 a[204:207], v12 offset:49152\n"
"v_exp_f32 v127, v127\n"
"v_cvt_pk_bf16_f32 v238, v124, v125\n"
"v_cvt_pk_bf16_f32 v239, v126, v127\n"
"s_waitcnt lgkmcnt(2)\n"
"v_mfma_f32_32x32x16_bf16 v[112:127], a[192:195], v[32:35], 0\n"
"s_add_u32 m0, s68, 16384\n"
"s_mov_b32 s69, 81920\n"
"buffer_load_dwordx4 v1, s[48:51], s69 offen offset:0 lds\n"
"ds_read_b128 a[208:211], v13 offset:49152\n"
"v_mfma_f32_32x32x16_bf16 v[112:127], a[196:199], v[36:39], v[112:127]\n"
"ds_read_b128 a[212:215], v14 offset:49152\n"
"v_max3_f32 v18, v96, v97, v18\n"
"v_max3_f32 v18, v98, v99, v18\n"
"v_max3_f32 v18, v100, v101, v18\n"
"v_max3_f32 v18, v102, v103, v18\n"
"s_waitcnt lgkmcnt(2)\n"
"v_mfma_f32_32x32x16_bf16 v[112:127], a[200:203], v[40:43], v[112:127]\n"
"ds_read_b128 a[216:219], v15 offset:49152\n"
"v_max3_f32 v18, v104, v105, v18\n"
"v_max3_f32 v18, v106, v107, v18\n"
"v_max3_f32 v18, v108, v109, v18\n"
"v_max3_f32 v18, v110, v111, v18\n"
"v_mul_f32 v19, s73, v18\n"
"v_mfma_f32_32x32x16_bf16 v[112:127], a[204:207], v[44:47], v[112:127]\n"
"ds_read_b128 a[220:223], v16 offset:49152\n"
"v_floor_f32 v19, v19\n"
"v_accvgpr_write_b32 a62, v19\n"
"v_fmamk_f32 v96, v96, 0x3db8aa3b, v19\n"
"v_fmamk_f32 v97, v97, 0x3db8aa3b, v19\n"
"s_waitcnt lgkmcnt(2)\n"
"v_mfma_f32_32x32x16_bf16 v[112:127], a[208:211], v[48:51], v[112:127]\n"
"buffer_load_dwordx4 v2, s[48:51], s69 offen offset:1024 lds\n"
"ds_read_b128 a[192:195], v9 offset:49408\n"
"v_exp_f32 v96, v96\n"
"v_fmamk_f32 v98, v98, 0x3db8aa3b, v19\n"
"v_exp_f32 v97, v97\n"
"v_mfma_f32_32x32x16_bf16 v[112:127], a[212:215], v[52:55], v[112:127]\n"
"ds_read_b128 a[196:199], v10 offset:49408\n"
"v_fmamk_f32 v99, v99, 0x3db8aa3b, v19\n"
"v_exp_f32 v98, v98\n"
"v_fmamk_f32 v100, v100, 0x3db8aa3b, v19\n"
"s_waitcnt lgkmcnt(2)\n"
"v_mfma_f32_32x32x16_bf16 v[112:127], a[216:219], v[56:59], v[112:127]\n"
"ds_read_b128 a[200:203], v11 offset:49408\n"
"v_exp_f32 v99, v99\n"
"v_cvt_pk_bf16_f32 v240, v96, v97\n"
"v_fmamk_f32 v101, v101, 0x3db8aa3b, v19\n"
"v_mfma_f32_32x32x16_bf16 v[112:127], a[220:223], v[60:63], v[112:127]\n"
"ds_read_b128 a[204:207], v12 offset:49408\n"
"v_exp_f32 v100, v100\n"
"v_fmamk_f32 v102, v102, 0x3db8aa3b, v19\n"
"v_exp_f32 v101, v101\n"
"s_waitcnt lgkmcnt(2)\n"
"v_mfma_f32_32x32x16_bf16 v[112:127], a[192:195], v[64:67], v[112:127]\n"
"buffer_load_dwordx4 v3, s[48:51], s69 offen offset:2048 lds\n"
"ds_read_b128 a[208:211], v13 offset:49408\n"
"v_cvt_pk_bf16_f32 v241, v98, v99\n"
"v_fmamk_f32 v103, v103, 0x3db8aa3b, v19\n"
"v_exp_f32 v102, v102\n"
"v_fmamk_f32 v104, v104, 0x3db8aa3b, v19\n"
"v_mfma_f32_32x32x16_bf16 v[112:127], a[196:199], v[68:71], v[112:127]\n"
"ds_read_b128 a[212:215], v14 offset:49408\n"
"v_exp_f32 v103, v103\n"
"v_cvt_pk_bf16_f32 v242, v100, v101\n"
"v_fmamk_f32 v105, v105, 0x3db8aa3b, v19\n"
"s_waitcnt lgkmcnt(2)\n"
"v_mfma_f32_32x32x16_bf16 v[112:127], a[200:203], v[72:75], v[112:127]\n"
"ds_read_b128 a[216:219], v15 offset:49408\n"
"v_exp_f32 v104, v104\n"
"v_fmamk_f32 v106, v106, 0x3db8aa3b, v19\n"
"v_exp_f32 v105, v105\n"
"v_mfma_f32_32x32x16_bf16 v[112:127], a[204:207], v[76:79], v[112:127]\n"
"ds_read_b128 a[220:223], v16 offset:49408\n"
"v_cvt_pk_bf16_f32 v243, v102, v103\n"
"v_fmamk_f32 v107, v107, 0x3db8aa3b, v19\n"
"v_exp_f32 v106, v106\n"
"s_waitcnt lgkmcnt(2)\n"
"v_mfma_f32_32x32x16_bf16 v[112:127], a[208:211], v[80:83], v[112:127]\n"
"s_waitcnt vmcnt(22)\n"
"s_barrier\n"
"buffer_load_dwordx4 v4, s[48:51], s69 offen offset:3072 lds\n"
"v_xor_b32 v5, 0x10000, v5\n"
"ds_read_b64_tr_b16 a[192:193], v5 offset:0\n"
"ds_read_b64_tr_b16 a[194:195], v5 offset:2048\n"
"v_fmamk_f32 v108, v108, 0x3db8aa3b, v19\n"
"v_exp_f32 v107, v107\n"
"v_cvt_pk_bf16_f32 v244, v104, v105\n"
"v_fmamk_f32 v109, v109, 0x3db8aa3b, v19\n"
"v_mfma_f32_32x32x16_bf16 v[112:127], a[212:215], v[84:87], v[112:127]\n"
"v_xor_b32 v6, 0x10000, v6\n"
"ds_read_b64_tr_b16 a[196:197], v6 offset:0\n"
"ds_read_b64_tr_b16 a[198:199], v6 offset:2048\n"
"v_exp_f32 v108, v108\n"
"v_fmamk_f32 v110, v110, 0x3db8aa3b, v19\n"
"v_exp_f32 v109, v109\n"
"s_waitcnt lgkmcnt(4)\n"
"v_mfma_f32_32x32x16_bf16 v[112:127], a[216:219], v[88:91], v[112:127]\n"
"v_xor_b32 v7, 0x10000, v7\n"
"ds_read_b64_tr_b16 a[200:201], v7 offset:0\n"
"ds_read_b64_tr_b16 a[202:203], v7 offset:2048\n"
"v_cvt_pk_bf16_f32 v245, v106, v107\n"
"v_fmamk_f32 v111, v111, 0x3db8aa3b, v19\n"
"v_exp_f32 v110, v110\n"
"v_mfma_f32_32x32x16_bf16 v[112:127], a[220:223], v[92:95], v[112:127]\n"
"v_xor_b32 v8, 0x10000, v8\n"
"ds_read_b64_tr_b16 a[204:205], v8 offset:0\n"
"ds_read_b64_tr_b16 a[206:207], v8 offset:2048\n"
"v_exp_f32 v111, v111\n"
"v_cvt_pk_bf16_f32 v246, v108, v109\n"
"v_cvt_pk_bf16_f32 v247, v110, v111\n"
"s_nop 5\n"
"v_max3_f32 v18, v112, v113, v18\n"
"v_max3_f32 v18, v114, v115, v18\n"
"v_max3_f32 v18, v116, v117, v18\n"
"v_max3_f32 v18, v118, v119, v18\n"
"v_max3_f32 v18, v120, v121, v18\n"
"v_max3_f32 v18, v122, v123, v18\n"
"v_max3_f32 v18, v124, v125, v18\n"
"v_max3_f32 v18, v126, v127, v18\n"
"v_mov_b32 v25, v18\n"
"s_nop 1\n"
"v_permlane32_swap_b32 v18, v25\n"
"v_max_f32 v18, v18, v25\n"
"v_mul_f32 v19, s73, v18\n"
"v_floor_f32 v19, v19\n"
"v_mul_f32 v22, 0xc3000000, v19\n"
"v_accvgpr_read_b32 v24, a48\n"
"v_fmamk_f32 v24, v24, 0x43000000, v22\n"
"v_min_f32 v24, 0x46ff0000, v24\n"
"v_cvt_u32_f32 v24, v24\n"
"v_pk_sub_u16 v128, v128, v24 op_sel_hi:[1,0] clamp\n"
"v_pk_sub_u16 v129, v129, v24 op_sel_hi:[1,0] clamp\n"
"v_pk_sub_u16 v130, v130, v24 op_sel_hi:[1,0] clamp\n"
"v_pk_sub_u16 v131, v131, v24 op_sel_hi:[1,0] clamp\n"
"s_waitcnt lgkmcnt(4)\n"
"s_nop 0\n"
"v_mfma_f32_32x32x16_bf16 v[32:47], a[192:195], v[128:131], 0\n"
"s_add_u32 m0, s68, 32768\n"
"s_mov_b32 s69, 98304\n"
"buffer_load_dwordx4 v1, s[48:51], s69 offen offset:0 lds\n"
"ds_read_b64_tr_b16 a[208:209], v5 offset:256\n"
"ds_read_b64_tr_b16 a[210:211], v5 offset:2304\n"
"v_pk_sub_u16 v132, v132, v24 op_sel_hi:[1,0] clamp\n"
"v_pk_sub_u16 v133, v133, v24 op_sel_hi:[1,0] clamp\n"
"v_pk_sub_u16 v134, v134, v24 op_sel_hi:[1,0] clamp\n"
"v_pk_sub_u16 v135, v135, v24 op_sel_hi:[1,0] clamp\n"
"v_mfma_f32_32x32x16_bf16 v[48:63], a[196:199], v[128:131], 0\n"
"ds_read_b64_tr_b16 a[212:213], v6 offset:256\n"
"ds_read_b64_tr_b16 a[214:215], v6 offset:2304\n"
"v_accvgpr_read_b32 v24, a49\n"
"s_waitcnt lgkmcnt(4)\n"
"v_mfma_f32_32x32x16_bf16 v[64:79], a[200:203], v[128:131], 0\n"
"ds_read_b64_tr_b16 a[216:217], v7 offset:256\n"
"ds_read_b64_tr_b16 a[218:219], v7 offset:2304\n"
"v_fmamk_f32 v24, v24, 0x43000000, v22\n"
"s_mov_b64 s[44:45], %[w2]\n"
"s_mov_b32 s46, %[n2]\n"
"s_mov_b32 s47, 0x00020000\n"
"v_mfma_f32_32x32x16_bf16 v[80:95], a[204:207], v[128:131], 0\n"
"ds_read_b64_tr_b16 a[220:221], v8 offset:256\n"
"ds_read_b64_tr_b16 a[222:223], v8 offset:2304\n"
"v_min_f32 v24, 0x46ff0000, v24\n"
"v_mfma_f32_16x16x32_bf16 a[188:191], a[176:179], v[128:131], 0\n"
"s_waitcnt lgkmcnt(4)\n"
"v_mfma_f32_32x32x16_bf16 v[96:111], a[208:211], v[128:131], 0\n"
"buffer_load_dwordx4 v2, s[48:51], s69 offen offset:1024 lds\n"
"ds_read_b64_tr_b16 a[192:193], v5 offset:8192\n"
"ds_read_b64_tr_b16 a[194:195], v5 offset:10240\n"
"v_cvt_u32_f32 v24, v24\n"
"v_mfma_f32_32x32x16_bf16 a[0:15], a[212:215], v[128:131], 0\n"
"ds_read_b64_tr_b16 a[196:197], v6 offset:8192\n"
"ds_read_b64_tr_b16 a[198:199], v6 offset:10240\n"
"v_pk_sub_u16 v136, v136, v24 op_sel_hi:[1,0] clamp\n"
"s_waitcnt lgkmcnt(4)\n"
"v_mfma_f32_32x32x16_bf16 a[16:31], a[216:219], v[128:131], 0\n"
"ds_read_b64_tr_b16 a[200:201], v7 offset:8192\n"
"ds_read_b64_tr_b16 a[202:203], v7 offset:10240\n"
"v_pk_sub_u16 v137, v137, v24 op_sel_hi:[1,0] clamp\n"
"v_mfma_f32_32x32x16_bf16 a[32:47], a[220:223], v[128:131], 0\n"
"ds_read_b64_tr_b16 a[204:205], v8 offset:8192\n"
"ds_read_b64_tr_b16 a[206:207], v8 offset:10240\n"
"s_waitcnt lgkmcnt(4)\n"
"v_mfma_f32_32x32x16_bf16 v[32:47], a[192:195], v[132:135], v[32:47]\n"
"buffer_load_dwordx4 v3, s[48:51], s69 offen offset:2048 lds\n"
"ds_read_b64_tr_b16 a[208:209], v5 offset:8448\n"
"ds_read_b64_tr_b16 a[210:211], v5 offset:10496\n"
"v_pk_sub_u16 v138, v138, v24 op_sel_hi:[1,0] clamp\n"
"s_mov_b32 s74, 14692352\n"
"buffer_store_dwordx4 a[248:251], v23, s[64:67], s74 offen offset:1024 sc1\n"
"v_mfma_f32_32x32x16_bf16 v[48:63], a[196:199], v[132:135], v[48:63]\n"
"ds_read_b64_tr_b16 a[212:213], v6 offset:8448\n"
"ds_read_b64_tr_b16 a[214:215], v6 offset:10496\n"
"v_pk_sub_u16 v139, v139, v24 op_sel_hi:[1,0] clamp\n"
"s_waitcnt lgkmcnt(4)\n"
"v_mfma_f32_32x32x16_bf16 v[64:79], a[200:203], v[132:135], v[64:79]\n"
"ds_read_b64_tr_b16 a[216:217], v7 offset:8448\n"
"ds_read_b64_tr_b16 a[218:219], v7 offset:10496\n"
"v_pk_sub_u16 v140, v140, v24 op_sel_hi:[1,0] clamp\n"
"v_mfma_f32_32x32x16_bf16 v[80:95], a[204:207], v[132:135], v[80:95]\n"
"ds_read_b64_tr_b16 a[220:221], v8 offset:8448\n"
"ds_read_b64_tr_b16 a[222:223], v8 offset:10496\n"
"v_pk_sub_u16 v141, v141, v24 op_sel_hi:[1,0] clamp\n"
"v_mfma_f32_16x16x32_bf16 a[188:191], a[176:179], v[132:135], a[188:191]\n"
"s_waitcnt lgkmcnt(4)\n"
"v_mfma_f32_32x32x16_bf16 v[96:111], a[208:211], v[132:135], v[96:111]\n"
"s_waitcnt vmcnt(22)\n"
"s_barrier\n"
"buffer_load_dwordx4 v4, s[48:51], s69 offen offset:3072 lds\n"
"ds_read_b64_tr_b16 a[192:193], v5 offset:16384\n"
"ds_read_b64_tr_b16 a[194:195], v5 offset:18432\n"
"v_pk_sub_u16 v142, v142, v24 op_sel_hi:[1,0] clamp\n"
"v_mfma_f32_32x32x16_bf16 a[0:15], a[212:215], v[132:135], a[0:15]\n"
"ds_read_b64_tr_b16 a[196:197], v6 offset:16384\n"
"ds_read_b64_tr_b16 a[198:199], v6 offset:18432\n"
"v_pk_sub_u16 v143, v143, v24 op_sel_hi:[1,0] clamp\n"
"s_waitcnt lgkmcnt(4)\n"
"v_mfma_f32_32x32x16_bf16 a[16:31], a[216:219], v[132:135], a[16:31]\n"
"ds_read_b64_tr_b16 a[200:201], v7 offset:16384\n"
"ds_read_b64_tr_b16 a[202:203], v7 offset:18432\n"
"v_mfma_f32_32x32x16_bf16 a[32:47], a[220:223], v[132:135], a[32:47]\n"
"ds_read_b64_tr_b16 a[204:205], v8 offset:16384\n"
"ds_read_b64_tr_b16 a[206:207], v8 offset:18432\n"
"s_waitcnt lgkmcnt(4)\n"
"v_mfma_f32_32x32x16_bf16 v[32:47], a[192:195], v[136:139], v[32:47]\n"
"s_add_u32 m0, s68, 49152\n"
"s_mov_b32 s69, 114688\n"
"buffer_load_dwordx4 v1, s[48:51], s69 offen offset:0 lds\n"
"ds_read_b64_tr_b16 a[208:209], v5 offset:16640\n"
"ds_read_b64_tr_b16 a[210:211], v5 offset:18688\n"
"v_fmamk_f32 v112, v112, 0x3db8aa3b, v19\n"
"v_mfma_f32_32x32x16_bf16 v[48:63], a[196:199], v[136:139], v[48:63]\n"
"ds_read_b64_tr_b16 a[212:213], v6 offset:16640\n"
"ds_read_b64_tr_b16 a[214:215], v6 offset:18688\n"
"v_accvgpr_read_b32 v24, a50\n"
"v_fmamk_f32 v113, v113, 0x3db8aa3b, v19\n"
"s_waitcnt lgkmcnt(4)\n"
"v_mfma_f32_32x32x16_bf16 v[64:79], a[200:203], v[136:139], v[64:79]\n"
"ds_read_b64_tr_b16 a[216:217], v7 offset:16640\n"
"ds_read_b64_tr_b16 a[218:219], v7 offset:18688\n"
"v_fmamk_f32 v24, v24, 0x43000000, v22\n"
"v_mfma_f32_32x32x16_bf16 v[80:95], a[204:207], v[136:139], v[80:95]\n"
"ds_read_b64_tr_b16 a[220:221], v8 offset:16640\n"
"ds_read_b64_tr_b16 a[222:223], v8 offset:18688\n"
"v_min_f32 v24, 0x46ff0000, v24\n"
"v_exp_f32 v112, v112\n"
"v_mfma_f32_16x16x32_bf16 a[188:191], a[176:179], v[136:139], a[188:191]\n"
"s_waitcnt lgkmcnt(4)\n"
"v_mfma_f32_32x32x16_bf16 v[96:111], a[208:211], v[136:139], v[96:111]\n"
"buffer_load_dwordx4 v2, s[48:51], s69 offen offset:1024 lds\n"
"ds_read_b64_tr_b16 a[192:193], v5 offset:24576\n"
"ds_read_b64_tr_b16 a[194:195], v5 offset:26624\n"
"v_cvt_u32_f32 v24, v24\n"
"v_mfma_f32_32x32x16_bf16 a[0:15], a[212:215], v[136:139], a[0:15]\n"
"ds_read_b64_tr_b16 a[196:197], v6 offset:24576\n"
"ds_read_b64_tr_b16 a[198:199], v6 offset:26624\n"
"v_pk_sub_u16 v144, v144, v24 op_sel_hi:[1,0] clamp\n"
"v_fmamk_f32 v114, v114, 0x3db8aa3b, v19\n"
"s_waitcnt lgkmcnt(4)\n"
"v_mfma_f32_32x32x16_bf16 a[16:31], a[216:219], v[136:139], a[16:31]\n"
"ds_read_b64_tr_b16 a[200:201], v7 offset:24576\n"
"ds_read_b64_tr_b16 a[202:203], v7 offset:26624\n"
"v_pk_sub_u16 v145, v145, v24 op_sel_hi:[1,0] clamp\n"
"v_exp_f32 v113, v113\n"
"v_mfma_f32_32x32x16_bf16 a[32:47], a[220:223], v[136:139], a[32:47]\n"
"ds_read_b64_tr_b16 a[204:205], v8 offset:24576\n"
"ds_read_b64_tr_b16 a[206:207], v8 offset:26624\n"
"s_waitcnt lgkmcnt(4)\n"
"v_mfma_f32_32x32x16_bf16 v[32:47], a[192:195], v[140:143], v[32:47]\n"
"buffer_load_dwordx4 v3, s[48:51], s69 offen offset:2048 lds\n"
"ds_read_b64_tr_b16 a[208:209], v5 offset:24832\n"
"ds_read_b64_tr_b16 a[210:211], v5 offset:26880\n"
"v_pk_sub_u16 v146, v146, v24 op_sel_hi:[1,0] clamp\n"
"v_fmamk_f32 v115, v115, 0x3db8aa3b, v19\n"
"v_mfma_f32_32x32x16_bf16 v[48:63], a[196:199], v[140:143], v[48:63]\n"
"ds_read_b64_tr_b16 a[212:213], v6 offset:24832\n"
"ds_read_b64_tr_b16 a[214:215], v6 offset:26880\n"
"v_pk_sub_u16 v147, v147, v24 op_sel_hi:[1,0] clamp\n"
"s_waitcnt lgkmcnt(4)\n"
"v_mfma_f32_32x32x16_bf16 v[64:79], a[200:203], v[140:143], v[64:79]\n"
"ds_read_b64_tr_b16 a[216:217], v7 offset:24832\n"
"ds_read_b64_tr_b16 a[218:219], v7 offset:26880\n"
"v_pk_sub_u16 v148, v148, v24 op_sel_hi:[1,0] clamp\n"
"v_exp_f32 v114, v114\n"
"v_mfma_f32_32x32x16_bf16 v[80:95], a[204:207], v[140:143], v[80:95]\n"
"ds_read_b64_tr_b16 a[220:221], v8 offset:24832\n"
"ds_read_b64_tr_b16 a[222:223], v8 offset:26880\n"
"v_pk_sub_u16 v149, v149, v24 op_sel_hi:[1,0] clamp\n"
"v_mfma_f32_16x16x32_bf16 a[188:191], a[176:179], v[140:143], a[188:191]\n"
"s_waitcnt lgkmcnt(4)\n"
"v_mfma_f32_32x32x16_bf16 v[96:111], a[208:211], v[140:143], v[96:111]\n"
"s_waitcnt vmcnt(21)\n"
"s_barrier\n"
"buffer_load_dwordx4 v4, s[48:51], s69 offen offset:3072 lds\n"
"ds_read_b64_tr_b16 a[192:193], v5 offset:32768\n"
"ds_read_b64_tr_b16 a[194:195], v5 offset:34816\n"
"v_pk_sub_u16 v150, v150, v24 op_sel_hi:[1,0] clamp\n"
"v_fmamk_f32 v116, v116, 0x3db8aa3b, v19\n"
"v_mfma_f32_32x32x16_bf16 a[0:15], a[212:215], v[140:143], a[0:15]\n"
"ds_read_b64_tr_b16 a[196:197], v6 offset:32768\n"
"ds_read_b64_tr_b16 a[198:199], v6 offset:34816\n"
"v_pk_sub_u16 v151, v151, v24 op_sel_hi:[1,0] clamp\n"
"v_exp_f32 v115, v115\n"
"s_waitcnt lgkmcnt(4)\n"
"v_mfma_f32_32x32x16_bf16 a[16:31], a[216:219], v[140:143], a[16:31]\n"
"ds_read_b64_tr_b16 a[200:201], v7 offset:32768\n"
"ds_read_b64_tr_b16 a[202:203], v7 offset:34816\n"
"v_mfma_f32_32x32x16_bf16 a[32:47], a[220:223], v[140:143], a[32:47]\n"
"ds_read_b64_tr_b16 a[204:205], v8 offset:32768\n"
"ds_read_b64_tr_b16 a[206:207], v8 offset:34816\n"
"v_cvt_pk_bf16_f32 v248, v112, v113\n"
"s_waitcnt lgkmcnt(4)\n"
"v_mfma_f32_32x32x16_bf16 v[32:47], a[192:195], v[144:147], v[32:47]\n"
"s_add_u32 m0, s68, 65536\n"
"s_mov_b32 s69, 131072\n"
"buffer_load_dwordx4 v1, s[48:51], s69 offen offset:0 lds\n"
"ds_read_b64_tr_b16 a[208:209], v5 offset:33024\n"
"ds_read_b64_tr_b16 a[210:211], v5 offset:35072\n"
"v_fmamk_f32 v117, v117, 0x3db8aa3b, v19\n"
"v_mfma_f32_32x32x16_bf16 v[48:63], a[196:199], v[144:147], v[48:63]\n"
"ds_read_b64_tr_b16 a[212:213], v6 offset:33024\n"
"ds_read_b64_tr_b16 a[214:215], v6 offset:35072\n"
"v_accvgpr_read_b32 v24, a51\n"
"s_waitcnt lgkmcnt(4)\n"
"v_mfma_f32_32x32x16_bf16 v[64:79], a[200:203], v[144:147], v[64:79]\n"
"ds_read_b64_tr_b16 a[216:217], v7 offset:33024\n"
"ds_read_b64_tr_b16 a[218:219], v7 offset:35072\n"
"v_fmamk_f32 v24, v24, 0x43000000, v22\n"
"v_exp_f32 v116, v116\n"
"v_mfma_f32_32x32x16_bf16 v[80:95], a[204:207], v[144:147], v[80:95]\n"
"ds_read_b64_tr_b16 a[220:221], v8 offset:33024\n"
"ds_read_b64_tr_b16 a[222:223], v8 offset:35072\n"
"v_min_f32 v24, 0x46ff0000, v24\n"
"s_lshl_b32 s75, %[T], 2\n"
"s_add_u32 s75, s75, s71\n"
"s_mul_i32 s75, s75, 14\n"
"v_min_u32 v30, 13, v0\n"
"v_add_u32 v30, s75, v30\n"
"v_lshrrev_b32 v31, 0, v30\n"
"v_mul_u32_u24 v31, 0, v31\n"
"v_lshl_add_u32 v30, v30, 7, v31\n"
"buffer_load_dword a229, v30, s[44:47], 0 offen\n"
"v_mfma_f32_16x16x32_bf16 a[188:191], a[176:179], v[144:147], a[188:191]\n"
"s_waitcnt lgkmcnt(4)\n"
"v_mfma_f32_32x32x16_bf16 v[96:111], a[208:211], v[144:147], v[96:111]\n"
"buffer_load_dwordx4 v2, s[48:51], s69 offen offset:1024 lds\n"
"ds_read_b64_tr_b16 a[192:193], v5 offset:40960\n"
"ds_read_b64_tr_b16 a[194:195], v5 offset:43008\n"
"v_cvt_u32_f32 v24, v24\n"
"v_fmamk_f32 v118, v118, 0x3db8aa3b, v19\n"
"v_mfma_f32_32x32x16_bf16 a[0:15], a[212:215], v[144:147], a[0:15]\n"
"ds_read_b64_tr_b16 a[196:197], v6 offset:40960\n"
"ds_read_b64_tr_b16 a[198:199], v6 offset:43008\n"
"v_pk_sub_u16 v152, v152, v24 op_sel_hi:[1,0] clamp\n"
"v_exp_f32 v117, v117\n"
"s_waitcnt lgkmcnt(4)\n"
"v_mfma_f32_32x32x16_bf16 a[16:31], a[216:219], v[144:147], a[16:31]\n"
"ds_read_b64_tr_b16 a[200:201], v7 offset:40960\n"
"ds_read_b64_tr_b16 a[202:203], v7 offset:43008\n"
"v_pk_sub_u16 v153, v153, v24 op_sel_hi:[1,0] clamp\n"
"v_mfma_f32_32x32x16_bf16 a[32:47], a[220:223], v[144:147], a[32:47]\n"
"ds_read_b64_tr_b16 a[204:205], v8 offset:40960\n"
"ds_read_b64_tr_b16 a[206:207], v8 offset:43008\n"
"v_cvt_pk_bf16_f32 v249, v114, v115\n"
"s_waitcnt lgkmcnt(4)\n"
"v_mfma_f32_32x32x16_bf16 v[32:47], a[192:195], v[148:151], v[32:47]\n"
"buffer_load_dwordx4 v3, s[48:51], s69 offen offset:2048 lds\n"
"ds_read_b64_tr_b16 a[208:209], v5 offset:41216\n"
"ds_read_b64_tr_b16 a[210:211], v5 offset:43264\n"
"v_pk_sub_u16 v154, v154, v24 op_sel_hi:[1,0] clamp\n"
"v_fmamk_f32 v119, v119, 0x3db8aa3b, v19\n"
"v_mfma_f32_32x32x16_bf16 v[48:63], a[196:199], v[148:151], v[48:63]\n"
"ds_read_b64_tr_b16 a[212:213], v6 offset:41216\n"
"ds_read_b64_tr_b16 a[214:215], v6 offset:43264\n"
"v_pk_sub_u16 v155, v155, v24 op_sel_hi:[1,0] clamp\n"
"s_waitcnt lgkmcnt(4)\n"
"v_mfma_f32_32x32x16_bf16 v[64:79], a[200:203], v[148:151], v[64:79]\n"
"ds_read_b64_tr_b16 a[216:217], v7 offset:41216\n"
"ds_read_b64_tr_b16 a[218:219], v7 offset:43264\n"
"v_pk_sub_u16 v156, v156, v24 op_sel_hi:[1,0] clamp\n"
"v_exp_f32 v118, v118\n"
"s_mov_b32 s74, 14696448\n"
"buffer_store_dwordx4 a[248:251], v23, s[64:67], s74 offen offset:0 sc1\n"
"v_mfma_f32_32x32x16_bf16 v[80:95], a[204:207], v[148:151], v[80:95]\n"
"ds_read_b64_tr_b16 a[220:221], v8 offset:41216\n"
"ds_read_b64_tr_b16 a[222:223], v8 offset:43264\n"
"v_pk_sub_u16 v157, v157, v24 op_sel_hi:[1,0] clamp\n"
"v_mfma_f32_16x16x32_bf16 a[188:191], a[176:179], v[148:151], a[188:191]\n"
"s_waitcnt lgkmcnt(4)\n"
"v_mfma_f32_32x32x16_bf16 v[96:111], a[208:211], v[148:151], v[96:111]\n"
"s_waitcnt vmcnt(23)\n"
"s_barrier\n"
"buffer_load_dwordx4 v4, s[48:51], s69 offen offset:3072 lds\n"
"ds_read_b64_tr_b16 a[192:193], v5 offset:49152\n"
"ds_read_b64_tr_b16 a[194:195], v5 offset:51200\n"
"v_pk_sub_u16 v158, v158, v24 op_sel_hi:[1,0] clamp\n"
"v_fmamk_f32 v120, v120, 0x3db8aa3b, v19\n"
"v_mfma_f32_32x32x16_bf16 a[0:15], a[212:215], v[148:151], a[0:15]\n"
"ds_read_b64_tr_b16 a[196:197], v6 offset:49152\n"
"ds_read_b64_tr_b16 a[198:199], v6 offset:51200\n"
"v_pk_sub_u16 v159, v159, v24 op_sel_hi:[1,0] clamp\n"
"v_exp_f32 v119, v119\n"
"s_waitcnt lgkmcnt(4)\n"
"v_mfma_f32_32x32x16_bf16 a[16:31], a[216:219], v[148:151], a[16:31]\n"
"ds_read_b64_tr_b16 a[200:201], v7 offset:49152\n"
"ds_read_b64_tr_b16 a[202:203], v7 offset:51200\n"
"v_mfma_f32_32x32x16_bf16 a[32:47], a[220:223], v[148:151], a[32:47]\n"
"ds_read_b64_tr_b16 a[204:205], v8 offset:49152\n"
"ds_read_b64_tr_b16 a[206:207], v8 offset:51200\n"
"v_cvt_pk_bf16_f32 v250, v116, v117\n"
"s_waitcnt lgkmcnt(4)\n"
"v_mfma_f32_32x32x16_bf16 v[32:47], a[192:195], v[152:155], v[32:47]\n"
"s_add_u32 m0, s68, 81920\n"
"s_mov_b32 s69, 147456\n"
"buffer_load_dwordx4 v1, s[48:51], s69 offen offset:0 lds\n"
"ds_read_b64_tr_b16 a[208:209], v5 offset:49408\n"
"ds_read_b64_tr_b16 a[210:211], v5 offset:51456\n"
"v_fmamk_f32 v121, v121, 0x3db8aa3b, v19\n"
"v_mfma_f32_32x32x16_bf16 v[48:63], a[196:199], v[152:155], v[48:63]\n"
"ds_read_b64_tr_b16 a[212:213], v6 offset:49408\n"
"ds_read_b64_tr_b16 a[214:215], v6 offset:51456\n"
"v_accvgpr_read_b32 v24, a52\n"
"s_waitcnt lgkmcnt(4)\n"
"v_mfma_f32_32x32x16_bf16 v[64:79], a[200:203], v[152:155], v[64:79]\n"
"ds_read_b64_tr_b16 a[216:217], v7 offset:49408\n"
"ds_read_b64_tr_b16 a[218:219], v7 offset:51456\n"
"v_fmamk_f32 v24, v24, 0x43000000, v22\n"
"v_exp_f32 v120, v120\n"
"v_mfma_f32_32x32x16_bf16 v[80:95], a[204:207], v[152:155], v[80:95]\n"
"ds_read_b64_tr_b16 a[220:221], v8 offset:49408\n"
"ds_read_b64_tr_b16 a[222:223], v8 offset:51456\n"
"v_min_f32 v24, 0x46ff0000, v24\n"
"v_mfma_f32_16x16x32_bf16 a[188:191], a[176:179], v[152:155], a[188:191]\n"
"s_waitcnt lgkmcnt(4)\n"
"v_mfma_f32_32x32x16_bf16 v[96:111], a[208:211], v[152:155], v[96:111]\n"
"buffer_load_dwordx4 v2, s[48:51], s69 offen offset:1024 lds\n"
"ds_read_b64_tr_b16 a[192:193], v5 offset:57344\n"
"ds_read_b64_tr_b16 a[194:195], v5 offset:59392\n"
"v_cvt_u32_f32 v24, v24\n"
"v_fmamk_f32 v122, v122, 0x3db8aa3b, v19\n"
"v_mfma_f32_32x32x16_bf16 a[0:15], a[212:215], v[152:155], a[0:15]\n"
"ds_read_b64_tr_b16 a[196:197], v6 offset:57344\n"
"ds_read_b64_tr_b16 a[198:199], v6 offset:59392\n"
"v_pk_sub_u16 v160, v160, v24 op_sel_hi:[1,0] clamp\n"
"v_exp_f32 v121, v121\n"
"s_waitcnt lgkmcnt(4)\n"
"v_mfma_f32_32x32x16_bf16 a[16:31], a[216:219], v[152:155], a[16:31]\n"
"ds_read_b64_tr_b16 a[200:201], v7 offset:57344\n"
"ds_read_b64_tr_b16 a[202:203], v7 offset:59392\n"
"v_pk_sub_u16 v161, v161, v24 op_sel_hi:[1,0] clamp\n"
"v_mfma_f32_32x32x16_bf16 a[32:47], a[220:223], v[152:155], a[32:47]\n"
"ds_read_b64_tr_b16 a[204:205], v8 offset:57344\n"
"ds_read_b64_tr_b16 a[206:207], v8 offset:59392\n"
"v_cvt_pk_bf16_f32 v251, v118, v119\n"
"s_waitcnt lgkmcnt(4)\n"
"v_mfma_f32_32x32x16_bf16 v[32:47], a[192:195], v[156:159], v[32:47]\n"
"buffer_load_dwordx4 v3, s[48:51], s69 offen offset:2048 lds\n"
"ds_read_b64_tr_b16 a[208:209], v5 offset:57600\n"
"ds_read_b64_tr_b16 a[210:211], v5 offset:59648\n"
"v_pk_sub_u16 v162, v162, v24 op_sel_hi:[1,0] clamp\n"
"v_fmamk_f32 v123, v123, 0x3db8aa3b, v19\n"
"v_mfma_f32_32x32x16_bf16 v[48:63], a[196:199], v[156:159], v[48:63]\n"
"ds_read_b64_tr_b16 a[212:213], v6 offset:57600\n"
"ds_read_b64_tr_b16 a[214:215], v6 offset:59648\n"
"v_pk_sub_u16 v163, v163, v24 op_sel_hi:[1,0] clamp\n"
"s_waitcnt lgkmcnt(4)\n"
"v_mfma_f32_32x32x16_bf16 v[64:79], a[200:203], v[156:159], v[64:79]\n"
"ds_read_b64_tr_b16 a[216:217], v7 offset:57600\n"
"ds_read_b64_tr_b16 a[218:219], v7 offset:59648\n"
"v_pk_sub_u16 v164, v164, v24 op_sel_hi:[1,0] clamp\n"
"v_exp_f32 v122, v122\n"
"v_mfma_f32_32x32x16_bf16 v[80:95], a[204:207], v[156:159], v[80:95]\n"
"ds_read_b64_tr_b16 a[220:221], v8 offset:57600\n"
"ds_read_b64_tr_b16 a[222:223], v8 offset:59648\n"
"v_pk_sub_u16 v165, v165, v24 op_sel_hi:[1,0] clamp\n"
"v_mfma_f32_16x16x32_bf16 a[188:191], a[176:179], v[156:159], a[188:191]\n"
"s_waitcnt lgkmcnt(4)\n"
"v_mfma_f32_32x32x16_bf16 v[96:111], a[208:211], v[156:159], v[96:111]\n"
"s_waitcnt vmcnt(22)\n"
"s_barrier\n"
"buffer_load_dwordx4 v4, s[48:51], s69 offen offset:3072 lds\n"
"v_xor_b32 v5, 0x10000, v5\n"
"ds_read_b64_tr_b16 a[192:193], v5 offset:0\n"
"ds_read_b64_tr_b16 a[194:195], v5 offset:2048\n"
"v_pk_sub_u16 v166, v166, v24 op_sel_hi:[1,0] clamp\n"
"v_fmamk_f32 v124, v124, 0x3db8aa3b, v19\n"
"v_mfma_f32_32x32x16_bf16 a[0:15], a[212:215], v[156:159], a[0:15]\n"
"v_xor_b32 v6, 0x10000, v6\n"
"ds_read_b64_tr_b16 a[196:197], v6 offset:0\n"
"ds_read_b64_tr_b16 a[198:199], v6 offset:2048\n"
"v_pk_sub_u16 v167, v167, v24 op_sel_hi:[1,0] clamp\n"
"v_exp_f32 v123, v123\n"
"s_waitcnt lgkmcnt(4)\n"
"v_mfma_f32_32x32x16_bf16 a[16:31], a[216:219], v[156:159], a[16:31]\n"
"v_xor_b32 v7, 0x10000, v7\n"
"ds_read_b64_tr_b16 a[200:201], v7 offset:0\n"
"ds_read_b64_tr_b16 a[202:203], v7 offset:2048\n"
"v_mfma_f32_32x32x16_bf16 a[32:47], a[220:223], v[156:159], a[32:47]\n"
"v_xor_b32 v8, 0x10000, v8\n"
"ds_read_b64_tr_b16 a[204:205], v8 offset:0\n"
"ds_read_b64_tr_b16 a[206:207], v8 offset:2048\n"
"v_cvt_pk_bf16_f32 v252, v120, v121\n"
"s_waitcnt lgkmcnt(4)\n"
"v_mfma_f32_32x32x16_bf16 v[32:47], a[192:195], v[160:163], v[32:47]\n"
"s_add_u32 m0, s68, 98304\n"
"s_mov_b32 s69, 163840\n"
"buffer_load_dwordx4 v1, s[48:51], s69 offen offset:0 lds\n"
"ds_read_b64_tr_b16 a[208:209], v5 offset:256\n"
"ds_read_b64_tr_b16 a[210:211], v5 offset:2304\n"
"v_fmamk_f32 v125, v125, 0x3db8aa3b, v19\n"
"v_mfma_f32_32x32x16_bf16 v[48:63], a[196:199], v[160:163], v[48:63]\n"
"ds_read_b64_tr_b16 a[212:213], v6 offset:256\n"
"ds_read_b64_tr_b16 a[214:215], v6 offset:2304\n"
"v_accvgpr_read_b32 v24, a53\n"
"s_waitcnt lgkmcnt(4)\n"
"v_mfma_f32_32x32x16_bf16 v[64:79], a[200:203], v[160:163], v[64:79]\n"
"ds_read_b64_tr_b16 a[216:217], v7 offset:256\n"
"ds_read_b64_tr_b16 a[218:219], v7 offset:2304\n"
"v_fmamk_f32 v24, v24, 0x43000000, v22\n"
"v_exp_f32 v124, v124\n"
"v_mfma_f32_32x32x16_bf16 v[80:95], a[204:207], v[160:163], v[80:95]\n"
"ds_read_b64_tr_b16 a[220:221], v8 offset:256\n"
"ds_read_b64_tr_b16 a[222:223], v8 offset:2304\n"
"v_min_f32 v24, 0x46ff0000, v24\n"
"v_mfma_f32_16x16x32_bf16 a[188:191], a[176:179], v[160:163], a[188:191]\n"
"s_waitcnt lgkmcnt(4)\n"
"v_mfma_f32_32x32x16_bf16 v[96:111], a[208:211], v[160:163], v[96:111]\n"
"buffer_load_dwordx4 v2, s[48:51], s69 offen offset:1024 lds\n"
"ds_read_b64_tr_b16 a[192:193], v5 offset:8192\n"
"ds_read_b64_tr_b16 a[194:195], v5 offset:10240\n"
"v_cvt_u32_f32 v24, v24\n"
"v_fmamk_f32 v126, v126, 0x3db8aa3b, v19\n"
"v_mfma_f32_32x32x16_bf16 a[0:15], a[212:215], v[160:163], a[0:15]\n"
"ds_read_b64_tr_b16 a[196:197], v6 offset:8192\n"
"ds_read_b64_tr_b16 a[198:199], v6 offset:10240\n"
"v_pk_sub_u16 v168, v168, v24 op_sel_hi:[1,0] clamp\n"
"v_exp_f32 v125, v125\n"
"s_waitcnt lgkmcnt(4)\n"
"v_mfma_f32_32x32x16_bf16 a[16:31], a[216:219], v[160:163], a[16:31]\n"
"ds_read_b64_tr_b16 a[200:201], v7 offset:8192\n"
"ds_read_b64_tr_b16 a[202:203], v7 offset:10240\n"
"v_pk_sub_u16 v169, v169, v24 op_sel_hi:[1,0] clamp\n"
"v_mfma_f32_32x32x16_bf16 a[32:47], a[220:223], v[160:163], a[32:47]\n"
"ds_read_b64_tr_b16 a[204:205], v8 offset:8192\n"
"ds_read_b64_tr_b16 a[206:207], v8 offset:10240\n"
"v_cvt_pk_bf16_f32 v253, v122, v123\n"
"s_waitcnt lgkmcnt(4)\n"
"v_mfma_f32_32x32x16_bf16 v[32:47], a[192:195], v[164:167], v[32:47]\n"
"buffer_load_dwordx4 v3, s[48:51], s69 offen offset:2048 lds\n"
"ds_read_b64_tr_b16 a[208:209], v5 offset:8448\n"
"ds_read_b64_tr_b16 a[210:211], v5 offset:10496\n"
"v_pk_sub_u16 v170, v170, v24 op_sel_hi:[1,0] clamp\n"
"v_fmamk_f32 v127, v127, 0x3db8aa3b, v19\n"
"v_mfma_f32_32x32x16_bf16 v[48:63], a[196:199], v[164:167], v[48:63]\n"
"ds_read_b64_tr_b16 a[212:213], v6 offset:8448\n"
"ds_read_b64_tr_b16 a[214:215], v6 offset:10496\n"
"v_pk_sub_u16 v171, v171, v24 op_sel_hi:[1,0] clamp\n"
"s_waitcnt lgkmcnt(4)\n"
"v_mfma_f32_32x32x16_bf16 v[64:79], a[200:203], v[164:167], v[64:79]\n"
"ds_read_b64_tr_b16 a[216:217], v7 offset:8448\n"
"ds_read_b64_tr_b16 a[218:219], v7 offset:10496\n"
"v_pk_sub_u16 v172, v172, v24 op_sel_hi:[1,0] clamp\n"
"v_exp_f32 v126, v126\n"
"v_mfma_f32_32x32x16_bf16 v[80:95], a[204:207], v[164:167], v[80:95]\n"
"ds_read_b64_tr_b16 a[220:221], v8 offset:8448\n"
"ds_read_b64_tr_b16 a[222:223], v8 offset:10496\n"
"v_pk_sub_u16 v173, v173, v24 op_sel_hi:[1,0] clamp\n"
"v_mfma_f32_16x16x32_bf16 a[188:191], a[176:179], v[164:167], a[188:191]\n"
"s_waitcnt lgkmcnt(4)\n"
"v_mfma_f32_32x32x16_bf16 v[96:111], a[208:211], v[164:167], v[96:111]\n"
"s_waitcnt vmcnt(22)\n"
"s_barrier\n"
"buffer_load_dwordx4 v4, s[48:51], s69 offen offset:3072 lds\n"
"ds_read_b64_tr_b16 a[192:193], v5 offset:16384\n"
"ds_read_b64_tr_b16 a[194:195], v5 offset:18432\n"
"v_pk_sub_u16 v174, v174, v24 op_sel_hi:[1,0] clamp\n"
"v_exp_f32 v127, v127\n"
"s_mov_b32 s74, 14696448\n"
"buffer_store_dwordx4 a[248:251], v23, s[64:67], s74 offen offset:1024 sc1\n"
"v_mfma_f32_32x32x16_bf16 a[0:15], a[212:215], v[164:167], a[0:15]\n"
"ds_read_b64_tr_b16 a[196:197], v6 offset:16384\n"
"ds_read_b64_tr_b16 a[198:199], v6 offset:18432\n"
"v_pk_sub_u16 v175, v175, v24 op_sel_hi:[1,0] clamp\n"
"s_waitcnt lgkmcnt(4)\n"
"v_mfma_f32_32x32x16_bf16 a[16:31], a[216:219], v[164:167], a[16:31]\n"
"ds_read_b64_tr_b16 a[200:201], v7 offset:16384\n"
"ds_read_b64_tr_b16 a[202:203], v7 offset:18432\n"
"v_cvt_pk_bf16_f32 v254, v124, v125\n"
"v_mfma_f32_32x32x16_bf16 a[32:47], a[220:223], v[164:167], a[32:47]\n"
"ds_read_b64_tr_b16 a[204:205], v8 offset:16384\n"
"ds_read_b64_tr_b16 a[206:207], v8 offset:18432\n"
"v_cvt_pk_bf16_f32 v255, v126, v127\n"
"s_waitcnt lgkmcnt(4)\n"
"v_mfma_f32_32x32x16_bf16 v[32:47], a[192:195], v[168:171], v[32:47]\n"
"s_add_u32 m0, s68, 114688\n"
"s_mov_b32 s69, 180224\n"
"buffer_load_dwordx4 v1, s[48:51], s69 offen offset:0 lds\n"
"ds_read_b64_tr_b16 a[208:209], v5 offset:16640\n"
"ds_read_b64_tr_b16 a[210:211], v5 offset:18688\n"
"v_mfma_f32_32x32x16_bf16 v[48:63], a[196:199], v[168:171], v[48:63]\n"
"ds_read_b64_tr_b16 a[212:213], v6 offset:16640\n"
"ds_read_b64_tr_b16 a[214:215], v6 offset:18688\n"
"v_accvgpr_read_b32 v24, a54\n"
"s_waitcnt lgkmcnt(4)\n"
"v_mfma_f32_32x32x16_bf16 v[64:79], a[200:203], v[168:171], v[64:79]\n"
"ds_read_b64_tr_b16 a[216:217], v7 offset:16640\n"
"ds_read_b64_tr_b16 a[218:219], v7 offset:18688\n"
"v_fmamk_f32 v24, v24, 0x43000000, v22\n"
"v_mfma_f32_32x32x16_bf16 v[80:95], a[204:207], v[168:171], v[80:95]\n"
"ds_read_b64_tr_b16 a[220:221], v8 offset:16640\n"
"ds_read_b64_tr_b16 a[222:223], v8 offset:18688\n"
"v_min_f32 v24, 0x46ff0000, v24\n"
"v_mfma_f32_16x16x32_bf16 a[188:191], a[176:179], v[168:171], a[188:191]\n"
"s_waitcnt lgkmcnt(4)\n"
"v_mfma_f32_32x32x16_bf16 v[96:111], a[208:211], v[168:171], v[96:111]\n"
"buffer_load_dwordx4 v2, s[48:51], s69 offen offset:1024 lds\n"
"ds_read_b64_tr_b16 a[192:193], v5 offset:24576\n"
"ds_read_b64_tr_b16 a[194:195], v5 offset:26624\n"
"v_cvt_u32_f32 v24, v24\n"
"v_mfma_f32_32x32x16_bf16 a[0:15], a[212:215], v[168:171], a[0:15]\n"
"ds_read_b64_tr_b16 a[196:197], v6 offset:24576\n"
"ds_read_b64_tr_b16 a[198:199], v6 offset:26624\n"
"v_pk_sub_u16 v176, v176, v24 op_sel_hi:[1,0] clamp\n"
"s_waitcnt lgkmcnt(4)\n"
"v_mfma_f32_32x32x16_bf16 a[16:31], a[216:219], v[168:171], a[16:31]\n"
"ds_read_b64_tr_b16 a[200:201], v7 offset:24576\n"
"ds_read_b64_tr_b16 a[202:203], v7 offset:26624\n"
"v_pk_sub_u16 v177, v177, v24 op_sel_hi:[1,0] clamp\n"
"v_mfma_f32_32x32x16_bf16 a[32:47], a[220:223], v[168:171], a[32:47]\n"
"ds_read_b64_tr_b16 a[204:205], v8 offset:24576\n"
"ds_read_b64_tr_b16 a[206:207], v8 offset:26624\n"
"s_waitcnt lgkmcnt(4)\n"
"v_mfma_f32_32x32x16_bf16 v[32:47], a[192:195], v[172:175], v[32:47]\n"
"buffer_load_dwordx4 v3, s[48:51], s69 offen offset:2048 lds\n"
"ds_read_b64_tr_b16 a[208:209], v5 offset:24832\n"
"ds_read_b64_tr_b16 a[210:211], v5 offset:26880\n"
"v_pk_sub_u16 v178, v178, v24 op_sel_hi:[1,0] clamp\n"
"v_mfma_f32_32x32x16_bf16 v[48:63], a[196:199], v[172:175], v[48:63]\n"
"ds_read_b64_tr_b16 a[212:213], v6 offset:24832\n"
"ds_read_b64_tr_b16 a[214:215], v6 offset:26880\n"
"v_pk_sub_u16 v179, v179, v24 op_sel_hi:[1,0] clamp\n"
"s_waitcnt lgkmcnt(4)\n"
"v_mfma_f32_32x32x16_bf16 v[64:79], a[200:203], v[172:175], v[64:79]\n"
"ds_read_b64_tr_b16 a[216:217], v7 offset:24832\n"
"ds_read_b64_tr_b16 a[218:219], v7 offset:26880\n"
"v_pk_sub_u16 v180, v180, v24 op_sel_hi:[1,0] clamp\n"
"v_mfma_f32_32x32x16_bf16 v[80:95], a[204:207], v[172:175], v[80:95]\n"
"ds_read_b64_tr_b16 a[220:221], v8 offset:24832\n"
"ds_read_b64_tr_b16 a[222:223], v8 offset:26880\n"
"v_pk_sub_u16 v181, v181, v24 op_sel_hi:[1,0] clamp\n"
"v_mfma_f32_16x16x32_bf16 a[188:191], a[176:179], v[172:175], a[188:191]\n"
"s_waitcnt lgkmcnt(4)\n"
"v_mfma_f32_32x32x16_bf16 v[96:111], a[208:211], v[172:175], v[96:111]\n"
"s_waitcnt vmcnt(22)\n"
"s_barrier\n"
"buffer_load_dwordx4 v4, s[48:51], s69 offen offset:3072 lds\n"
"ds_read_b64_tr_b16 a[192:193], v5 offset:32768\n"
"ds_read_b64_tr_b16 a[194:195], v5 offset:34816\n"
"v_pk_sub_u16 v182, v182, v24 op_sel_hi:[1,0] clamp\n"
"v_mfma_f32_32x32x16_bf16 a[0:15], a[212:215], v[172:175], a[0:15]\n"
"ds_read_b64_tr_b16 a[196:197], v6 offset:32768\n"
"ds_read_b64_tr_b16 a[198:199], v6 offset:34816\n"
"v_pk_sub_u16 v183, v183, v24 op_sel_hi:[1,0] clamp\n"
"s_waitcnt lgkmcnt(4)\n"
"v_mfma_f32_32x32x16_bf16 a[16:31], a[216:219], v[172:175], a[16:31]\n"
"ds_read_b64_tr_b16 a[200:201], v7 offset:32768\n"
"ds_read_b64_tr_b16 a[202:203], v7 offset:34816\n"
"v_mfma_f32_32x32x16_bf16 a[32:47], a[220:223], v[172:175], a[32:47]\n"
"ds_read_b64_tr_b16 a[204:205], v8 offset:32768\n"
"ds_read_b64_tr_b16 a[206:207], v8 offset:34816\n"
"s_waitcnt lgkmcnt(4)\n"
"v_mfma_f32_32x32x16_bf16 v[32:47], a[192:195], v[176:179], v[32:47]\n"
"s_add_u32 m0, s68, 0\n"
"s_mov_b32 s69, 196608\n"
"buffer_load_dwordx4 v1, s[48:51], s69 offen offset:0 lds\n"
"ds_read_b64_tr_b16 a[208:209], v5 offset:33024\n"
"ds_read_b64_tr_b16 a[210:211], v5 offset:35072\n"
"v_lshrrev_b32 v30, 5, v0\n"
"v_mfma_f32_32x32x16_bf16 v[48:63], a[196:199], v[176:179], v[48:63]\n"
"ds_read_b64_tr_b16 a[212:213], v6 offset:33024\n"
"ds_read_b64_tr_b16 a[214:215], v6 offset:35072\n"
"v_accvgpr_read_b32 v24, a55\n"
"s_waitcnt lgkmcnt(4)\n"
"v_mfma_f32_32x32x16_bf16 v[64:79], a[200:203], v[176:179], v[64:79]\n"
"ds_read_b64_tr_b16 a[216:217], v7 offset:33024\n"
"ds_read_b64_tr_b16 a[218:219], v7 offset:35072\n"
"v_fmamk_f32 v24, v24, 0x43000000, v22\n"
"v_bfe_u32 v29, v0, 2, 2\n"
"v_mfma_f32_32x32x16_bf16 v[80:95], a[204:207], v[176:179], v[80:95]\n"
"ds_read_b64_tr_b16 a[220:221], v8 offset:33024\n"
"ds_read_b64_tr_b16 a[222:223], v8 offset:35072\n"
"v_min_f32 v24, 0x46ff0000, v24\n"
"v_bfe_u32 v26, v0, 4, 1\n"
"v_mfma_f32_16x16x32_bf16 a[188:191], a[176:179], v[176:179], a[188:191]\n"
"s_waitcnt lgkmcnt(4)\n"
"v_mfma_f32_32x32x16_bf16 v[96:111], a[208:211], v[176:179], v[96:111]\n"
"buffer_load_dwordx4 v2, s[48:51], s69 offen offset:1024 lds\n"
"ds_read_b64_tr_b16 a[192:193], v5 offset:40960\n"
"ds_read_b64_tr_b16 a[194:195], v5 offset:43008\n"
"v_cvt_u32_f32 v24, v24\n"
"v_mfma_f32_32x32x16_bf16 a[0:15], a[212:215], v[176:179], a[0:15]\n"
"ds_read_b64_tr_b16 a[196:197], v6 offset:40960\n"
"ds_read_b64_tr_b16 a[198:199], v6 offset:43008\n"
"v_pk_sub_u16 v184, v184, v24 op_sel_hi:[1,0] clamp\n"
"v_lshlrev_b32 v26, 5, v26\n"
"s_waitcnt lgkmcnt(4)\n"
"v_mfma_f32_32x32x16_bf16 a[16:31], a[216:219], v[176:179], a[16:31]\n"
"ds_read_b64_tr_b16 a[200:201], v7 offset:40960\n"
"ds_read_b64_tr_b16 a[202:203], v7 offset:43008\n"
"v_pk_sub_u16 v185, v185, v24 op_sel_hi:[1,0] clamp\n"
"v_mfma_f32_32x32x16_bf16 a[32:47], a[220:223], v[176:179], a[32:47]\n"
"ds_read_b64_tr_b16 a[204:205], v8 offset:40960\n"
"ds_read_b64_tr_b16 a[206:207], v8 offset:43008\n"
"v_and_b32 v27, 1, v0\n"
"s_waitcnt lgkmcnt(4)\n"
"v_mfma_f32_32x32x16_bf16 v[32:47], a[192:195], v[180:183], v[32:47]\n"
"buffer_load_dwordx4 v3, s[48:51], s69 offen offset:2048 lds\n"
"ds_read_b64_tr_b16 a[208:209], v5 offset:41216\n"
"ds_read_b64_tr_b16 a[210:211], v5 offset:43264\n"
"v_pk_sub_u16 v186, v186, v24 op_sel_hi:[1,0] clamp\n"
"v_lshl_or_b32 v26, v27, 4, v26\n"
"v_mfma_f32_32x32x16_bf16 v[48:63], a[196:199], v[180:183], v[48:63]\n"
"ds_read_b64_tr_b16 a[212:213], v6 offset:41216\n"
"ds_read_b64_tr_b16 a[214:215], v6 offset:43264\n"
"v_pk_sub_u16 v187, v187, v24 op_sel_hi:[1,0] clamp\n"
"s_waitcnt lgkmcnt(4)\n"
"v_mfma_f32_32x32x16_bf16 v[64:79], a[200:203], v[180:183], v[64:79]\n"
"ds_read_b64_tr_b16 a[216:217], v7 offset:41216\n"
"ds_read_b64_tr_b16 a[218:219], v7 offset:43264\n"
"v_pk_sub_u16 v188, v188, v24 op_sel_hi:[1,0] clamp\n"
"v_bfe_u32 v27, v0, 1, 1\n"
"v_mfma_f32_32x32x16_bf16 v[80:95], a[204:207], v[180:183], v[80:95]\n"
"ds_read_b64_tr_b16 a[220:221], v8 offset:41216\n"
"ds_read_b64_tr_b16 a[222:223], v8 offset:43264\n"
"v_pk_sub_u16 v189, v189, v24 op_sel_hi:[1,0] clamp\n"
"v_lshl_or_b32 v26, v27, 3, v26\n"
"v_mfma_f32_16x16x32_bf16 a[188:191], a[176:179], v[180:183], a[188:191]\n"
"s_waitcnt lgkmcnt(4)\n"
"v_mfma_f32_32x32x16_bf16 v[96:111], a[208:211], v[180:183], v[96:111]\n"
"s_waitcnt vmcnt(22)\n"
"s_barrier\n"
"buffer_load_dwordx4 v4, s[48:51], s69 offen offset:3072 lds\n"
"ds_read_b64_tr_b16 a[192:193], v5 offset:49152\n"
"ds_read_b64_tr_b16 a[194:195], v5 offset:51200\n"
"v_pk_sub_u16 v190, v190, v24 op_sel_hi:[1,0] clamp\n"
"v_mfma_f32_32x32x16_bf16 a[0:15], a[212:215], v[180:183], a[0:15]\n"
"ds_read_b64_tr_b16 a[196:197], v6 offset:49152\n"
"ds_read_b64_tr_b16 a[198:199], v6 offset:51200\n"
"v_pk_sub_u16 v191, v191, v24 op_sel_hi:[1,0] clamp\n"
"v_lshl_add_u32 v27, v30, 3, v29\n"
"s_waitcnt lgkmcnt(4)\n"
"v_mfma_f32_32x32x16_bf16 a[16:31], a[216:219], v[180:183], a[16:31]\n"
"ds_read_b64_tr_b16 a[200:201], v7 offset:49152\n"
"ds_read_b64_tr_b16 a[202:203], v7 offset:51200\n"
"v_lshl_add_u32 v28, v27, 7, v26\n"
"s_mov_b32 s74, 14700544\n"
"buffer_store_dwordx4 a[248:251], v23, s[64:67], s74 offen offset:0 sc1\n"
"v_mfma_f32_32x32x16_bf16 a[32:47], a[220:223], v[180:183], a[32:47]\n"
"ds_read_b64_tr_b16 a[204:205], v8 offset:49152\n"
"ds_read_b64_tr_b16 a[206:207], v8 offset:51200\n"
"s_waitcnt lgkmcnt(4)\n"
"v_mfma_f32_32x32x16_bf16 v[32:47], a[192:195], v[184:187], v[32:47]\n"
"s_add_u32 m0, s68, 16384\n"
"s_mov_b32 s69, 212992\n"
"buffer_load_dwordx4 v1, s[48:51], s69 offen offset:0 lds\n"
"ds_read_b64_tr_b16 a[208:209], v5 offset:49408\n"
"ds_read_b64_tr_b16 a[210:211], v5 offset:51456\n"
"v_lshl_add_u32 v28, %[ss], 13, v28\n"
"v_mfma_f32_32x32x16_bf16 v[48:63], a[196:199], v[184:187], v[48:63]\n"
"ds_read_b64_tr_b16 a[212:213], v6 offset:49408\n"
"ds_read_b64_tr_b16 a[214:215], v6 offset:51456\n"
"v_accvgpr_read_b32 v24, a56\n"
"v_lshrrev_b32 v29, 1, v29\n"
"s_waitcnt lgkmcnt(4)\n"
"v_mfma_f32_32x32x16_bf16 v[64:79], a[200:203], v[184:187], v[64:79]\n"
"ds_read_b64_tr_b16 a[216:217], v7 offset:49408\n"
"ds_read_b64_tr_b16 a[218:219], v7 offset:51456\n"
"v_fmamk_f32 v24, v24, 0x43000000, v22\n"
"v_mfma_f32_32x32x16_bf16 v[80:95], a[204:207], v[184:187], v[80:95]\n"
"ds_read_b64_tr_b16 a[220:221], v8 offset:49408\n"
"ds_read_b64_tr_b16 a[222:223], v8 offset:51456\n"
"v_min_f32 v24, 0x46ff0000, v24\n"
"v_xor_b32 v29, %[ss], v29\n"
"v_mfma_f32_16x16x32_bf16 a[188:191], a[176:179], v[184:187], a[188:191]\n"
"s_waitcnt lgkmcnt(4)\n"
"v_mfma_f32_32x32x16_bf16 v[96:111], a[208:211], v[184:187], v[96:111]\n"
"buffer_load_dwordx4 v2, s[48:51], s69 offen offset:1024 lds\n"
"ds_read_b64_tr_b16 a[192:193], v5 offset:57344\n"
"ds_read_b64_tr_b16 a[194:195], v5 offset:59392\n"
"v_cvt_u32_f32 v24, v24\n"
"v_mfma_f32_32x32x16_bf16 a[0:15], a[212:215], v[184:187], a[0:15]\n"
"ds_read_b64_tr_b16 a[196:197], v6 offset:57344\n"
"ds_read_b64_tr_b16 a[198:199], v6 offset:59392\n"
"v_pk_sub_u16 v192, v192, v24 op_sel_hi:[1,0] clamp\n"
"v_lshl_add_u32 v10, v29, 6, v28\n"
"s_waitcnt lgkmcnt(4)\n"
"v_mfma_f32_32x32x16_bf16 a[16:31], a[216:219], v[184:187], a[16:31]\n"
"ds_read_b64_tr_b16 a[200:201], v7 offset:57344\n"
"ds_read_b64_tr_b16 a[202:203], v7 offset:59392\n"
"v_pk_sub_u16 v193, v193, v24 op_sel_hi:[1,0] clamp\n"
"v_xor_b32 v29, 1, v29\n"
"v_mfma_f32_32x32x16_bf16 a[32:47], a[220:223], v[184:187], a[32:47]\n"
"ds_read_b64_tr_b16 a[204:205], v8 offset:57344\n"
"ds_read_b64_tr_b16 a[206:207], v8 offset:59392\n"
"s_waitcnt lgkmcnt(4)\n"
"v_mfma_f32_32x32x16_bf16 v[32:47], a[192:195], v[188:191], v[32:47]\n"
"buffer_load_dwordx4 v3, s[48:51], s69 offen offset:2048 lds\n"
"ds_read_b64_tr_b16 a[208:209], v5 offset:57600\n"
"ds_read_b64_tr_b16 a[210:211], v5 offset:59648\n"
"v_pk_sub_u16 v194, v194, v24 op_sel_hi:[1,0] clamp\n"
"v_lshl_add_u32 v9, v29, 6, v28\n"
"v_mfma_f32_32x32x16_bf16 v[48:63], a[196:199], v[188:191], v[48:63]\n"
"ds_read_b64_tr_b16 a[212:213], v6 offset:57600\n"
"ds_read_b64_tr_b16 a[214:215], v6 offset:59648\n"
"v_pk_sub_u16 v195, v195, v24 op_sel_hi:[1,0] clamp\n"
"s_waitcnt lgkmcnt(4)\n"
"v_mfma_f32_32x32x16_bf16 v[64:79], a[200:203], v[188:191], v[64:79]\n"
"ds_read_b64_tr_b16 a[216:217], v7 offset:57600\n"
"ds_read_b64_tr_b16 a[218:219], v7 offset:59648\n"
"v_pk_sub_u16 v196, v196, v24 op_sel_hi:[1,0] clamp\n"
"v_mfma_f32_32x32x16_bf16 v[80:95], a[204:207], v[188:191], v[80:95]\n"
"ds_read_b64_tr_b16 a[220:221], v8 offset:57600\n"
"ds_read_b64_tr_b16 a[222:223], v8 offset:59648\n"
"v_pk_sub_u16 v197, v197, v24 op_sel_hi:[1,0] clamp\n"
"v_mfma_f32_16x16x32_bf16 a[188:191], a[176:179], v[188:191], a[188:191]\n"
"s_waitcnt lgkmcnt(4)\n"
"v_mfma_f32_32x32x16_bf16 v[96:111], a[208:211], v[188:191], v[96:111]\n"
"s_waitcnt vmcnt(21)\n"
"s_barrier\n"
"buffer_load_dwordx4 v4, s[48:51], s69 offen offset:3072 lds\n"
"v_xor_b32 v5, 0x10000, v5\n"
"ds_read_b64_tr_b16 a[192:193], v5 offset:0\n"
"ds_read_b64_tr_b16 a[194:195], v5 offset:2048\n"
"v_pk_sub_u16 v198, v198, v24 op_sel_hi:[1,0] clamp\n"
"v_mfma_f32_32x32x16_bf16 a[0:15], a[212:215], v[188:191], a[0:15]\n"
"v_xor_b32 v6, 0x10000, v6\n"
"ds_read_b64_tr_b16 a[196:197], v6 offset:0\n"
"ds_read_b64_tr_b16 a[198:199], v6 offset:2048\n"
"v_pk_sub_u16 v199, v199, v24 op_sel_hi:[1,0] clamp\n"
"s_waitcnt lgkmcnt(4)\n"
"v_mfma_f32_32x32x16_bf16 a[16:31], a[216:219], v[188:191], a[16:31]\n"
"v_xor_b32 v7, 0x10000, v7\n"
"ds_read_b64_tr_b16 a[200:201], v7 offset:0\n"
"ds_read_b64_tr_b16 a[202:203], v7 offset:2048\n"
"v_mfma_f32_32x32x16_bf16 a[32:47], a[220:223], v[188:191], a[32:47]\n"
"v_xor_b32 v8, 0x10000, v8\n"
"ds_read_b64_tr_b16 a[204:205], v8 offset:0\n"
"ds_read_b64_tr_b16 a[206:207], v8 offset:2048\n"
"s_waitcnt lgkmcnt(4)\n"
"v_mfma_f32_32x32x16_bf16 v[32:47], a[192:195], v[192:195], v[32:47]\n"
"s_add_u32 m0, s68, 32768\n"
"s_mov_b32 s69, 229376\n"
"buffer_load_dwordx4 v1, s[48:51], s69 offen offset:0 lds\n"
"ds_read_b64_tr_b16 a[208:209], v5 offset:256\n"
"ds_read_b64_tr_b16 a[210:211], v5 offset:2304\n"
"v_mfma_f32_32x32x16_bf16 v[48:63], a[196:199], v[192:195], v[48:63]\n"
"ds_read_b64_tr_b16 a[212:213], v6 offset:256\n"
"ds_read_b64_tr_b16 a[214:215], v6 offset:2304\n"
"v_accvgpr_read_b32 v24, a57\n"
"s_waitcnt lgkmcnt(4)\n"
"v_mfma_f32_32x32x16_bf16 v[64:79], a[200:203], v[192:195], v[64:79]\n"
"ds_read_b64_tr_b16 a[216:217], v7 offset:256\n"
"ds_read_b64_tr_b16 a[218:219], v7 offset:2304\n"
"v_fmamk_f32 v24, v24, 0x43000000, v22\n"
"v_mfma_f32_32x32x16_bf16 v[80:95], a[204:207], v[192:195], v[80:95]\n"
"ds_read_b64_tr_b16 a[220:221], v8 offset:256\n"
"ds_read_b64_tr_b16 a[222:223], v8 offset:2304\n"
"v_min_f32 v24, 0x46ff0000, v24\n"
"v_mfma_f32_16x16x32_bf16 a[188:191], a[176:179], v[192:195], a[188:191]\n"
"s_waitcnt lgkmcnt(4)\n"
"v_mfma_f32_32x32x16_bf16 v[96:111], a[208:211], v[192:195], v[96:111]\n"
"buffer_load_dwordx4 v2, s[48:51], s69 offen offset:1024 lds\n"
"ds_read_b64_tr_b16 a[192:193], v5 offset:8192\n"
"ds_read_b64_tr_b16 a[194:195], v5 offset:10240\n"
"v_cvt_u32_f32 v24, v24\n"
"v_mfma_f32_32x32x16_bf16 a[0:15], a[212:215], v[192:195], a[0:15]\n"
"ds_read_b64_tr_b16 a[196:197], v6 offset:8192\n"
"ds_read_b64_tr_b16 a[198:199], v6 offset:10240\n"
"v_pk_sub_u16 v200, v200, v24 op_sel_hi:[1,0] clamp\n"
"s_waitcnt lgkmcnt(4)\n"
"v_mfma_f32_32x32x16_bf16 a[16:31], a[216:219], v[192:195], a[16:31]\n"
"ds_read_b64_tr_b16 a[200:201], v7 offset:8192\n"
"ds_read_b64_tr_b16 a[202:203], v7 offset:10240\n"
"v_pk_sub_u16 v201, v201, v24 op_sel_hi:[1,0] clamp\n"
"v_mfma_f32_32x32x16_bf16 a[32:47], a[220:223], v[192:195], a[32:47]\n"
"ds_read_b64_tr_b16 a[204:205], v8 offset:8192\n"
"ds_read_b64_tr_b16 a[206:207], v8 offset:10240\n"
"s_waitcnt lgkmcnt(4)\n"
"v_mfma_f32_32x32x16_bf16 v[32:47], a[192:195], v[196:199], v[32:47]\n"
"buffer_load_dwordx4 v3, s[48:51], s69 offen offset:2048 lds\n"
"ds_read_b64_tr_b16 a[208:209], v5 offset:8448\n"
"ds_read_b64_tr_b16 a[210:211], v5 offset:10496\n"
"v_pk_sub_u16 v202, v202, v24 op_sel_hi:[1,0] clamp\n"
"v_mfma_f32_32x32x16_bf16 v[48:63], a[196:199], v[196:199], v[48:63]\n"
"ds_read_b64_tr_b16 a[212:213], v6 offset:8448\n"
"ds_read_b64_tr_b16 a[214:215], v6 offset:10496\n"
"v_pk_sub_u16 v203, v203, v24 op_sel_hi:[1,0] clamp\n"
"s_waitcnt lgkmcnt(4)\n"
"v_mfma_f32_32x32x16_bf16 v[64:79], a[200:203], v[196:199], v[64:79]\n"
"ds_read_b64_tr_b16 a[216:217], v7 offset:8448\n"
"ds_read_b64_tr_b16 a[218:219], v7 offset:10496\n"
"v_pk_sub_u16 v204, v204, v24 op_sel_hi:[1,0] clamp\n"
"v_mfma_f32_32x32x16_bf16 v[80:95], a[204:207], v[196:199], v[80:95]\n"
"ds_read_b64_tr_b16 a[220:221], v8 offset:8448\n"
"ds_read_b64_tr_b16 a[222:223], v8 offset:10496\n"
"v_pk_sub_u16 v205, v205, v24 op_sel_hi:[1,0] clamp\n"
"v_mfma_f32_16x16x32_bf16 a[188:191], a[176:179], v[196:199], a[188:191]\n"
"s_waitcnt lgkmcnt(4)\n"
"v_mfma_f32_32x32x16_bf16 v[96:111], a[208:211], v[196:199], v[96:111]\n"
"s_waitcnt vmcnt(21)\n"
"s_barrier\n"
"buffer_load_dwordx4 v4, s[48:51], s69 offen offset:3072 lds\n"
"ds_read_b64_tr_b16 a[192:193], v5 offset:16384\n"
"ds_read_b64_tr_b16 a[194:195], v5 offset:18432\n"
"v_pk_sub_u16 v206, v206, v24 op_sel_hi:[1,0] clamp\n"
"s_lshr_b32 s84, %[g], 2\n"
"s_lshl_b32 s85, %[T], 1\n"
"s_add_u32 s85, s85, s84\n"
"s_mul_i32 s85, s85, 28672\n"
"s_and_b32 s84, %[g], 3\n"
"s_lshl_b32 s84, s84, 7\n"
"v_mfma_f32_32x32x16_bf16 a[0:15], a[212:215], v[196:199], a[0:15]\n"
"ds_read_b64_tr_b16 a[196:197], v6 offset:16384\n"
"ds_read_b64_tr_b16 a[198:199], v6 offset:18432\n"
"v_pk_sub_u16 v207, v207, v24 op_sel_hi:[1,0] clamp\n"
"s_add_u32 s85, s85, s84\n"
"s_add_u32 s85, s85, 131072\n"
"s_mov_b64 s[80:81], %[ws]\n"
"s_add_u32 s80, s80, s85\n"
"s_addc_u32 s81, s81, 0\n"
"s_mov_b32 s82, 0\n"
"s_mov_b32 s83, 0x00020000\n"
"s_sub_u32 s82, 28672, s84\n"
"s_waitcnt lgkmcnt(4)\n"
"v_mfma_f32_32x32x16_bf16 a[16:31], a[216:219], v[196:199], a[16:31]\n"
"ds_read_b64_tr_b16 a[200:201], v7 offset:16384\n"
"ds_read_b64_tr_b16 a[202:203], v7 offset:18432\n"
"v_lshrrev_b32 v30, 3, v0\n"
"v_and_b32 v31, 7, v0\n"
"v_lshlrev_b32 v31, 4, v31\n"
"v_lshl_add_u32 v30, v30, 9, v31\n"
"s_add_u32 s85, s68, 16384\n"
"v_mfma_f32_32x32x16_bf16 a[32:47], a[220:223], v[196:199], a[32:47]\n"
"ds_read_b64_tr_b16 a[204:205], v8 offset:16384\n"
"ds_read_b64_tr_b16 a[206:207], v8 offset:18432\n"
"buffer_store_dwordx4 a[248:251], v30, s[80:83], s68 offen sc1\n"
"buffer_store_dwordx4 a[248:251], v30, s[80:83], s85 offen sc1\n"
"s_waitcnt lgkmcnt(4)\n"
"v_mfma_f32_32x32x16_bf16 v[32:47], a[192:195], v[200:203], v[32:47]\n"
"s_add_u32 m0, s68, 49152\n"
"s_mov_b32 s69, 245760\n"
"buffer_load_dwordx4 v1, s[48:51], s69 offen offset:0 lds\n"
"ds_read_b64_tr_b16 a[208:209], v5 offset:16640\n"
"ds_read_b64_tr_b16 a[210:211], v5 offset:18688\n"
"s_mov_b32 s74, 14700544\n"
"buffer_store_dwordx4 a[248:251], v23, s[64:67], s74 offen offset:1024 sc1\n"
"v_mfma_f32_32x32x16_bf16 v[48:63], a[196:199], v[200:203], v[48:63]\n"
"ds_read_b64_tr_b16 a[212:213], v6 offset:16640\n"
"ds_read_b64_tr_b16 a[214:215], v6 offset:18688\n"
"v_accvgpr_read_b32 v24, a58\n"
"s_waitcnt lgkmcnt(4)\n"
"v_mfma_f32_32x32x16_bf16 v[64:79], a[200:203], v[200:203], v[64:79]\n"
"ds_read_b64_tr_b16 a[216:217], v7 offset:16640\n"
"ds_read_b64_tr_b16 a[218:219], v7 offset:18688\n"
"v_fmamk_f32 v24, v24, 0x43000000, v22\n"
"v_mfma_f32_32x32x16_bf16 v[80:95], a[204:207], v[200:203], v[80:95]\n"
"ds_read_b64_tr_b16 a[220:221], v8 offset:16640\n"
"ds_read_b64_tr_b16 a[222:223], v8 offset:18688\n"
"v_min_f32 v24, 0x46ff0000, v24\n"
"v_mfma_f32_16x16x32_bf16 a[188:191], a[176:179], v[200:203], a[188:191]\n"
"s_waitcnt lgkmcnt(4)\n"
"v_mfma_f32_32x32x16_bf16 v[96:111], a[208:211], v[200:203], v[96:111]\n"
"buffer_load_dwordx4 v2, s[48:51], s69 offen offset:1024 lds\n"
"ds_read_b64_tr_b16 a[192:193], v5 offset:24576\n"
"ds_read_b64_tr_b16 a[194:195], v5 offset:26624\n"
"v_cvt_u32_f32 v24, v24\n"
"v_mfma_f32_32x32x16_bf16 a[0:15], a[212:215], v[200:203], a[0:15]\n"
"ds_read_b64_tr_b16 a[196:197], v6 offset:24576\n"
"ds_read_b64_tr_b16 a[198:199], v6 offset:26624\n"
"v_pk_sub_u16 v208, v208, v24 op_sel_hi:[1,0] clamp\n"
"s_waitcnt lgkmcnt(4)\n"
"v_mfma_f32_32x32x16_bf16 a[16:31], a[216:219], v[200:203], a[16:31]\n"
"ds_read_b64_tr_b16 a[200:201], v7 offset:24576\n"
"ds_read_b64_tr_b16 a[202:203], v7 offset:26624\n"
"v_pk_sub_u16 v209, v209, v24 op_sel_hi:[1,0] clamp\n"
"v_mfma_f32_32x32x16_bf16 a[32:47], a[220:223], v[200:203], a[32:47]\n"
"ds_read_b64_tr_b16 a[204:205], v8 offset:24576\n"
"ds_read_b64_tr_b16 a[206:207], v8 offset:26624\n"
"s_waitcnt lgkmcnt(4)\n"
"v_mfma_f32_32x32x16_bf16 v[32:47], a[192:195], v[204:207], v[32:47]\n"
"buffer_load_dwordx4 v3, s[48:51], s69 offen offset:2048 lds\n"
"ds_read_b64_tr_b16 a[208:209], v5 offset:24832\n"
"ds_read_b64_tr_b16 a[210:211], v5 offset:26880\n"
"v_pk_sub_u16 v210, v210, v24 op_sel_hi:[1,0] clamp\n"
"v_mfma_f32_32x32x16_bf16 v[48:63], a[196:199], v[204:207], v[48:63]\n"
"ds_read_b64_tr_b16 a[212:213], v6 offset:24832\n"
"ds_read_b64_tr_b16 a[214:215], v6 offset:26880\n"
"v_pk_sub_u16 v211, v211, v24 op_sel_hi:[1,0] clamp\n"
"s_waitcnt lgkmcnt(4)\n"
"v_mfma_f32_32x32x16_bf16 v[64:79], a[200:203], v[204:207], v[64:79]\n"
"ds_read_b64_tr_b16 a[216:217], v7 offset:24832\n"
"ds_read_b64_tr_b16 a[218:219], v7 offset:26880\n"
"v_pk_sub_u16 v212, v212, v24 op_sel_hi:[1,0] clamp\n"
"v_mfma_f32_32x32x16_bf16 v[80:95], a[204:207], v[204:207], v[80:95]\n"
"ds_read_b64_tr_b16 a[220:221], v8 offset:24832\n"
"ds_read_b64_tr_b16 a[222:223], v8 offset:26880\n"
"v_pk_sub_u16 v213, v213, v24 op_sel_hi:[1,0] clamp\n"
"v_mfma_f32_16x16x32_bf16 a[188:191], a[176:179], v[204:207], a[188:191]\n"
"s_waitcnt lgkmcnt(4)\n"
"v_mfma_f32_32x32x16_bf16 v[96:111], a[208:211], v[204:207], v[96:111]\n"
"s_waitcnt vmcnt(24)\n"
"s_barrier\n"
"buffer_load_dwordx4 v4, s[48:51], s69 offen offset:3072 lds\n"
"ds_read_b64_tr_b16 a[192:193], v5 offset:32768\n"
"ds_read_b64_tr_b16 a[194:195], v5 offset:34816\n"
"v_pk_sub_u16 v214, v214, v24 op_sel_hi:[1,0] clamp\n"
"v_mfma_f32_32x32x16_bf16 a[0:15], a[212:215], v[204:207], a[0:15]\n"
"ds_read_b64_tr_b16 a[196:197], v6 offset:32768\n"
"ds_read_b64_tr_b16 a[198:199], v6 offset:34816\n"
"v_pk_sub_u16 v215, v215, v24 op_sel_hi:[1,0] clamp\n"
"s_waitcnt lgkmcnt(4)\n"
"v_mfma_f32_32x32x16_bf16 a[16:31], a[216:219], v[204:207], a[16:31]\n"
"ds_read_b64_tr_b16 a[200:201], v7 offset:32768\n"
"ds_read_b64_tr_b16 a[202:203], v7 offset:34816\n"
"v_mfma_f32_32x32x16_bf16 a[32:47], a[220:223], v[204:207], a[32:47]\n"
"v_lshrrev_b32 v28, 3, v0\n"
"s_lshl_b32 s75, %[ss], 5\n"
"v_add_u32 v28, s75, v28\n"
"s_lshl_b32 s75, %[rr], 8\n"
"v_add_u32 v28, s75, v28\n"
"v_bfe_u32 v29, v0, 4, 1\n"
"v_lshlrev_b32 v29, 2, v29\n"
"v_and_b32 v26, 7, v0\n"
"v_xor_b32 v29, v29, v26\n"
"ds_read_b64_tr_b16 a[204:205], v8 offset:32768\n"
"ds_read_b64_tr_b16 a[206:207], v8 offset:34816\n"
"s_waitcnt lgkmcnt(4)\n"
"v_mfma_f32_32x32x16_bf16 v[32:47], a[192:195], v[208:211], v[32:47]\n"
"v_lshlrev_b32 v29, 4, v29\n"
"v_mul_u32_u24 v28, 896, v28\n"
"v_add_u32 v1, v28, v29\n"
"v_add_u32 v2, 6144, v1\n"
"v_add_u32 v3, 12288, v1\n"
"v_add_u32 v4, 18432, v1\n"
"s_add_u32 m0, s68, 65536\n"
"s_mov_b32 s69, 0\n"
"buffer_load_dwordx4 v1, s[52:55], s69 offen offset:0 lds\n"
"ds_read_b64_tr_b16 a[208:209], v5 offset:33024\n"
"ds_read_b64_tr_b16 a[210:211], v5 offset:35072\n"
"v_mfma_f32_32x32x16_bf16 v[48:63], a[196:199], v[208:211], v[48:63]\n"
"ds_read_b64_tr_b16 a[212:213], v6 offset:33024\n"
"ds_read_b64_tr_b16 a[214:215], v6 offset:35072\n"
"v_accvgpr_read_b32 v24, a59\n"
"s_waitcnt lgkmcnt(4)\n"
"v_mfma_f32_32x32x16_bf16 v[64:79], a[200:203], v[208:211], v[64:79]\n"
"ds_read_b64_tr_b16 a[216:217], v7 offset:33024\n"
"ds_read_b64_tr_b16 a[218:219], v7 offset:35072\n"
"v_fmamk_f32 v24, v24, 0x43000000, v22\n"
"v_mfma_f32_32x32x16_bf16 v[80:95], a[204:207], v[208:211], v[80:95]\n"
"ds_read_b64_tr_b16 a[220:221], v8 offset:33024\n"
"ds_read_b64_tr_b16 a[222:223], v8 offset:35072\n"
"v_min_f32 v24, 0x46ff0000, v24\n"
"v_mfma_f32_16x16x32_bf16 a[188:191], a[176:179], v[208:211], a[188:191]\n"
"s_waitcnt lgkmcnt(4)\n"
"v_mfma_f32_32x32x16_bf16 v[96:111], a[208:211], v[208:211], v[96:111]\n"
"buffer_load_dwordx4 v2, s[52:55], s69 offen offset:1024 lds\n"
"ds_read_b64_tr_b16 a[192:193], v5 offset:40960\n"
"ds_read_b64_tr_b16 a[194:195], v5 offset:43008\n"
"v_cvt_u32_f32 v24, v24\n"
"s_add_u32 s70, s71, 0\n"
"s_min_u32 s70, s70, 13\n"
"v_mfma_f32_32x32x16_bf16 a[0:15], a[212:215], v[208:211], a[0:15]\n"
"ds_read_b64_tr_b16 a[196:197], v6 offset:40960\n"
"ds_read_b64_tr_b16 a[198:199], v6 offset:43008\n"
"v_pk_sub_u16 v216, v216, v24 op_sel_hi:[1,0] clamp\n"
"s_lshl_b32 s70, s70, 10\n"
"s_add_u32 s70, s70, 109568\n"
"buffer_load_dwordx4 v[112:115], v23, s[60:63], s70 offen sc1\n"
"s_waitcnt lgkmcnt(4)\n"
"v_mfma_f32_32x32x16_bf16 a[16:31], a[216:219], v[208:211], a[16:31]\n"
"ds_read_b64_tr_b16 a[200:201], v7 offset:40960\n"
"ds_read_b64_tr_b16 a[202:203], v7 offset:43008\n"
"v_pk_sub_u16 v217, v217, v24 op_sel_hi:[1,0] clamp\n"
"s_add_u32 s70, s71, 4\n"
"s_min_u32 s70, s70, 13\n"
"v_mfma_f32_32x32x16_bf16 a[32:47], a[220:223], v[208:211], a[32:47]\n"
"ds_read_b64_tr_b16 a[204:205], v8 offset:40960\n"
"ds_read_b64_tr_b16 a[206:207], v8 offset:43008\n"
"s_lshl_b32 s70, s70, 10\n"
"s_add_u32 s70, s70, 109568\n"
"buffer_load_dwordx4 v[116:119], v23, s[60:63], s70 offen sc1\n"
"s_waitcnt lgkmcnt(4)\n"
"v_mfma_f32_32x32x16_bf16 v[32:47], a[192:195], v[212:215], v[32:47]\n"
"buffer_load_dwordx4 v3, s[52:55], s69 offen offset:2048 lds\n"
"ds_read_b64_tr_b16 a[208:209], v5 offset:41216\n"
"ds_read_b64_tr_b16 a[210:211], v5 offset:43264\n"
"v_pk_sub_u16 v218, v218, v24 op_sel_hi:[1,0] clamp\n"
"s_add_u32 s70, s71, 8\n"
"s_min_u32 s70, s70, 13\n"
"v_mfma_f32_32x32x16_bf16 v[48:63], a[196:199], v[212:215], v[48:63]\n"
"ds_read_b64_tr_b16 a[212:213], v6 offset:41216\n"
"ds_read_b64_tr_b16 a[214:215], v6 offset:43264\n"
"v_pk_sub_u16 v219, v219, v24 op_sel_hi:[1,0] clamp\n"
"s_lshl_b32 s70, s70, 10\n"
"s_add_u32 s70, s70, 109568\n"
"buffer_load_dwordx4 v[120:123], v23, s[60:63], s70 offen sc1\n"
"s_waitcnt lgkmcnt(4)\n"
"v_mfma_f32_32x32x16_bf16 v[64:79], a[200:203], v[212:215], v[64:79]\n"
"ds_read_b64_tr_b16 a[216:217], v7 offset:41216\n"
"ds_read_b64_tr_b16 a[218:219], v7 offset:43264\n"
"v_pk_sub_u16 v220, v220, v24 op_sel_hi:[1,0] clamp\n"
"s_add_u32 s70, s71, 12\n"
"s_min_u32 s70, s70, 13\n"
"v_mfma_f32_32x32x16_bf16 v[80:95], a[204:207], v[212:215], v[80:95]\n"
"ds_read_b64_tr_b16 a[220:221], v8 offset:41216\n"
"ds_read_b64_tr_b16 a[222:223], v8 offset:43264\n"
"v_pk_sub_u16 v221, v221, v24 op_sel_hi:[1,0] clamp\n"
"s_lshl_b32 s70, s70, 10\n"
"s_add_u32 s70, s70, 109568\n"
"buffer_load_dwordx4 v[124:127], v23, s[60:63], s70 offen sc1\n"
"v_mfma_f32_16x16x32_bf16 a[188:191], a[176:179], v[212:215], a[188:191]\n"
"s_waitcnt lgkmcnt(4)\n"
"v_mfma_f32_32x32x16_bf16 v[96:111], a[208:211], v[212:215], v[96:111]\n"
"s_waitcnt vmcnt(27)\n"
"s_barrier\n"
"buffer_load_dwordx4 v4, s[52:55], s69 offen offset:3072 lds\n"
"ds_read_b64_tr_b16 a[192:193], v5 offset:49152\n"
"ds_read_b64_tr_b16 a[194:195], v5 offset:51200\n"
"v_pk_sub_u16 v222, v222, v24 op_sel_hi:[1,0] clamp\n"
"v_mov_b32 v11, s72\n"
"v_mov_b32 v29, 0x7ff00000\n"
"v_mfma_f32_32x32x16_bf16 a[0:15], a[212:215], v[212:215], a[0:15]\n"
"ds_read_b64_tr_b16 a[196:197], v6 offset:49152\n"
"ds_read_b64_tr_b16 a[198:199], v6 offset:51200\n"
"v_pk_sub_u16 v223, v223, v24 op_sel_hi:[1,0] clamp\n"
"v_cmp_eq_u32 vcc, 0, v0\n"
"v_and_b32 v26, 15, v0\n"
"s_waitcnt lgkmcnt(4)\n"
"v_mfma_f32_32x32x16_bf16 a[16:31], a[216:219], v[212:215], a[16:31]\n"
"ds_read_b64_tr_b16 a[200:201], v7 offset:49152\n"
"ds_read_b64_tr_b16 a[202:203], v7 offset:51200\n"
"v_lshlrev_b32 v26, 2, v26\n"
"s_lshl_b32 s85, %[T], 9\n"
"v_mfma_f32_32x32x16_bf16 a[32:47], a[220:223], v[212:215], a[32:47]\n"
"ds_read_b64_tr_b16 a[204:205], v8 offset:49152\n"
"ds_read_b64_tr_b16 a[206:207], v8 offset:51200\n"
"s_lshl_b32 s75, %[rr], 7\n"
"s_add_u32 s75, s75, s85\n"
"v_add_u32 v15, s75, v26\n"
"s_waitcnt lgkmcnt(4)\n"
"v_mfma_f32_32x32x16_bf16 v[32:47], a[192:195], v[216:219], v[32:47]\n"
"s_add_u32 m0, s68, 81920\n"
"s_mov_b32 s69, 57344\n"
"buffer_load_dwordx4 v1, s[52:55], s69 offen offset:0 lds\n"
"ds_read_b64_tr_b16 a[208:209], v5 offset:49408\n"
"ds_read_b64_tr_b16 a[210:211], v5 offset:51456\n"
"s_lshl_b32 s75, s71, 5\n"
"s_add_u32 s75, s75, %[g]\n"
"v_mfma_f32_32x32x16_bf16 v[48:63], a[196:199], v[216:219], v[48:63]\n"
"ds_read_b64_tr_b16 a[212:213], v6 offset:49408\n"
"ds_read_b64_tr_b16 a[214:215], v6 offset:51456\n"
"v_accvgpr_read_b32 v24, a60\n"
"s_lshl_b32 s75, s75, 2\n"
"s_add_u32 s75, s75, s85\n"
"s_waitcnt lgkmcnt(4)\n"
"v_mfma_f32_32x32x16_bf16 v[64:79], a[200:203], v[216:219], v[64:79]\n"
"ds_read_b64_tr_b16 a[216:217], v7 offset:49408\n"
"ds_read_b64_tr_b16 a[218:219], v7 offset:51456\n"
"v_fmamk_f32 v24, v24, 0x43000000, v22\n"
"s_mov_b32 s74, 14704640\n"
"buffer_store_dwordx4 a[248:251], v23, s[64:67], s74 offen offset:0 sc1\n"
"v_mov_b32 v12, s75\n"
"v_cndmask_b32 v12, v29, v12, vcc\n"
"v_mfma_f32_32x32x16_bf16 v[80:95], a[204:207], v[216:219], v[80:95]\n"
"ds_read_b64_tr_b16 a[220:221], v8 offset:49408\n"
"ds_read_b64_tr_b16 a[222:223], v8 offset:51456\n"
"v_min_f32 v24, 0x46ff0000, v24\n"
"s_lshr_b32 s84, %[g], 2\n"
"s_lshl_b32 s75, s84, 1\n"
"v_mfma_f32_16x16x32_bf16 a[188:191], a[176:179], v[216:219], a[188:191]\n"
"s_waitcnt lgkmcnt(4)\n"
"v_mfma_f32_32x32x16_bf16 v[96:111], a[208:211], v[216:219], v[96:111]\n"
"buffer_load_dwordx4 v2, s[52:55], s69 offen offset:1024 lds\n"
"ds_read_b64_tr_b16 a[192:193], v5 offset:57344\n"
"ds_read_b64_tr_b16 a[194:195], v5 offset:59392\n"
"v_cvt_u32_f32 v24, v24\n"
"s_add_u32 s75, s75, %[rr]\n"
"s_lshl_b32 s75, s75, 7\n"
"v_mfma_f32_32x32x16_bf16 a[0:15], a[212:215], v[216:219], a[0:15]\n"
"ds_read_b64_tr_b16 a[196:197], v6 offset:57344\n"
"ds_read_b64_tr_b16 a[198:199], v6 offset:59392\n"
"v_pk_sub_u16 v224, v224, v24 op_sel_hi:[1,0] clamp\n"
"s_add_u32 s75, s75, s85\n"
"v_and_b32 v26, 28, v26\n"
"s_waitcnt lgkmcnt(4)\n"
"v_mfma_f32_32x32x16_bf16 a[16:31], a[216:219], v[216:219], a[16:31]\n"
"ds_read_b64_tr_b16 a[200:201], v7 offset:57344\n"
"ds_read_b64_tr_b16 a[202:203], v7 offset:59392\n"
"v_pk_sub_u16 v225, v225, v24 op_sel_hi:[1,0] clamp\n"
"v_add_u32 v13, s75, v26\n"
"s_lshl_b32 s75, s84, 7\n"
"v_mfma_f32_32x32x16_bf16 a[32:47], a[220:223], v[216:219], a[32:47]\n"
"ds_read_b64_tr_b16 a[204:205], v8 offset:57344\n"
"ds_read_b64_tr_b16 a[206:207], v8 offset:59392\n"
"s_add_u32 s85, s85, s75\n"
"s_and_b32 s75, %[g], 3\n"
"s_lshl_b32 s75, s75, 2\n"
"s_waitcnt lgkmcnt(4)\n"
"v_mfma_f32_32x32x16_bf16 v[32:47], a[192:195], v[220:223], v[32:47]\n"
"buffer_load_dwordx4 v3, s[52:55], s69 offen offset:2048 lds\n"
"ds_read_b64_tr_b16 a[208:209], v5 offset:57600\n"
"ds_read_b64_tr_b16 a[210:211], v5 offset:59648\n"
"v_pk_sub_u16 v226, v226, v24 op_sel_hi:[1,0] clamp\n"
"s_add_u32 s75, s75, s71\n"
"s_lshl_b32 s75, s75, 2\n"
"v_mfma_f32_32x32x16_bf16 v[48:63], a[196:199], v[220:223], v[48:63]\n"
"ds_read_b64_tr_b16 a[212:213], v6 offset:57600\n"
"ds_read_b64_tr_b16 a[214:215], v6 offset:59648\n"
"v_pk_sub_u16 v227, v227, v24 op_sel_hi:[1,0] clamp\n"
"s_add_u32 s75, s75, s85\n"
"v_mov_b32 v14, s75\n"
"s_waitcnt lgkmcnt(4)\n"
"v_mfma_f32_32x32x16_bf16 v[64:79], a[200:203], v[220:223], v[64:79]\n"
"ds_read_b64_tr_b16 a[216:217], v7 offset:57600\n"
"ds_read_b64_tr_b16 a[218:219], v7 offset:59648\n"
"v_pk_sub_u16 v228, v228, v24 op_sel_hi:[1,0] clamp\n"
"v_cndmask_b32 v14, v29, v14, vcc\n"
"s_lshl_b32 s85, %[T], 1\n"
"v_mfma_f32_32x32x16_bf16 v[80:95], a[204:207], v[220:223], v[80:95]\n"
"ds_read_b64_tr_b16 a[220:221], v8 offset:57600\n"
"ds_read_b64_tr_b16 a[222:223], v8 offset:59648\n"
"v_pk_sub_u16 v229, v229, v24 op_sel_hi:[1,0] clamp\n"
"s_add_u32 s85, s85, s84\n"
"s_mul_i32 s85, s85, 28672\n"
"v_mfma_f32_16x16x32_bf16 a[188:191], a[176:179], v[220:223], a[188:191]\n"
"s_waitcnt lgkmcnt(4)\n"
"v_mfma_f32_32x32x16_bf16 v[96:111], a[208:211], v[220:223], v[96:111]\n"
"s_waitcnt vmcnt(28)\n"
"s_barrier\n"
"buffer_load_dwordx4 v4, s[52:55], s69 offen offset:3072 lds\n"
"v_xor_b32 v5, 0x10000, v5\n"
"ds_read_b64_tr_b16 a[192:193], v5 offset:0\n"
"ds_read_b64_tr_b16 a[194:195], v5 offset:2048\n"
"v_pk_sub_u16 v230, v230, v24 op_sel_hi:[1,0] clamp\n"
"s_and_b32 s84, %[g], 3\n"
"s_mul_i32 s75, s84, 7168\n"
"v_mfma_f32_32x32x16_bf16 a[0:15], a[212:215], v[220:223], a[0:15]\n"
"v_xor_b32 v6, 0x10000, v6\n"
"ds_read_b64_tr_b16 a[196:197], v6 offset:0\n"
"ds_read_b64_tr_b16 a[198:199], v6 offset:2048\n"
"v_pk_sub_u16 v231, v231, v24 op_sel_hi:[1,0] clamp\n"
"s_add_u32 s75, s75, s85\n"
"s_mov_b64 s[80:81], %[x]\n"
"s_waitcnt lgkmcnt(4)\n"
"v_mfma_f32_32x32x16_bf16 a[16:31], a[216:219], v[220:223], a[16:31]\n"
"v_xor_b32 v7, 0x10000, v7\n"
"ds_read_b64_tr_b16 a[200:201], v7 offset:0\n"
"ds_read_b64_tr_b16 a[202:203], v7 offset:2048\n"
"s_add_u32 s80, s80, s75\n"
"s_addc_u32 s81, s81, 0\n"
"s_mov_b32 s82, 7168\n"
"s_mov_b32 s83, 0x00020000\n"
"v_mfma_f32_32x32x16_bf16 a[32:47], a[220:223], v[220:223], a[32:47]\n"
"v_xor_b32 v8, 0x10000, v8\n"
"ds_read_b64_tr_b16 a[204:205], v8 offset:0\n"
"ds_read_b64_tr_b16 a[206:207], v8 offset:2048\n"
"s_lshl_b32 s84, s84, 7\n"
"s_add_u32 s85, s85, s84\n"
"s_waitcnt lgkmcnt(4)\n"
"v_mfma_f32_32x32x16_bf16 v[32:47], a[192:195], v[224:227], v[32:47]\n"
"s_add_u32 m0, s68, 98304\n"
"s_mov_b32 s69, 114688\n"
"buffer_load_dwordx4 v1, s[52:55], s69 offen offset:0 lds\n"
"ds_read_b64_tr_b16 a[208:209], v5 offset:256\n"
"ds_read_b64_tr_b16 a[210:211], v5 offset:2304\n"
"s_add_u32 s75, s85, 131072\n"
"s_mov_b64 s[48:49], %[ws]\n"
"v_mfma_f32_32x32x16_bf16 v[48:63], a[196:199], v[224:227], v[48:63]\n"
"ds_read_b64_tr_b16 a[212:213], v6 offset:256\n"
"ds_read_b64_tr_b16 a[214:215], v6 offset:2304\n"
"v_accvgpr_read_b32 v24, a61\n"
"s_add_u32 s48, s48, s75\n"
"s_addc_u32 s49, s49, 0\n"
"s_mov_b32 s50, 0\n"
"s_waitcnt lgkmcnt(4)\n"
"v_mfma_f32_32x32x16_bf16 v[64:79], a[200:203], v[224:227], v[64:79]\n"
"ds_read_b64_tr_b16 a[216:217], v7 offset:256\n"
"ds_read_b64_tr_b16 a[218:219], v7 offset:2304\n"
"v_fmamk_f32 v24, v24, 0x43000000, v22\n"
"s_mov_b32 s51, 0x00020000\n"
"s_sub_u32 s50, 28672, s84\n"
"v_mfma_f32_32x32x16_bf16 v[80:95], a[204:207], v[224:227], v[80:95]\n"
"ds_read_b64_tr_b16 a[220:221], v8 offset:256\n"
"ds_read_b64_tr_b16 a[222:223], v8 offset:2304\n"
"v_min_f32 v24, 0x46ff0000, v24\n"
"s_add_u32 s75, s85, 1966080\n"
"s_mov_b64 s[76:77], %[ws]\n"
"v_mfma_f32_16x16x32_bf16 a[188:191], a[176:179], v[224:227], a[188:191]\n"
"s_waitcnt lgkmcnt(4)\n"
"v_mfma_f32_32x32x16_bf16 v[96:111], a[208:211], v[224:227], v[96:111]\n"
"buffer_load_dwordx4 v2, s[52:55], s69 offen offset:1024 lds\n"
"ds_read_b64_tr_b16 a[192:193], v5 offset:8192\n"
"ds_read_b64_tr_b16 a[194:195], v5 offset:10240\n"
"v_cvt_u32_f32 v24, v24\n"
"s_add_u32 s76, s76, s75\n"
"s_addc_u32 s77, s77, 0\n"
"s_mov_b32 s78, 0\n"
"v_mfma_f32_32x32x16_bf16 a[0:15], a[212:215], v[224:227], a[0:15]\n"
"ds_read_b64_tr_b16 a[196:197], v6 offset:8192\n"
"ds_read_b64_tr_b16 a[198:199], v6 offset:10240\n"
"v_pk_sub_u16 v232, v232, v24 op_sel_hi:[1,0] clamp\n"
"s_mov_b32 s79, 0x00020000\n"
"s_sub_u32 s78, 29360128, s85\n"
"v_and_b32 v26, 7, v0\n"
"s_waitcnt lgkmcnt(4)\n"
"v_mfma_f32_32x32x16_bf16 a[16:31], a[216:219], v[224:227], a[16:31]\n"
"ds_read_b64_tr_b16 a[200:201], v7 offset:8192\n"
"ds_read_b64_tr_b16 a[202:203], v7 offset:10240\n"
"v_pk_sub_u16 v233, v233, v24 op_sel_hi:[1,0] clamp\n"
"v_bfe_u32 v27, v0, 3, 1\n"
"v_lshlrev_b32 v28, 4, v26\n"
"v_mfma_f32_32x32x16_bf16 a[32:47], a[220:223], v[224:227], a[32:47]\n"
"ds_read_b64_tr_b16 a[204:205], v8 offset:8192\n"
"ds_read_b64_tr_b16 a[206:207], v8 offset:10240\n"
"v_lshl_add_u32 v28, v27, 9, v28\n"
"s_lshl_b32 s75, s71, 10\n"
"s_waitcnt lgkmcnt(4)\n"
"v_mfma_f32_32x32x16_bf16 v[32:47], a[192:195], v[228:231], v[32:47]\n"
"buffer_load_dwordx4 v3, s[52:55], s69 offen offset:2048 lds\n"
"ds_read_b64_tr_b16 a[208:209], v5 offset:8448\n"
"ds_read_b64_tr_b16 a[210:211], v5 offset:10496\n"
"v_pk_sub_u16 v234, v234, v24 op_sel_hi:[1,0] clamp\n"
"v_add_u32 v28, s75, v28\n"
"v_lshrrev_b32 v29, 4, v0\n"
"v_mfma_f32_32x32x16_bf16 v[48:63], a[196:199], v[228:231], v[48:63]\n"
"ds_read_b64_tr_b16 a[212:213], v6 offset:8448\n"
"ds_read_b64_tr_b16 a[214:215], v6 offset:10496\n"
"v_pk_sub_u16 v235, v235, v24 op_sel_hi:[1,0] clamp\n"
"v_mul_u32_u24 v29, 1835008, v29\n"
"v_add_u32 v16, v28, v29\n"
"s_waitcnt lgkmcnt(4)\n"
"v_mfma_f32_32x32x16_bf16 v[64:79], a[200:203], v[228:231], v[64:79]\n"
"ds_read_b64_tr_b16 a[216:217], v7 offset:8448\n"
"ds_read_b64_tr_b16 a[218:219], v7 offset:10496\n"
"v_pk_sub_u16 v236, v236, v24 op_sel_hi:[1,0] clamp\n"
"v_lshrrev_b32 v29, 5, v0\n"
"v_lshl_add_u32 v28, v29, 12, v28\n"
"v_mfma_f32_32x32x16_bf16 v[80:95], a[204:207], v[228:231], v[80:95]\n"
"ds_read_b64_tr_b16 a[220:221], v8 offset:8448\n"
"ds_read_b64_tr_b16 a[222:223], v8 offset:10496\n"
"v_pk_sub_u16 v237, v237, v24 op_sel_hi:[1,0] clamp\n"
"v_bfe_u32 v30, v0, 4, 1\n"
"v_lshl_add_u32 v21, v30, 3, v28\n"
"v_mfma_f32_16x16x32_bf16 a[188:191], a[176:179], v[228:231], a[188:191]\n"
"s_waitcnt lgkmcnt(4)\n"
"v_mfma_f32_32x32x16_bf16 v[96:111], a[208:211], v[228:231], v[96:111]\n"
"s_waitcnt vmcnt(27)\n"
"s_barrier\n"
"buffer_load_dwordx4 v4, s[52:55], s69 offen offset:3072 lds\n"
"ds_read_b64_tr_b16 a[192:193], v5 offset:16384\n"
"ds_read_b64_tr_b16 a[194:195], v5 offset:18432\n"
"v_pk_sub_u16 v238, v238, v24 op_sel_hi:[1,0] clamp\n"
"v_lshlrev_b32 v18, 3, v30\n"
"v_lshl_add_u32 v18, v27, 4, v18\n"
"v_mfma_f32_32x32x16_bf16 a[0:15], a[212:215], v[228:231], a[0:15]\n"
"ds_read_b64_tr_b16 a[196:197], v6 offset:16384\n"
"ds_read_b64_tr_b16 a[198:199], v6 offset:18432\n"
"v_pk_sub_u16 v239, v239, v24 op_sel_hi:[1,0] clamp\n"
"v_lshl_add_u32 v18, v29, 7, v18\n"
"s_lshl_b32 s75, s71, 5\n"
"v_add_u32 v18, s75, v18\n"
"s_waitcnt lgkmcnt(4)\n"
"v_mfma_f32_32x32x16_bf16 a[16:31], a[216:219], v[228:231], a[16:31]\n"
"ds_read_b64_tr_b16 a[200:201], v7 offset:16384\n"
"ds_read_b64_tr_b16 a[202:203], v7 offset:18432\n"
"v_mul_u32_u24 v26, 896, v26\n"
"v_add_u32 v19, v26, v18\n"
"v_mfma_f32_32x32x16_bf16 a[32:47], a[220:223], v[228:231], a[32:47]\n"
"ds_read_b64_tr_b16 a[204:205], v8 offset:16384\n"
"ds_read_b64_tr_b16 a[206:207], v8 offset:18432\n"
"buffer_load_dwordx2 a[160:161], v19, s[80:83], 0 offen offset:0\n"
"buffer_load_dwordx2 a[162:163], v19, s[80:83], 0 offen offset:256\n"
"s_waitcnt lgkmcnt(4)\n"
"v_mfma_f32_32x32x16_bf16 v[32:47], a[192:195], v[232:235], v[32:47]\n"
"s_add_u32 m0, s68, 114688\n"
"s_mov_b32 s69, 172032\n"
"buffer_load_dwordx4 v1, s[52:55], s69 offen offset:0 lds\n"
"ds_read_b64_tr_b16 a[208:209], v5 offset:16640\n"
"ds_read_b64_tr_b16 a[210:211], v5 offset:18688\n"
"buffer_load_dwordx2 a[164:165], v19, s[80:83], 0 offen offset:512\n"
"buffer_load_dwordx2 a[166:167], v19, s[80:83], 0 offen offset:768\n"
"v_mfma_f32_32x32x16_bf16 v[48:63], a[196:199], v[232:235], v[48:63]\n"
"ds_read_b64_tr_b16 a[212:213], v6 offset:16640\n"
"ds_read_b64_tr_b16 a[214:215], v6 offset:18688\n"
"v_accvgpr_read_b32 v24, a62\n"
"s_waitcnt lgkmcnt(4)\n"
"v_mfma_f32_32x32x16_bf16 v[64:79], a[200:203], v[232:235], v[64:79]\n"
"ds_read_b64_tr_b16 a[216:217], v7 offset:16640\n"
"ds_read_b64_tr_b16 a[218:219], v7 offset:18688\n"
"v_fmamk_f32 v24, v24, 0x43000000, v22\n"
"s_waitcnt vmcnt(19)\n"
"v_xor_b32 v113, s72, v113\n"
"v_xor_b32 v115, s72, v115\n"
"s_waitcnt vmcnt(18)\n"
"v_xor_b32 v117, s72, v117\n"
"v_xor_b32 v119, s72, v119\n"
"s_waitcnt vmcnt(16)\n"
"v_xor_b32 v121, s72, v121\n"
"v_xor_b32 v123, s72, v123\n"
"s_waitcnt vmcnt(15)\n"
"v_xor_b32 v125, s72, v125\n"
"v_xor_b32 v127, s72, v127\n"
"v_or_b32 v25, v113, v115\n"
"v_or3_b32 v25, v117, v119, v25\n"
"v_or3_b32 v25, v121, v123, v25\n"
"v_or3_b32 v25, v125, v127, v25\n"
"v_cmp_ne_u32 vcc, 0, v25\n"
"s_cmp_eq_u64 vcc, 0\n"
"s_cbranch_scc1 .Lok1_%=\n"
"s_movk_i32 s84, 0x7000\n"
".Lsp1_%=:\n"
"s_add_u32 s70, s71, 0\n"
"s_min_u32 s70, s70, 13\n"
"s_lshl_b32 s70, s70, 10\n"
"s_add_u32 s70, s70, 109568\n"
"buffer_load_dwordx4 v[112:115], v23, s[60:63], s70 offen sc1\n"
"s_add_u32 s70, s71, 4\n"
"s_min_u32 s70, s70, 13\n"
"s_lshl_b32 s70, s70, 10\n"
"s_add_u32 s70, s70, 109568\n"
"buffer_load_dwordx4 v[116:119], v23, s[60:63], s70 offen sc1\n"
"s_add_u32 s70, s71, 8\n"
"s_min_u32 s70, s70, 13\n"
"s_lshl_b32 s70, s70, 10\n"
"s_add_u32 s70, s70, 109568\n"
"buffer_load_dwordx4 v[120:123], v23, s[60:63], s70 offen sc1\n"
"s_add_u32 s70, s71, 12\n"
"s_min_u32 s70, s70, 13\n"
"s_lshl_b32 s70, s70, 10\n"
"s_add_u32 s70, s70, 109568\n"
"buffer_load_dwordx4 v[124:127], v23, s[60:63], s70 offen sc1\n"
"s_waitcnt vmcnt(0)\n"
"v_xor_b32 v113, s72, v113\n"
"v_xor_b32 v115, s72, v115\n"
"v_xor_b32 v117, s72, v117\n"
"v_xor_b32 v119, s72, v119\n"
"v_xor_b32 v121, s72, v121\n"
"v_xor_b32 v123, s72, v123\n"
"v_xor_b32 v125, s72, v125\n"
"v_xor_b32 v127, s72, v127\n"
"v_or_b32 v25, v113, v115\n"
"v_or3_b32 v25, v117, v119, v25\n"
"v_or3_b32 v25, v121, v123, v25\n"
"v_or3_b32 v25, v125, v127, v25\n"
"v_cmp_ne_u32 vcc, 0, v25\n"
"s_cmp_eq_u64 vcc, 0\n"
"s_cbranch_scc1 .Lok1_%=\n"
"s_sub_u32 s84, s84, 1\n"
"s_cmp_lg_u32 s84, 0\n"
"s_cbranch_scc1 .Lsp1_%=\n"
".Lok1_%=:\n"
"v_mfma_f32_32x32x16_bf16 v[80:95], a[204:207], v[232:235], v[80:95]\n"
"ds_read_b64_tr_b16 a[220:221], v8 offset:16640\n"
"ds_read_b64_tr_b16 a[222:223], v8 offset:18688\n"
"v_min_f32 v24, 0x46ff0000, v24\n"
"v_mfma_f32_16x16x32_bf16 a[188:191], a[176:179], v[232:235], a[188:191]\n"
"s_waitcnt lgkmcnt(4)\n"
"v_mfma_f32_32x32x16_bf16 v[96:111], a[208:211], v[232:235], v[96:111]\n"
"buffer_load_dwordx4 v2, s[52:55], s69 offen offset:1024 lds\n"
"ds_read_b64_tr_b16 a[192:193], v5 offset:24576\n"
"ds_read_b64_tr_b16 a[194:195], v5 offset:26624\n"
"v_cvt_u32_f32 v24, v24\n"
"s_add_u32 s70, s71, 0\n"
"s_min_u32 s70, s70, 13\n"
"s_mov_b32 s74, 14704640\n"
"buffer_store_dwordx4 a[248:251], v23, s[64:67], s74 offen offset:1024 sc1\n"
"v_mfma_f32_32x32x16_bf16 a[0:15], a[212:215], v[232:235], a[0:15]\n"
"ds_read_b64_tr_b16 a[196:197], v6 offset:24576\n"
"ds_read_b64_tr_b16 a[198:199], v6 offset:26624\n"
"v_pk_sub_u16 v240, v240, v24 op_sel_hi:[1,0] clamp\n"
"s_lshl_b32 s70, s70, 9\n"
"s_add_u32 s70, s70, 136192\n"
"s_waitcnt lgkmcnt(4)\n"
"v_mfma_f32_32x32x16_bf16 a[16:31], a[216:219], v[232:235], a[16:31]\n"
"ds_read_b64_tr_b16 a[200:201], v7 offset:24576\n"
"ds_read_b64_tr_b16 a[202:203], v7 offset:26624\n"
"v_pk_sub_u16 v241, v241, v24 op_sel_hi:[1,0] clamp\n"
"v_lshl_add_u32 v25, v0, 3, s70\n"
"ds_write2_b32 v25, v112, v114 offset0:0 offset1:1\n"
"v_mfma_f32_32x32x16_bf16 a[32:47], a[220:223], v[232:235], a[32:47]\n"
"ds_read_b64_tr_b16 a[204:205], v8 offset:24576\n"
"ds_read_b64_tr_b16 a[206:207], v8 offset:26624\n"
"s_add_u32 s70, s71, 4\n"
"s_min_u32 s70, s70, 13\n"
"s_waitcnt lgkmcnt(5)\n"
"v_mfma_f32_32x32x16_bf16 v[32:47], a[192:195], v[236:239], v[32:47]\n"
"buffer_load_dwordx4 v3, s[52:55], s69 offen offset:2048 lds\n"
"ds_read_b64_tr_b16 a[208:209], v5 offset:24832\n"
"ds_read_b64_tr_b16 a[210:211], v5 offset:26880\n"
"v_pk_sub_u16 v242, v242, v24 op_sel_hi:[1,0] clamp\n"
"s_lshl_b32 s70, s70, 9\n"
"s_add_u32 s70, s70, 136192\n"
"v_mfma_f32_32x32x16_bf16 v[48:63], a[196:199], v[236:239], v[48:63]\n"
"ds_read_b64_tr_b16 a[212:213], v6 offset:24832\n"
"ds_read_b64_tr_b16 a[214:215], v6 offset:26880\n"
"v_pk_sub_u16 v243, v243, v24 op_sel_hi:[1,0] clamp\n"
"v_lshl_add_u32 v25, v0, 3, s70\n"
"ds_write2_b32 v25, v116, v118 offset0:0 offset1:1\n"
"s_waitcnt lgkmcnt(5)\n"
"v_mfma_f32_32x32x16_bf16 v[64:79], a[200:203], v[236:239], v[64:79]\n"
"ds_read_b64_tr_b16 a[216:217], v7 offset:24832\n"
"ds_read_b64_tr_b16 a[218:219], v7 offset:26880\n"
"v_pk_sub_u16 v244, v244, v24 op_sel_hi:[1,0] clamp\n"
"s_add_u32 s70, s71, 8\n"
"s_min_u32 s70, s70, 13\n"
"v_mfma_f32_32x32x16_bf16 v[80:95], a[204:207], v[236:239], v[80:95]\n"
"ds_read_b64_tr_b16 a[220:221], v8 offset:24832\n"
"ds_read_b64_tr_b16 a[222:223], v8 offset:26880\n"
"v_pk_sub_u16 v245, v245, v24 op_sel_hi:[1,0] clamp\n"
"s_lshl_b32 s70, s70, 9\n"
"s_add_u32 s70, s70, 136192\n"
"v_mfma_f32_16x16x32_bf16 a[188:191], a[176:179], v[236:239], a[188:191]\n"
"s_waitcnt lgkmcnt(5)\n"
"v_mfma_f32_32x32x16_bf16 v[96:111], a[208:211], v[236:239], v[96:111]\n"
"s_barrier\n"
"buffer_load_dwordx4 v4, s[52:55], s69 offen offset:3072 lds\n"
"ds_read_b64_tr_b16 a[192:193], v5 offset:32768\n"
"ds_read_b64_tr_b16 a[194:195], v5 offset:34816\n"
"v_pk_sub_u16 v246, v246, v24 op_sel_hi:[1,0] clamp\n"
"v_lshl_add_u32 v25, v0, 3, s70\n"
"ds_write2_b32 v25, v120, v122 offset0:0 offset1:1\n"
"v_mfma_f32_32x32x16_bf16 a[0:15], a[212:215], v[236:239], a[0:15]\n"
"ds_read_b64_tr_b16 a[196:197], v6 offset:32768\n"
"ds_read_b64_tr_b16 a[198:199], v6 offset:34816\n"
"v_pk_sub_u16 v247, v247, v24 op_sel_hi:[1,0] clamp\n"
"s_add_u32 s70, s71, 12\n"
"s_min_u32 s70, s70, 13\n"
"s_waitcnt lgkmcnt(5)\n"
"v_mfma_f32_32x32x16_bf16 a[16:31], a[216:219], v[236:239], a[16:31]\n"
"ds_read_b64_tr_b16 a[200:201], v7 offset:32768\n"
"ds_read_b64_tr_b16 a[202:203], v7 offset:34816\n"
"s_lshl_b32 s70, s70, 9\n"
"s_add_u32 s70, s70, 136192\n"
"v_mfma_f32_32x32x16_bf16 a[32:47], a[220:223], v[236:239], a[32:47]\n"
"ds_read_b64_tr_b16 a[204:205], v8 offset:32768\n"
"ds_read_b64_tr_b16 a[206:207], v8 offset:34816\n"
"v_lshl_add_u32 v25, v0, 3, s70\n"
"ds_write2_b32 v25, v124, v126 offset0:0 offset1:1\n"
"s_waitcnt lgkmcnt(5)\n"
"v_mfma_f32_32x32x16_bf16 v[32:47], a[192:195], v[240:243], v[32:47]\n"
"s_add_u32 m0, s68, 0\n"
"s_mov_b32 s69, 128\n"
"buffer_load_dwordx4 v1, s[52:55], s69 offen offset:0 lds\n"
"ds_read_b64_tr_b16 a[208:209], v5 offset:33024\n"
"ds_read_b64_tr_b16 a[210:211], v5 offset:35072\n"
"v_mfma_f32_32x32x16_bf16 v[48:63], a[196:199], v[240:243], v[48:63]\n"
"ds_read_b64_tr_b16 a[212:213], v6 offset:33024\n"
"ds_read_b64_tr_b16 a[214:215], v6 offset:35072\n"
"s_waitcnt lgkmcnt(5)\n"
"v_mfma_f32_32x32x16_bf16 v[64:79], a[200:203], v[240:243], v[64:79]\n"
"ds_read_b64_tr_b16 a[216:217], v7 offset:33024\n"
"ds_read_b64_tr_b16 a[218:219], v7 offset:35072\n"
"v_mfma_f32_32x32x16_bf16 v[80:95], a[204:207], v[240:243], v[80:95]\n"
"ds_read_b64_tr_b16 a[220:221], v8 offset:33024\n"
"ds_read_b64_tr_b16 a[222:223], v8 offset:35072\n"
"v_mfma_f32_16x16x32_bf16 a[188:191], a[176:179], v[240:243], a[188:191]\n"
"s_waitcnt lgkmcnt(4)\n"
"v_mfma_f32_32x32x16_bf16 v[96:111], a[208:211], v[240:243], v[96:111]\n"
"buffer_load_dwordx4 v2, s[52:55], s69 offen offset:1024 lds\n"
"ds_read_b64_tr_b16 a[192:193], v5 offset:40960\n"
"ds_read_b64_tr_b16 a[194:195], v5 offset:43008\n"
"v_mfma_f32_32x32x16_bf16 a[0:15], a[212:215], v[240:243], a[0:15]\n"
"ds_read_b64_tr_b16 a[196:197], v6 offset:40960\n"
"ds_read_b64_tr_b16 a[198:199], v6 offset:43008\n"
"v_mfma_f32_16x16x32_bf16 a[188:191], a[176:179], v[248:251], a[188:191]\n"
"s_waitcnt lgkmcnt(4)\n"
"v_mfma_f32_32x32x16_bf16 a[16:31], a[216:219], v[240:243], a[16:31]\n"
"ds_read_b64_tr_b16 a[200:201], v7 offset:40960\n"
"ds_read_b64_tr_b16 a[202:203], v7 offset:43008\n"
"v_mfma_f32_32x32x16_bf16 a[32:47], a[220:223], v[240:243], a[32:47]\n"
"ds_read_b64_tr_b16 a[204:205], v8 offset:40960\n"
"ds_read_b64_tr_b16 a[206:207], v8 offset:43008\n"
"s_waitcnt lgkmcnt(4)\n"
"v_mfma_f32_32x32x16_bf16 v[32:47], a[192:195], v[244:247], v[32:47]\n"
"buffer_load_dwordx4 v3, s[52:55], s69 offen offset:2048 lds\n"
"ds_read_b64_tr_b16 a[208:209], v5 offset:41216\n"
"ds_read_b64_tr_b16 a[210:211], v5 offset:43264\n"
"v_mfma_f32_32x32x16_bf16 v[48:63], a[196:199], v[244:247], v[48:63]\n"
"ds_read_b64_tr_b16 a[212:213], v6 offset:41216\n"
"ds_read_b64_tr_b16 a[214:215], v6 offset:43264\n"
"s_waitcnt lgkmcnt(4)\n"
"v_mfma_f32_32x32x16_bf16 v[64:79], a[200:203], v[244:247], v[64:79]\n"
"ds_read_b64_tr_b16 a[216:217], v7 offset:41216\n"
"ds_read_b64_tr_b16 a[218:219], v7 offset:43264\n"
"v_mfma_f32_32x32x16_bf16 v[80:95], a[204:207], v[244:247], v[80:95]\n"
"ds_read_b64_tr_b16 a[220:221], v8 offset:41216\n"
"ds_read_b64_tr_b16 a[222:223], v8 offset:43264\n"
"v_mfma_f32_16x16x32_bf16 a[188:191], a[176:179], v[244:247], a[188:191]\n"
"s_waitcnt lgkmcnt(4)\n"
"v_mfma_f32_32x32x16_bf16 v[96:111], a[208:211], v[244:247], v[96:111]\n"
"s_barrier\n"
"buffer_load_dwordx4 v4, s[52:55], s69 offen offset:3072 lds\n"
"ds_read_b64_tr_b16 a[192:193], v5 offset:49152\n"
"ds_read_b64_tr_b16 a[194:195], v5 offset:51200\n"
"v_mfma_f32_32x32x16_bf16 a[0:15], a[212:215], v[244:247], a[0:15]\n"
"ds_read_b64_tr_b16 a[196:197], v6 offset:49152\n"
"ds_read_b64_tr_b16 a[198:199], v6 offset:51200\n"
"v_mfma_f32_16x16x32_bf16 a[188:191], a[176:179], v[252:255], a[188:191]\n"
"s_waitcnt lgkmcnt(4)\n"
"v_mfma_f32_32x32x16_bf16 a[16:31], a[216:219], v[244:247], a[16:31]\n"
"ds_read_b64_tr_b16 a[200:201], v7 offset:49152\n"
"ds_read_b64_tr_b16 a[202:203], v7 offset:51200\n"
"v_mfma_f32_32x32x16_bf16 a[32:47], a[220:223], v[244:247], a[32:47]\n"
"ds_read_b64_tr_b16 a[204:205], v8 offset:49152\n"
"ds_read_b64_tr_b16 a[206:207], v8 offset:51200\n"
"s_waitcnt lgkmcnt(4)\n"
"v_mfma_f32_32x32x16_bf16 v[32:47], a[192:195], v[248:251], v[32:47]\n"
"s_add_u32 m0, s68, 16384\n"
"s_mov_b32 s69, 57472\n"
"buffer_load_dwordx4 v1, s[52:55], s69 offen offset:0 lds\n"
"ds_read_b64_tr_b16 a[208:209], v5 offset:49408\n"
"ds_read_b64_tr_b16 a[210:211], v5 offset:51456\n"
"v_mfma_f32_32x32x16_bf16 v[48:63], a[196:199], v[248:251], v[48:63]\n"
"ds_read_b64_tr_b16 a[212:213], v6 offset:49408\n"
"ds_read_b64_tr_b16 a[214:215], v6 offset:51456\n"
"s_waitcnt lgkmcnt(4)\n"
"v_mfma_f32_32x32x16_bf16 v[64:79], a[200:203], v[248:251], v[64:79]\n"
"ds_read_b64_tr_b16 a[216:217], v7 offset:49408\n"
"ds_read_b64_tr_b16 a[218:219], v7 offset:51456\n"
"v_accvgpr_read_b32 v20, a188\n"
"v_mfma_f32_32x32x16_bf16 v[80:95], a[204:207], v[248:251], v[80:95]\n"
"ds_read_b64_tr_b16 a[220:221], v8 offset:49408\n"
"ds_read_b64_tr_b16 a[222:223], v8 offset:51456\n"
"s_waitcnt lgkmcnt(4)\n"
"v_mfma_f32_32x32x16_bf16 v[96:111], a[208:211], v[248:251], v[96:111]\n"
"buffer_load_dwordx4 v2, s[52:55], s69 offen offset:1024 lds\n"
"ds_read_b64_tr_b16 a[192:193], v5 offset:57344\n"
"ds_read_b64_tr_b16 a[194:195], v5 offset:59392\n"
"v_rcp_f32 v20, v20\n"
"v_mfma_f32_32x32x16_bf16 a[0:15], a[212:215], v[248:251], a[0:15]\n"
"ds_read_b64_tr_b16 a[196:197], v6 offset:57344\n"
"ds_read_b64_tr_b16 a[198:199], v6 offset:59392\n"
"s_waitcnt lgkmcnt(4)\n"
"v_mfma_f32_32x32x16_bf16 a[16:31], a[216:219], v[248:251], a[16:31]\n"
"ds_read_b64_tr_b16 a[200:201], v7 offset:57344\n"
"ds_read_b64_tr_b16 a[202:203], v7 offset:59392\n"
"v_lshlrev_b32 v22, 1, v18\n"
"v_add_u32 v22, 136192, v22\n"
"v_mfma_f32_32x32x16_bf16 a[32:47], a[220:223], v[248:251], a[32:47]\n"
"ds_read_b64_tr_b16 a[204:205], v8 offset:57344\n"
"ds_read_b64_tr_b16 a[206:207], v8 offset:59392\n"
"s_waitcnt lgkmcnt(4)\n"
"v_mfma_f32_32x32x16_bf16 v[32:47], a[192:195], v[252:255], v[32:47]\n"
"buffer_load_dwordx4 v3, s[52:55], s69 offen offset:2048 lds\n"
"ds_read_b64_tr_b16 a[208:209], v5 offset:57600\n"
"ds_read_b64_tr_b16 a[210:211], v5 offset:59648\n"
"v_mfma_f32_32x32x16_bf16 v[48:63], a[196:199], v[252:255], v[48:63]\n"
"ds_read_b64_tr_b16 a[212:213], v6 offset:57600\n"
"ds_read_b64_tr_b16 a[214:215], v6 offset:59648\n"
"s_waitcnt lgkmcnt(4)\n"
"v_mfma_f32_32x32x16_bf16 v[64:79], a[200:203], v[252:255], v[64:79]\n"
"ds_read_b64_tr_b16 a[216:217], v7 offset:57600\n"
"ds_read_b64_tr_b16 a[218:219], v7 offset:59648\n"
"v_mul_f32 v32, v32, v20\n"
"v_mul_f32 v33, v33, v20\n"
"v_mul_f32 v34, v34, v20\n"
"v_mul_f32 v35, v35, v20\n"
"v_mul_f32 v36, v36, v20\n"
"v_mul_f32 v37, v37, v20\n"
"v_mfma_f32_32x32x16_bf16 v[80:95], a[204:207], v[252:255], v[80:95]\n"
"ds_read_b64_tr_b16 a[220:221], v8 offset:57600\n"
"ds_read_b64_tr_b16 a[222:223], v8 offset:59648\n"
"v_mul_f32 v38, v38, v20\n"
"v_mul_f32 v39, v39, v20\n"
"v_cvt_pk_bf16_f32 v32, v32, v33\n"
"v_cvt_pk_bf16_f32 v33, v34, v35\n"
"v_cvt_pk_bf16_f32 v34, v36, v37\n"
"v_cvt_pk_bf16_f32 v35, v38, v39\n"
"s_waitcnt lgkmcnt(4)\n"
"v_mfma_f32_32x32x16_bf16 v[96:111], a[208:211], v[252:255], v[96:111]\n"
"s_waitcnt vmcnt(20)\n"
"s_barrier\n"
"buffer_load_dwordx4 v4, s[52:55], s69 offen offset:3072 lds\n"
"v_xor_b32 v9, 0x10000, v9\n"
"ds_read_b64_tr_b16 a[192:193], v9 offset:0\n"
"ds_read_b64_tr_b16 a[194:195], v9 offset:512\n"
"v_mul_f32 v40, v40, v20\n"
"v_mul_f32 v41, v41, v20\n"
"v_mul_f32 v42, v42, v20\n"
"v_mul_f32 v43, v43, v20\n"
"v_mul_f32 v44, v44, v20\n"
"v_mul_f32 v45, v45, v20\n"
"v_mfma_f32_32x32x16_bf16 a[0:15], a[212:215], v[252:255], a[0:15]\n"
"v_xor_b32 v10, 0x10000, v10\n"
"ds_read_b64_tr_b16 a[196:197], v10 offset:0\n"
"ds_read_b64_tr_b16 a[198:199], v10 offset:512\n"
"v_mul_f32 v46, v46, v20\n"
"v_mul_f32 v47, v47, v20\n"
"v_cvt_pk_bf16_f32 v36, v40, v41\n"
"v_cvt_pk_bf16_f32 v37, v42, v43\n"
"v_cvt_pk_bf16_f32 v38, v44, v45\n"
"v_cvt_pk_bf16_f32 v39, v46, v47\n"
"s_waitcnt lgkmcnt(4)\n"
"v_mfma_f32_32x32x16_bf16 a[16:31], a[216:219], v[252:255], a[16:31]\n"
"s_add_u32 m0, s68, 32768\n"
"s_mov_b32 s69, 114816\n"
"buffer_load_dwordx4 v1, s[52:55], s69 offen offset:0 lds\n"
"ds_read_b64_tr_b16 a[200:201], v9 offset:2048\n"
"ds_read_b64_tr_b16 a[202:203], v9 offset:2560\n"
"v_mul_f32 v48, v48, v20\n"
"v_mul_f32 v49, v49, v20\n"
"v_mul_f32 v50, v50, v20\n"
"v_mul_f32 v51, v51, v20\n"
"v_mul_f32 v52, v52, v20\n"
"v_mul_f32 v53, v53, v20\n"
"v_mfma_f32_32x32x16_bf16 a[32:47], a[220:223], v[252:255], a[32:47]\n"
"ds_read_b64_tr_b16 a[204:205], v10 offset:2048\n"
"ds_read_b64_tr_b16 a[206:207], v10 offset:2560\n"
"v_mul_f32 v54, v54, v20\n"
"v_mul_f32 v55, v55, v20\n"
"v_cvt_pk_bf16_f32 v48, v48, v49\n"
"v_cvt_pk_bf16_f32 v49, v50, v51\n"
"v_cvt_pk_bf16_f32 v50, v52, v53\n"
"v_cvt_pk_bf16_f32 v51, v54, v55\n"
"s_waitcnt lgkmcnt(4)\n"
"v_mfma_f32_32x32x16_bf16 v[128:143], a[192:195], v[32:35], 0\n"
"buffer_load_dwordx4 v2, s[52:55], s69 offen offset:1024 lds\n"
"ds_read_b64_tr_b16 a[208:209], v9 offset:4096\n"
"ds_read_b64_tr_b16 a[210:211], v9 offset:4608\n"
"v_mul_f32 v56, v56, v20\n"
"v_mul_f32 v57, v57, v20\n"
"v_mul_f32 v58, v58, v20\n"
"v_mul_f32 v59, v59, v20\n"
"v_mul_f32 v60, v60, v20\n"
"v_mul_f32 v61, v61, v20\n"
"v_mfma_f32_32x32x16_bf16 v[144:159], a[196:199], v[32:35], 0\n"
"ds_read_b64_tr_b16 a[212:213], v10 offset:4096\n"
"ds_read_b64_tr_b16 a[214:215], v10 offset:4608\n"
"v_mul_f32 v62, v62, v20\n"
"v_mul_f32 v63, v63, v20\n"
"v_cvt_pk_bf16_f32 v52, v56, v57\n"
"v_cvt_pk_bf16_f32 v53, v58, v59\n"
"v_cvt_pk_bf16_f32 v54, v60, v61\n"
"v_cvt_pk_bf16_f32 v55, v62, v63\n"
"s_waitcnt lgkmcnt(4)\n"
"v_mfma_f32_32x32x16_bf16 v[128:143], a[200:203], v[36:39], v[128:143]\n"
"buffer_load_dwordx4 v3, s[52:55], s69 offen offset:2048 lds\n"
"ds_read_b64_tr_b16 a[216:217], v9 offset:6144\n"
"ds_read_b64_tr_b16 a[218:219], v9 offset:6656\n"
"v_mul_f32 v64, v64, v20\n"
"v_mul_f32 v65, v65, v20\n"
"v_mul_f32 v66, v66, v20\n"
"v_mul_f32 v67, v67, v20\n"
"v_mul_f32 v68, v68, v20\n"
"v_mul_f32 v69, v69, v20\n"
"v_mfma_f32_32x32x16_bf16 v[144:159], a[204:207], v[36:39], v[144:159]\n"
"ds_read_b64_tr_b16 a[220:221], v10 offset:6144\n"
"ds_read_b64_tr_b16 a[222:223], v10 offset:6656\n"
"v_mul_f32 v70, v70, v20\n"
"v_mul_f32 v71, v71, v20\n"
"v_cvt_pk_bf16_f32 v64, v64, v65\n"
"v_cvt_pk_bf16_f32 v65, v66, v67\n"
"v_cvt_pk_bf16_f32 v66, v68, v69\n"
"v_cvt_pk_bf16_f32 v67, v70, v71\n"
"s_waitcnt lgkmcnt(4)\n"
"v_mfma_f32_32x32x16_bf16 v[128:143], a[208:211], v[48:51], v[128:143]\n"
"buffer_load_dwordx4 v4, s[52:55], s69 offen offset:3072 lds\n"
"ds_read_b64_tr_b16 a[192:193], v9 offset:16384\n"
"ds_read_b64_tr_b16 a[194:195], v9 offset:16896\n"
"v_mul_f32 v72, v72, v20\n"
"v_mul_f32 v73, v73, v20\n"
"v_mul_f32 v74, v74, v20\n"
"v_mul_f32 v75, v75, v20\n"
"v_mul_f32 v76, v76, v20\n"
"v_mul_f32 v77, v77, v20\n"
"v_mfma_f32_32x32x16_bf16 v[144:159], a[212:215], v[48:51], v[144:159]\n"
"ds_read_b64_tr_b16 a[196:197], v10 offset:16384\n"
"ds_read_b64_tr_b16 a[198:199], v10 offset:16896\n"
"v_mul_f32 v78, v78, v20\n"
"v_mul_f32 v79, v79, v20\n"
"v_cvt_pk_bf16_f32 v68, v72, v73\n"
"v_cvt_pk_bf16_f32 v69, v74, v75\n"
"v_cvt_pk_bf16_f32 v70, v76, v77\n"
"v_cvt_pk_bf16_f32 v71, v78, v79\n"
"s_waitcnt lgkmcnt(4)\n"
"v_mfma_f32_32x32x16_bf16 v[128:143], a[216:219], v[52:55], v[128:143]\n"
"ds_read_b64_tr_b16 a[200:201], v9 offset:18432\n"
"ds_read_b64_tr_b16 a[202:203], v9 offset:18944\n"
"v_mul_f32 v80, v80, v20\n"
"v_mul_f32 v81, v81, v20\n"
"v_mul_f32 v82, v82, v20\n"
"v_mul_f32 v83, v83, v20\n"
"v_mul_f32 v84, v84, v20\n"
"v_mul_f32 v85, v85, v20\n"
"v_mfma_f32_32x32x16_bf16 v[144:159], a[220:223], v[52:55], v[144:159]\n"
"ds_read_b64_tr_b16 a[204:205], v10 offset:18432\n"
"ds_read_b64_tr_b16 a[206:207], v10 offset:18944\n"
"v_mul_f32 v86, v86, v20\n"
"v_mul_f32 v87, v87, v20\n"
"v_cvt_pk_bf16_f32 v80, v80, v81\n"
"v_cvt_pk_bf16_f32 v81, v82, v83\n"
"v_cvt_pk_bf16_f32 v82, v84, v85\n"
"v_cvt_pk_bf16_f32 v83, v86, v87\n"
"s_waitcnt lgkmcnt(4)\n"
"v_mfma_f32_32x32x16_bf16 v[128:143], a[192:195], v[64:67], v[128:143]\n"
"ds_read_b64_tr_b16 a[208:209], v9 offset:20480\n"
"ds_read_b64_tr_b16 a[210:211], v9 offset:20992\n"
"v_mul_f32 v88, v88, v20\n"
"v_mul_f32 v89, v89, v20\n"
"v_mul_f32 v90, v90, v20\n"
"v_mul_f32 v91, v91, v20\n"
"v_mul_f32 v92, v92, v20\n"
"v_mul_f32 v93, v93, v20\n"
"v_mfma_f32_32x32x16_bf16 v[144:159], a[196:199], v[64:67], v[144:159]\n"
"ds_read_b64_tr_b16 a[212:213], v10 offset:20480\n"
"ds_read_b64_tr_b16 a[214:215], v10 offset:20992\n"
"v_mul_f32 v94, v94, v20\n"
"v_mul_f32 v95, v95, v20\n"
"v_cvt_pk_bf16_f32 v84, v88, v89\n"
"v_cvt_pk_bf16_f32 v85, v90, v91\n"
"v_cvt_pk_bf16_f32 v86, v92, v93\n"
"v_cvt_pk_bf16_f32 v87, v94, v95\n"
"s_waitcnt lgkmcnt(4)\n"
"v_mfma_f32_32x32x16_bf16 v[128:143], a[200:203], v[68:71], v[128:143]\n"
"ds_read_b64_tr_b16 a[216:217], v9 offset:22528\n"
"ds_read_b64_tr_b16 a[218:219], v9 offset:23040\n"
"v_mul_f32 v96, v96, v20\n"
"v_mul_f32 v97, v97, v20\n"
"v_mul_f32 v98, v98, v20\n"
"v_mul_f32 v99, v99, v20\n"
"v_mul_f32 v100, v100, v20\n"
"v_mul_f32 v101, v101, v20\n"
"v_mfma_f32_32x32x16_bf16 v[144:159], a[204:207], v[68:71], v[144:159]\n"
"ds_read_b64_tr_b16 a[220:221], v10 offset:22528\n"
"ds_read_b64_tr_b16 a[222:223], v10 offset:23040\n"
"v_mul_f32 v102, v102, v20\n"
"v_mul_f32 v103, v103, v20\n"
"v_cvt_pk_bf16_f32 v96, v96, v97\n"
"v_cvt_pk_bf16_f32 v97, v98, v99\n"
"v_cvt_pk_bf16_f32 v98, v100, v101\n"
"v_cvt_pk_bf16_f32 v99, v102, v103\n"
"s_waitcnt lgkmcnt(4)\n"
"v_mfma_f32_32x32x16_bf16 v[128:143], a[208:211], v[80:83], v[128:143]\n"
"s_waitcnt vmcnt(12)\n"
"s_barrier\n"
"ds_read_b64_tr_b16 a[192:193], v9 offset:32768\n"
"ds_read_b64_tr_b16 a[194:195], v9 offset:33280\n"
"v_mul_f32 v104, v104, v20\n"
"v_mul_f32 v105, v105, v20\n"
"v_mul_f32 v106, v106, v20\n"
"v_mul_f32 v107, v107, v20\n"
"v_mul_f32 v108, v108, v20\n"
"v_mul_f32 v109, v109, v20\n"
"v_mfma_f32_32x32x16_bf16 v[144:159], a[212:215], v[80:83], v[144:159]\n"
"s_add_u32 m0, s68, 49152\n"
"s_mov_b32 s69, 172160\n"
"buffer_load_dwordx4 v1, s[52:55], s69 offen offset:0 lds\n"
"ds_read_b64_tr_b16 a[196:197], v10 offset:32768\n"
"ds_read_b64_tr_b16 a[198:199], v10 offset:33280\n"
"v_mul_f32 v110, v110, v20\n"
"v_mul_f32 v111, v111, v20\n"
"v_cvt_pk_bf16_f32 v100, v104, v105\n"
"v_cvt_pk_bf16_f32 v101, v106, v107\n"
"v_cvt_pk_bf16_f32 v102, v108, v109\n"
"v_cvt_pk_bf16_f32 v103, v110, v111\n"
"s_waitcnt lgkmcnt(4)\n"
"v_mfma_f32_32x32x16_bf16 v[128:143], a[216:219], v[84:87], v[128:143]\n"
"ds_read_b64_tr_b16 a[200:201], v9 offset:34816\n"
"ds_read_b64_tr_b16 a[202:203], v9 offset:35328\n"
"v_accvgpr_read_b32 v216, a0\n"
"v_accvgpr_read_b32 v217, a1\n"
"v_accvgpr_read_b32 v218, a2\n"
"v_accvgpr_read_b32 v219, a3\n"
"v_accvgpr_read_b32 v220, a4\n"
"v_accvgpr_read_b32 v221, a5\n"
"v_accvgpr_read_b32 v222, a6\n"
"v_mfma_f32_32x32x16_bf16 v[144:159], a[220:223], v[84:87], v[144:159]\n"
"buffer_load_dwordx4 v2, s[52:55], s69 offen offset:1024 lds\n"
"ds_read_b64_tr_b16 a[204:205], v10 offset:34816\n"
"ds_read_b64_tr_b16 a[206:207], v10 offset:35328\n"
"v_accvgpr_read_b32 v223, a7\n"
"v_mul_f32 v216, v216, v20\n"
"v_mul_f32 v217, v217, v20\n"
"v_mul_f32 v218, v218, v20\n"
"v_mul_f32 v219, v219, v20\n"
"v_mul_f32 v220, v220, v20\n"
"s_waitcnt lgkmcnt(4)\n"
"v_mfma_f32_32x32x16_bf16 v[128:143], a[192:195], v[96:99], v[128:143]\n"
"ds_read_b64_tr_b16 a[208:209], v9 offset:36864\n"
"ds_read_b64_tr_b16 a[210:211], v9 offset:37376\n"
"v_mul_f32 v221, v221, v20\n"
"v_mul_f32 v222, v222, v20\n"
"v_mul_f32 v223, v223, v20\n"
"v_cvt_pk_bf16_f32 v112, v216, v217\n"
"v_cvt_pk_bf16_f32 v113, v218, v219\n"
"v_cvt_pk_bf16_f32 v114, v220, v221\n"
"v_cvt_pk_bf16_f32 v115, v222, v223\n"
"v_accvgpr_read_b32 v216, a8\n"
"v_accvgpr_read_b32 v217, a9\n"
"v_accvgpr_read_b32 v218, a10\n"
"v_accvgpr_read_b32 v219, a11\n"
"v_accvgpr_read_b32 v220, a12\n"
"v_accvgpr_read_b32 v221, a13\n"
"v_accvgpr_read_b32 v222, a14\n"
"v_mfma_f32_32x32x16_bf16 v[144:159], a[196:199], v[96:99], v[144:159]\n"
"buffer_load_dwordx4 v3, s[52:55], s69 offen offset:2048 lds\n"
"ds_read_b64_tr_b16 a[212:213], v10 offset:36864\n"
"ds_read_b64_tr_b16 a[214:215], v10 offset:37376\n"
"v_accvgpr_read_b32 v223, a15\n"
"v_mul_f32 v216, v216, v20\n"
"v_mul_f32 v217, v217, v20\n"
"v_mul_f32 v218, v218, v20\n"
"v_mul_f32 v219, v219, v20\n"
"v_mul_f32 v220, v220, v20\n"
"s_waitcnt lgkmcnt(4)\n"
"v_mfma_f32_32x32x16_bf16 v[128:143], a[200:203], v[100:103], v[128:143]\n"
"ds_read_b64_tr_b16 a[216:217], v9 offset:38912\n"
"ds_read_b64_tr_b16 a[218:219], v9 offset:39424\n"
"v_mul_f32 v221, v221, v20\n"
"v_mul_f32 v222, v222, v20\n"
"v_mul_f32 v223, v223, v20\n"
"v_cvt_pk_bf16_f32 v116, v216, v217\n"
"v_cvt_pk_bf16_f32 v117, v218, v219\n"
"v_cvt_pk_bf16_f32 v118, v220, v221\n"
"v_cvt_pk_bf16_f32 v119, v222, v223\n"
"v_mfma_f32_32x32x16_bf16 v[144:159], a[204:207], v[100:103], v[144:159]\n"
"buffer_load_dwordx4 v4, s[52:55], s69 offen offset:3072 lds\n"
"ds_read_b64_tr_b16 a[220:221], v10 offset:38912\n"
"ds_read_b64_tr_b16 a[222:223], v10 offset:39424\n"
"v_accvgpr_read_b32 v216, a16\n"
"v_accvgpr_read_b32 v217, a17\n"
"v_accvgpr_read_b32 v218, a18\n"
"v_accvgpr_read_b32 v219, a19\n"
"v_accvgpr_read_b32 v220, a20\n"
"v_accvgpr_read_b32 v221, a21\n"
"v_accvgpr_read_b32 v222, a22\n"
"s_waitcnt lgkmcnt(4)\n"
"v_mfma_f32_32x32x16_bf16 v[128:143], a[208:211], v[112:115], v[128:143]\n"
"ds_read_b64_tr_b16 a[192:193], v9 offset:49152\n"
"ds_read_b64_tr_b16 a[194:195], v9 offset:49664\n"
"v_accvgpr_read_b32 v223, a23\n"
"v_mul_f32 v216, v216, v20\n"
"v_mul_f32 v217, v217, v20\n"
"v_mul_f32 v218, v218, v20\n"
"v_mul_f32 v219, v219, v20\n"
"v_mul_f32 v220, v220, v20\n"
"v_mfma_f32_32x32x16_bf16 v[144:159], a[212:215], v[112:115], v[144:159]\n"
"s_add_u32 m0, s68, 65536\n"
"s_mov_b32 s69, 256\n"
"buffer_load_dwordx4 v1, s[52:55], s69 offen offset:0 lds\n"
"ds_read_b64_tr_b16 a[196:197], v10 offset:49152\n"
"ds_read_b64_tr_b16 a[198:199], v10 offset:49664\n"
"v_mul_f32 v221, v221, v20\n"
"v_mul_f32 v222, v222, v20\n"
"v_mul_f32 v223, v223, v20\n"
"v_cvt_pk_bf16_f32 v120, v216, v217\n"
"v_cvt_pk_bf16_f32 v121, v218, v219\n"
"v_cvt_pk_bf16_f32 v122, v220, v221\n"
"v_cvt_pk_bf16_f32 v123, v222, v223\n"
"v_accvgpr_read_b32 v216, a24\n"
"v_accvgpr_read_b32 v217, a25\n"
"v_accvgpr_read_b32 v218, a26\n"
"v_accvgpr_read_b32 v219, a27\n"
"v_accvgpr_read_b32 v220, a28\n"
"v_accvgpr_read_b32 v221, a29\n"
"v_accvgpr_read_b32 v222, a30\n"
"s_waitcnt lgkmcnt(4)\n"
"v_mfma_f32_32x32x16_bf16 v[128:143], a[216:219], v[116:119], v[128:143]\n"
"ds_read_b64_tr_b16 a[200:201], v9 offset:51200\n"
"ds_read_b64_tr_b16 a[202:203], v9 offset:51712\n"
"v_accvgpr_read_b32 v223, a31\n"
"v_mul_f32 v216, v216, v20\n"
"v_mul_f32 v217, v217, v20\n"
"v_mul_f32 v218, v218, v20\n"
"v_mul_f32 v219, v219, v20\n"
"v_mul_f32 v220, v220, v20\n"
"v_mfma_f32_32x32x16_bf16 v[144:159], a[220:223], v[116:119], v[144:159]\n"
"buffer_load_dwordx4 v2, s[52:55], s69 offen offset:1024 lds\n"
"ds_read_b64_tr_b16 a[204:205], v10 offset:51200\n"
"ds_read_b64_tr_b16 a[206:207], v10 offset:51712\n"
"v_mul_f32 v221, v221, v20\n"
"v_mul_f32 v222, v222, v20\n"
"v_mul_f32 v223, v223, v20\n"
"v_cvt_pk_bf16_f32 v124, v216, v217\n"
"v_cvt_pk_bf16_f32 v125, v218, v219\n"
"v_cvt_pk_bf16_f32 v126, v220, v221\n"
"v_cvt_pk_bf16_f32 v127, v222, v223\n"
"s_waitcnt lgkmcnt(4)\n"
"v_mfma_f32_32x32x16_bf16 v[128:143], a[192:195], v[120:123], v[128:143]\n"
"ds_read_b64_tr_b16 a[208:209], v9 offset:53248\n"
"ds_read_b64_tr_b16 a[210:211], v9 offset:53760\n"
"v_accvgpr_read_b32 v216, a32\n"
"v_accvgpr_read_b32 v217, a33\n"
"v_accvgpr_read_b32 v218, a34\n"
"v_accvgpr_read_b32 v219, a35\n"
"v_accvgpr_read_b32 v220, a36\n"
"v_accvgpr_read_b32 v221, a37\n"
"v_accvgpr_read_b32 v222, a38\n"
"v_mfma_f32_32x32x16_bf16 v[144:159], a[196:199], v[120:123], v[144:159]\n"
"buffer_load_dwordx4 v3, s[52:55], s69 offen offset:2048 lds\n"
"ds_read_b64_tr_b16 a[212:213], v10 offset:53248\n"
"ds_read_b64_tr_b16 a[214:215], v10 offset:53760\n"
"v_accvgpr_read_b32 v223, a39\n"
"v_mul_f32 v216, v216, v20\n"
"v_mul_f32 v217, v217, v20\n"
"v_mul_f32 v218, v218, v20\n"
"v_mul_f32 v219, v219, v20\n"
"v_mul_f32 v220, v220, v20\n"
"s_waitcnt lgkmcnt(4)\n"
"v_mfma_f32_32x32x16_bf16 v[128:143], a[200:203], v[124:127], v[128:143]\n"
"ds_read_b64_tr_b16 a[216:217], v9 offset:55296\n"
"ds_read_b64_tr_b16 a[218:219], v9 offset:55808\n"
"v_mul_f32 v221, v221, v20\n"
"v_mul_f32 v222, v222, v20\n"
"v_mul_f32 v223, v223, v20\n"
"v_cvt_pk_bf16_f32 v208, v216, v217\n"
"v_cvt_pk_bf16_f32 v209, v218, v219\n"
"v_cvt_pk_bf16_f32 v210, v220, v221\n"
"v_cvt_pk_bf16_f32 v211, v222, v223\n"
"v_accvgpr_read_b32 v216, a40\n"
"v_accvgpr_read_b32 v217, a41\n"
"v_accvgpr_read_b32 v218, a42\n"
"v_accvgpr_read_b32 v219, a43\n"
"v_accvgpr_read_b32 v220, a44\n"
"v_accvgpr_read_b32 v221, a45\n"
"v_accvgpr_read_b32 v222, a46\n"
"v_mfma_f32_32x32x16_bf16 v[144:159], a[204:207], v[124:127], v[144:159]\n"
"buffer_load_dwordx4 v4, s[52:55], s69 offen offset:3072 lds\n"
"ds_read_b64_tr_b16 a[220:221], v10 offset:55296\n"
"ds_read_b64_tr_b16 a[222:223], v10 offset:55808\n"
"v_accvgpr_read_b32 v223, a47\n"
"v_mul_f32 v216, v216, v20\n"
"v_mul_f32 v217, v217, v20\n"
"v_mul_f32 v218, v218, v20\n"
"v_mul_f32 v219, v219, v20\n"
"v_mul_f32 v220, v220, v20\n"
"s_waitcnt lgkmcnt(4)\n"
"v_mfma_f32_32x32x16_bf16 v[128:143], a[208:211], v[208:211], v[128:143]\n"
"s_waitcnt vmcnt(12)\n"
"s_barrier\n"
"v_xor_b32 v9, 0x10000, v9\n"
"ds_read_b64_tr_b16 a[192:193], v9 offset:0\n"
"ds_read_b64_tr_b16 a[194:195], v9 offset:512\n"
"v_mul_f32 v221, v221, v20\n"
"v_mul_f32 v222, v222, v20\n"
"v_mul_f32 v223, v223, v20\n"
"v_cvt_pk_bf16_f32 v212, v216, v217\n"
"v_cvt_pk_bf16_f32 v213, v218, v219\n"
"v_cvt_pk_bf16_f32 v214, v220, v221\n"
"v_cvt_pk_bf16_f32 v215, v222, v223\n"
"v_mfma_f32_32x32x16_bf16 v[144:159], a[212:215], v[208:211], v[144:159]\n"
"s_add_u32 m0, s68, 81920\n"
"s_mov_b32 s69, 57600\n"
"buffer_load_dwordx4 v1, s[52:55], s69 offen offset:0 lds\n"
"v_xor_b32 v10, 0x10000, v10\n"
"ds_read_b64_tr_b16 a[196:197], v10 offset:0\n"
"ds_read_b64_tr_b16 a[198:199], v10 offset:512\n"
"s_waitcnt lgkmcnt(4)\n"
"v_mfma_f32_32x32x16_bf16 v[128:143], a[216:219], v[212:215], v[128:143]\n"
"ds_read_b64_tr_b16 a[200:201], v9 offset:2048\n"
"ds_read_b64_tr_b16 a[202:203], v9 offset:2560\n"
"s_add_u32 s75, s68, 146432\n"
"v_add_u32 v17, s75, v23\n"
"s_xor_b32 s75, s68, 4096\n"
"s_add_u32 s75, s75, 146432\n"
"v_add_u32 v24, s75, v23\n"
"v_mfma_f32_32x32x16_bf16 v[144:159], a[220:223], v[212:215], v[144:159]\n"
"buffer_load_dwordx4 v2, s[52:55], s69 offen offset:1024 lds\n"
"ds_read_b64_tr_b16 a[204:205], v10 offset:2048\n"
"ds_read_b64_tr_b16 a[206:207], v10 offset:2560\n"
"s_waitcnt lgkmcnt(4)\n"
"v_mfma_f32_32x32x16_bf16 v[160:175], a[192:195], v[32:35], 0\n"
"ds_read_b64_tr_b16 a[208:209], v9 offset:4096\n"
"ds_read_b64_tr_b16 a[210:211], v9 offset:4608\n"
"v_mfma_f32_32x32x16_bf16 v[176:191], a[196:199], v[32:35], 0\n"
"buffer_load_dwordx4 v3, s[52:55], s69 offen offset:2048 lds\n"
"ds_read_b64_tr_b16 a[212:213], v10 offset:4096\n"
"ds_read_b64_tr_b16 a[214:215], v10 offset:4608\n"
"v_cvt_pk_bf16_f32 v128, v128, v129\n"
"s_waitcnt lgkmcnt(4)\n"
"v_mfma_f32_32x32x16_bf16 v[160:175], a[200:203], v[36:39], v[160:175]\n"
"ds_read_b64_tr_b16 a[216:217], v9 offset:6144\n"
"ds_read_b64_tr_b16 a[218:219], v9 offset:6656\n"
"v_cvt_pk_bf16_f32 v129, v130, v131\n"
"v_mfma_f32_32x32x16_bf16 v[176:191], a[204:207], v[36:39], v[176:191]\n"
"buffer_load_dwordx4 v4, s[52:55], s69 offen offset:3072 lds\n"
"ds_read_b64_tr_b16 a[220:221], v10 offset:6144\n"
"ds_read_b64_tr_b16 a[222:223], v10 offset:6656\n"
"v_cvt_pk_bf16_f32 v130, v132, v133\n"
"s_waitcnt lgkmcnt(4)\n"
"v_mfma_f32_32x32x16_bf16 v[160:175], a[208:211], v[48:51], v[160:175]\n"
"ds_read_b64_tr_b16 a[192:193], v9 offset:16384\n"
"ds_read_b64_tr_b16 a[194:195], v9 offset:16896\n"
"v_cvt_pk_bf16_f32 v131, v134, v135\n"
"v_mfma_f32_32x32x16_bf16 v[176:191], a[212:215], v[48:51], v[176:191]\n"
"s_add_u32 m0, s68, 98304\n"
"s_mov_b32 s69, 114944\n"
"buffer_load_dwordx4 v1, s[52:55], s69 offen offset:0 lds\n"
"ds_read_b64_tr_b16 a[196:197], v10 offset:16384\n"
"ds_read_b64_tr_b16 a[198:199], v10 offset:16896\n"
"ds_write_b128 v17, v[128:131] offset:0\n"
"v_cvt_pk_bf16_f32 v136, v136, v137\n"
"s_waitcnt lgkmcnt(5)\n"
"v_mfma_f32_32x32x16_bf16 v[160:175], a[216:219], v[52:55], v[160:175]\n"
"ds_read_b64_tr_b16 a[200:201], v9 offset:18432\n"
"ds_read_b64_tr_b16 a[202:203], v9 offset:18944\n"
"v_cvt_pk_bf16_f32 v137, v138, v139\n"
"v_mfma_f32_32x32x16_bf16 v[176:191], a[220:223], v[52:55], v[176:191]\n"
"buffer_load_dwordx4 v2, s[52:55], s69 offen offset:1024 lds\n"
"ds_read_b64_tr_b16 a[204:205], v10 offset:18432\n"
"ds_read_b64_tr_b16 a[206:207], v10 offset:18944\n"
"v_cvt_pk_bf16_f32 v138, v140, v141\n"
"s_waitcnt lgkmcnt(5)\n"
"v_mfma_f32_32x32x16_bf16 v[160:175], a[192:195], v[64:67], v[160:175]\n"
"ds_read_b64_tr_b16 a[208:209], v9 offset:20480\n"
"ds_read_b64_tr_b16 a[210:211], v9 offset:20992\n"
"v_cvt_pk_bf16_f32 v139, v142, v143\n"
"v_mfma_f32_32x32x16_bf16 v[176:191], a[196:199], v[64:67], v[176:191]\n"
"buffer_load_dwordx4 v3, s[52:55], s69 offen offset:2048 lds\n"
"ds_read_b64_tr_b16 a[212:213], v10 offset:20480\n"
"ds_read_b64_tr_b16 a[214:215], v10 offset:20992\n"
"ds_write_b128 v17, v[136:139] offset:1024\n"
"s_waitcnt lgkmcnt(5)\n"
"v_mfma_f32_32x32x16_bf16 v[160:175], a[200:203], v[68:71], v[160:175]\n"
"ds_read_b64_tr_b16 a[216:217], v9 offset:22528\n"
"ds_read_b64_tr_b16 a[218:219], v9 offset:23040\n"
"v_mfma_f32_32x32x16_bf16 v[176:191], a[204:207], v[68:71], v[176:191]\n"
"buffer_load_dwordx4 v4, s[52:55], s69 offen offset:3072 lds\n"
"ds_read_b64_tr_b16 a[220:221], v10 offset:22528\n"
"ds_read_b64_tr_b16 a[222:223], v10 offset:23040\n"
"s_waitcnt lgkmcnt(5)\n"
"v_mfma_f32_32x32x16_bf16 v[160:175], a[208:211], v[80:83], v[160:175]\n"
"s_waitcnt vmcnt(12) lgkmcnt(4)\n"
"s_barrier\n"
"ds_read_b64_tr_b16 a[192:193], v9 offset:32768\n"
"ds_read_b64_tr_b16 a[194:195], v9 offset:33280\n"
"ds_read_b128 a[48:51], v24 offset:0\n"
"ds_read_b128 a[52:55], v24 offset:1024\n"
"v_mfma_f32_32x32x16_bf16 v[176:191], a[212:215], v[80:83], v[176:191]\n"
"s_add_u32 m0, s68, 114688\n"
"s_mov_b32 s69, 172288\n"
"buffer_load_dwordx4 v1, s[52:55], s69 offen offset:0 lds\n"
"ds_read_b64_tr_b16 a[196:197], v10 offset:32768\n"
"ds_read_b64_tr_b16 a[198:199], v10 offset:33280\n"
"s_waitcnt lgkmcnt(6)\n"
"v_mfma_f32_32x32x16_bf16 v[160:175], a[216:219], v[84:87], v[160:175]\n"
"ds_read_b64_tr_b16 a[200:201], v9 offset:34816\n"
"ds_read_b64_tr_b16 a[202:203], v9 offset:35328\n"
"v_mfma_f32_32x32x16_bf16 v[176:191], a[220:223], v[84:87], v[176:191]\n"
"buffer_load_dwordx4 v2, s[52:55], s69 offen offset:1024 lds\n"
"ds_read_b64_tr_b16 a[204:205], v10 offset:34816\n"
"ds_read_b64_tr_b16 a[206:207], v10 offset:35328\n"
"s_waitcnt lgkmcnt(7)\n"
"v_mfma_f32_32x32x16_bf16 v[144:159], a[240:243], a[48:51], v[144:159]\n"
"s_waitcnt lgkmcnt(6)\n"
"v_mfma_f32_32x32x16_bf16 v[144:159], a[244:247], a[52:55], v[144:159]\n"
"s_waitcnt lgkmcnt(4)\n"
"v_mfma_f32_32x32x16_bf16 v[160:175], a[192:195], v[96:99], v[160:175]\n"
"ds_read_b64_tr_b16 a[208:209], v9 offset:36864\n"
"ds_read_b64_tr_b16 a[210:211], v9 offset:37376\n"
"v_mfma_f32_32x32x16_bf16 v[176:191], a[196:199], v[96:99], v[176:191]\n"
"buffer_load_dwordx4 v3, s[52:55], s69 offen offset:2048 lds\n"
"ds_read_b64_tr_b16 a[212:213], v10 offset:36864\n"
"ds_read_b64_tr_b16 a[214:215], v10 offset:37376\n"
"s_waitcnt lgkmcnt(4)\n"
"v_mfma_f32_32x32x16_bf16 v[160:175], a[200:203], v[100:103], v[160:175]\n"
"ds_read_b64_tr_b16 a[216:217], v9 offset:38912\n"
"ds_read_b64_tr_b16 a[218:219], v9 offset:39424\n"
"s_mov_b32 s74, 0\n"
"v_mfma_f32_32x32x16_bf16 v[176:191], a[204:207], v[100:103], v[176:191]\n"
"buffer_load_dwordx4 v4, s[52:55], s69 offen offset:3072 lds\n"
"ds_read_b64_tr_b16 a[220:221], v10 offset:38912\n"
"ds_read_b64_tr_b16 a[222:223], v10 offset:39424\n"
"v_cvt_pk_bf16_f32 v144, v144, v145\n"
"s_waitcnt lgkmcnt(4)\n"
"v_mfma_f32_32x32x16_bf16 v[160:175], a[208:211], v[112:115], v[160:175]\n"
"ds_read_b64_tr_b16 a[192:193], v9 offset:49152\n"
"ds_read_b64_tr_b16 a[194:195], v9 offset:49664\n"
"v_cvt_pk_bf16_f32 v145, v146, v147\n"
"v_cvt_pk_bf16_f32 v146, v148, v149\n"
"v_mfma_f32_32x32x16_bf16 v[176:191], a[212:215], v[112:115], v[176:191]\n"
"s_add_u32 m0, s68, 0\n"
"s_mov_b32 s69, 384\n"
"buffer_load_dwordx4 v1, s[52:55], s69 offen offset:0 lds\n"
"ds_read_b64_tr_b16 a[196:197], v10 offset:49152\n"
"ds_read_b64_tr_b16 a[198:199], v10 offset:49664\n"
"v_cvt_pk_bf16_f32 v147, v150, v151\n"
"s_waitcnt lgkmcnt(4)\n"
"v_mfma_f32_32x32x16_bf16 v[160:175], a[216:219], v[116:119], v[160:175]\n"
"ds_read_b64_tr_b16 a[200:201], v9 offset:51200\n"
"ds_read_b64_tr_b16 a[202:203], v9 offset:51712\n"
"buffer_store_dwordx4 v[144:147], v23, s[64:67], s74 offen offset:0 sc1\n"
"v_mfma_f32_32x32x16_bf16 v[176:191], a[220:223], v[116:119], v[176:191]\n"
"buffer_load_dwordx4 v2, s[52:55], s69 offen offset:1024 lds\n"
"ds_read_b64_tr_b16 a[204:205], v10 offset:51200\n"
"ds_read_b64_tr_b16 a[206:207], v10 offset:51712\n"
"v_cvt_pk_bf16_f32 v152, v152, v153\n"
"v_lshrrev_b32 v30, 5, v0\n"
"s_waitcnt lgkmcnt(4)\n"
"v_mfma_f32_32x32x16_bf16 v[160:175], a[192:195], v[120:123], v[160:175]\n"
"ds_read_b64_tr_b16 a[208:209], v9 offset:53248\n"
"ds_read_b64_tr_b16 a[210:211], v9 offset:53760\n"
"v_cvt_pk_bf16_f32 v153, v154, v155\n"
"v_cvt_pk_bf16_f32 v154, v156, v157\n"
"v_mfma_f32_32x32x16_bf16 v[176:191], a[196:199], v[120:123], v[176:191]\n"
"buffer_load_dwordx4 v3, s[52:55], s69 offen offset:2048 lds\n"
"ds_read_b64_tr_b16 a[212:213], v10 offset:53248\n"
"ds_read_b64_tr_b16 a[214:215], v10 offset:53760\n"
"v_cvt_pk_bf16_f32 v155, v158, v159\n"
"v_bfe_u32 v29, v0, 2, 2\n"
"s_waitcnt lgkmcnt(4)\n"
"v_mfma_f32_32x32x16_bf16 v[160:175], a[200:203], v[124:127], v[160:175]\n"
"ds_read_b64_tr_b16 a[216:217], v9 offset:55296\n"
"ds_read_b64_tr_b16 a[218:219], v9 offset:55808\n"
"buffer_store_dwordx4 v[152:155], v23, s[64:67], s74 offen offset:1024 sc1\n"
"v_mfma_f32_32x32x16_bf16 v[176:191], a[204:207], v[124:127], v[176:191]\n"
"buffer_load_dwordx4 v4, s[52:55], s69 offen offset:3072 lds\n"
"ds_read_b64_tr_b16 a[220:221], v10 offset:55296\n"
"ds_read_b64_tr_b16 a[222:223], v10 offset:55808\n"
"v_bfe_u32 v26, v0, 4, 1\n"
"s_waitcnt lgkmcnt(4)\n"
"v_mfma_f32_32x32x16_bf16 v[160:175], a[208:211], v[208:211], v[160:175]\n"
"s_waitcnt vmcnt(14)\n"
"s_barrier\n"
"v_xor_b32 v9, 0x10000, v9\n"
"ds_read_b64_tr_b16 a[192:193], v9 offset:0\n"
"ds_read_b64_tr_b16 a[194:195], v9 offset:512\n"
"v_mfma_f32_32x32x16_bf16 v[176:191], a[212:215], v[208:211], v[176:191]\n"
"s_add_u32 m0, s68, 16384\n"
"s_mov_b32 s69, 57728\n"
"buffer_load_dwordx4 v1, s[52:55], s69 offen offset:0 lds\n"
"v_xor_b32 v10, 0x10000, v10\n"
"ds_read_b64_tr_b16 a[196:197], v10 offset:0\n"
"ds_read_b64_tr_b16 a[198:199], v10 offset:512\n"
"v_lshlrev_b32 v26, 5, v26\n"
"s_waitcnt lgkmcnt(4)\n"
"v_mfma_f32_32x32x16_bf16 v[160:175], a[216:219], v[212:215], v[160:175]\n"
"ds_read_b64_tr_b16 a[200:201], v9 offset:2048\n"
"ds_read_b64_tr_b16 a[202:203], v9 offset:2560\n"
"v_mfma_f32_32x32x16_bf16 v[176:191], a[220:223], v[212:215], v[176:191]\n"
"buffer_load_dwordx4 v2, s[52:55], s69 offen offset:1024 lds\n"
"ds_read_b64_tr_b16 a[204:205], v10 offset:2048\n"
"ds_read_b64_tr_b16 a[206:207], v10 offset:2560\n"
"v_and_b32 v27, 1, v0\n"
"s_waitcnt lgkmcnt(4)\n"
"v_mfma_f32_32x32x16_bf16 v[128:143], a[192:195], v[32:35], 0\n"
"ds_read_b64_tr_b16 a[208:209], v9 offset:4096\n"
"ds_read_b64_tr_b16 a[210:211], v9 offset:4608\n"
"v_mfma_f32_32x32x16_bf16 v[144:159], a[196:199], v[32:35], 0\n"
"buffer_load_dwordx4 v3, s[52:55], s69 offen offset:2048 lds\n"
"ds_read_b64_tr_b16 a[212:213], v10 offset:4096\n"
"ds_read_b64_tr_b16 a[214:215], v10 offset:4608\n"
"v_cvt_pk_bf16_f32 v160, v160, v161\n"
"v_lshl_or_b32 v26, v27, 4, v26\n"
"s_waitcnt lgkmcnt(4)\n"
"v_mfma_f32_32x32x16_bf16 v[128:143], a[200:203], v[36:39], v[128:143]\n"
"ds_read_b64_tr_b16 a[216:217], v9 offset:6144\n"
"ds_read_b64_tr_b16 a[218:219], v9 offset:6656\n"
"v_cvt_pk_bf16_f32 v161, v162, v163\n"
"v_mfma_f32_32x32x16_bf16 v[144:159], a[204:207], v[36:39], v[144:159]\n"
"buffer_load_dwordx4 v4, s[52:55], s69 offen offset:3072 lds\n"
"ds_read_b64_tr_b16 a[220:221], v10 offset:6144\n"
"ds_read_b64_tr_b16 a[222:223], v10 offset:6656\n"
"v_cvt_pk_bf16_f32 v162, v164, v165\n"
"s_waitcnt lgkmcnt(4)\n"
"v_mfma_f32_32x32x16_bf16 v[128:143], a[208:211], v[48:51], v[128:143]\n"
"ds_read_b64_tr_b16 a[192:193], v9 offset:16384\n"
"ds_read_b64_tr_b16 a[194:195], v9 offset:16896\n"
"v_cvt_pk_bf16_f32 v163, v166, v167\n"
"v_bfe_u32 v27, v0, 1, 1\n"
"v_mfma_f32_32x32x16_bf16 v[144:159], a[212:215], v[48:51], v[144:159]\n"
"s_add_u32 m0, s68, 32768\n"
"s_mov_b32 s69, 115072\n"
"buffer_load_dwordx4 v1, s[52:55], s69 offen offset:0 lds\n"
"ds_read_b64_tr_b16 a[196:197], v10 offset:16384\n"
"ds_read_b64_tr_b16 a[198:199], v10 offset:16896\n"
"ds_write_b128 v17, v[160:163] offset:2048\n"
"v_cvt_pk_bf16_f32 v168, v168, v169\n"
"s_waitcnt lgkmcnt(5)\n"
"v_mfma_f32_32x32x16_bf16 v[128:143], a[216:219], v[52:55], v[128:143]\n"
"ds_read_b64_tr_b16 a[200:201], v9 offset:18432\n"
"ds_read_b64_tr_b16 a[202:203], v9 offset:18944\n"
"v_cvt_pk_bf16_f32 v169, v170, v171\n"
"v_lshl_or_b32 v26, v27, 3, v26\n"
"v_mfma_f32_32x32x16_bf16 v[144:159], a[220:223], v[52:55], v[144:159]\n"
"buffer_load_dwordx4 v2, s[52:55], s69 offen offset:1024 lds\n"
"ds_read_b64_tr_b16 a[204:205], v10 offset:18432\n"
"ds_read_b64_tr_b16 a[206:207], v10 offset:18944\n"
"v_cvt_pk_bf16_f32 v170, v172, v173\n"
"s_waitcnt lgkmcnt(5)\n"
"v_mfma_f32_32x32x16_bf16 v[128:143], a[192:195], v[64:67], v[128:143]\n"
"ds_read_b64_tr_b16 a[208:209], v9 offset:20480\n"
"ds_read_b64_tr_b16 a[210:211], v9 offset:20992\n"
"v_cvt_pk_bf16_f32 v171, v174, v175\n"
"v_lshl_add_u32 v27, v30, 3, v29\n"
"v_mfma_f32_32x32x16_bf16 v[144:159], a[196:199], v[64:67], v[144:159]\n"
"buffer_load_dwordx4 v3, s[52:55], s69 offen offset:2048 lds\n"
"ds_read_b64_tr_b16 a[212:213], v10 offset:20480\n"
"ds_read_b64_tr_b16 a[214:215], v10 offset:20992\n"
"ds_write_b128 v17, v[168:171] offset:3072\n"
"s_waitcnt lgkmcnt(5)\n"
"v_mfma_f32_32x32x16_bf16 v[128:143], a[200:203], v[68:71], v[128:143]\n"
"ds_read_b64_tr_b16 a[216:217], v9 offset:22528\n"
"ds_read_b64_tr_b16 a[218:219], v9 offset:23040\n"
"v_lshl_add_u32 v28, v27, 9, v26\n"
"v_mfma_f32_32x32x16_bf16 v[144:159], a[204:207], v[68:71], v[144:159]\n"
"buffer_load_dwordx4 v4, s[52:55], s69 offen offset:3072 lds\n"
"ds_read_b64_tr_b16 a[220:221], v10 offset:22528\n"
"ds_read_b64_tr_b16 a[222:223], v10 offset:23040\n"
"s_waitcnt lgkmcnt(5)\n"
"v_mfma_f32_32x32x16_bf16 v[128:143], a[208:211], v[80:83], v[128:143]\n"
"s_waitcnt vmcnt(14) lgkmcnt(4)\n"
"s_barrier\n"
"ds_read_b64_tr_b16 a[192:193], v9 offset:32768\n"
"ds_read_b64_tr_b16 a[194:195], v9 offset:33280\n"
"ds_read_b128 a[48:51], v24 offset:2048\n"
"ds_read_b128 a[52:55], v24 offset:3072\n"
"v_lshl_add_u32 v28, %[ss], 8, v28\n"
"v_mfma_f32_32x32x16_bf16 v[144:159], a[212:215], v[80:83], v[144:159]\n"
"s_add_u32 m0, s68, 49152\n"
"s_mov_b32 s69, 172416\n"
"buffer_load_dwordx4 v1, s[52:55], s69 offen offset:0 lds\n"
"ds_read_b64_tr_b16 a[196:197], v10 offset:32768\n"
"ds_read_b64_tr_b16 a[198:199], v10 offset:33280\n"
"s_waitcnt lgkmcnt(6)\n"
"v_mfma_f32_32x32x16_bf16 v[128:143], a[216:219], v[84:87], v[128:143]\n"
"ds_read_b64_tr_b16 a[200:201], v9 offset:34816\n"
"ds_read_b64_tr_b16 a[202:203], v9 offset:35328\n"
"v_xor_b32 v27, 0, v29\n"
"v_mfma_f32_32x32x16_bf16 v[144:159], a[220:223], v[84:87], v[144:159]\n"
"buffer_load_dwordx4 v2, s[52:55], s69 offen offset:1024 lds\n"
"ds_read_b64_tr_b16 a[204:205], v10 offset:34816\n"
"ds_read_b64_tr_b16 a[206:207], v10 offset:35328\n"
"s_waitcnt lgkmcnt(7)\n"
"v_mfma_f32_32x32x16_bf16 v[176:191], a[240:243], a[48:51], v[176:191]\n"
"s_waitcnt lgkmcnt(6)\n"
"v_mfma_f32_32x32x16_bf16 v[176:191], a[244:247], a[52:55], v[176:191]\n"
"s_waitcnt lgkmcnt(4)\n"
"v_mfma_f32_32x32x16_bf16 v[128:143], a[192:195], v[96:99], v[128:143]\n"
"ds_read_b64_tr_b16 a[208:209], v9 offset:36864\n"
"ds_read_b64_tr_b16 a[210:211], v9 offset:37376\n"
"v_lshl_add_u32 v5, v27, 6, v28\n"
"v_mfma_f32_32x32x16_bf16 v[144:159], a[196:199], v[96:99], v[144:159]\n"
"buffer_load_dwordx4 v3, s[52:55], s69 offen offset:2048 lds\n"
"ds_read_b64_tr_b16 a[212:213], v10 offset:36864\n"
"ds_read_b64_tr_b16 a[214:215], v10 offset:37376\n"
"s_waitcnt lgkmcnt(4)\n"
"v_mfma_f32_32x32x16_bf16 v[128:143], a[200:203], v[100:103], v[128:143]\n"
"ds_read_b64_tr_b16 a[216:217], v9 offset:38912\n"
"ds_read_b64_tr_b16 a[218:219], v9 offset:39424\n"
"s_mov_b32 s74, 4096\n"
"v_mfma_f32_32x32x16_bf16 v[144:159], a[204:207], v[100:103], v[144:159]\n"
"buffer_load_dwordx4 v4, s[52:55], s69 offen offset:3072 lds\n"
"ds_read_b64_tr_b16 a[220:221], v10 offset:38912\n"
"ds_read_b64_tr_b16 a[222:223], v10 offset:39424\n"
"v_cvt_pk_bf16_f32 v176, v176, v177\n"
"v_xor_b32 v27, 1, v29\n"
"s_waitcnt lgkmcnt(4)\n"
"v_mfma_f32_32x32x16_bf16 v[128:143], a[208:211], v[112:115], v[128:143]\n"
"ds_read_b64_tr_b16 a[192:193], v9 offset:49152\n"
"ds_read_b64_tr_b16 a[194:195], v9 offset:49664\n"
"v_cvt_pk_bf16_f32 v177, v178, v179\n"
"v_cvt_pk_bf16_f32 v178, v180, v181\n"
"v_mfma_f32_32x32x16_bf16 v[144:159], a[212:215], v[112:115], v[144:159]\n"
"s_add_u32 m0, s68, 65536\n"
"s_mov_b32 s69, 512\n"
"buffer_load_dwordx4 v1, s[52:55], s69 offen offset:0 lds\n"
"ds_read_b64_tr_b16 a[196:197], v10 offset:49152\n"
"ds_read_b64_tr_b16 a[198:199], v10 offset:49664\n"
"v_cvt_pk_bf16_f32 v179, v182, v183\n"
"v_lshl_add_u32 v6, v27, 6, v28\n"
"s_waitcnt lgkmcnt(4)\n"
"v_mfma_f32_32x32x16_bf16 v[128:143], a[216:219], v[116:119], v[128:143]\n"
"ds_read_b64_tr_b16 a[200:201], v9 offset:51200\n"
"ds_read_b64_tr_b16 a[202:203], v9 offset:51712\n"
"buffer_store_dwordx4 v[176:179], v23, s[64:67], s74 offen offset:0 sc1\n"
"v_mfma_f32_32x32x16_bf16 v[144:159], a[220:223], v[116:119], v[144:159]\n"
"buffer_load_dwordx4 v2, s[52:55], s69 offen offset:1024 lds\n"
"ds_read_b64_tr_b16 a[204:205], v10 offset:51200\n"
"ds_read_b64_tr_b16 a[206:207], v10 offset:51712\n"
"v_cvt_pk_bf16_f32 v184, v184, v185\n"
"v_xor_b32 v27, 2, v29\n"
"s_waitcnt lgkmcnt(4)\n"
"v_mfma_f32_32x32x16_bf16 v[128:143], a[192:195], v[120:123], v[128:143]\n"
"ds_read_b64_tr_b16 a[208:209], v9 offset:53248\n"
"ds_read_b64_tr_b16 a[210:211], v9 offset:53760\n"
"v_cvt_pk_bf16_f32 v185, v186, v187\n"
"v_cvt_pk_bf16_f32 v186, v188, v189\n"
"v_mfma_f32_32x32x16_bf16 v[144:159], a[196:199], v[120:123], v[144:159]\n"
"buffer_load_dwordx4 v3, s[52:55], s69 offen offset:2048 lds\n"
"ds_read_b64_tr_b16 a[212:213], v10 offset:53248\n"
"ds_read_b64_tr_b16 a[214:215], v10 offset:53760\n"
"v_cvt_pk_bf16_f32 v187, v190, v191\n"
"v_lshl_add_u32 v7, v27, 6, v28\n"
"s_waitcnt lgkmcnt(4)\n"
"v_mfma_f32_32x32x16_bf16 v[128:143], a[200:203], v[124:127], v[128:143]\n"
"ds_read_b64_tr_b16 a[216:217], v9 offset:55296\n"
"ds_read_b64_tr_b16 a[218:219], v9 offset:55808\n"
"buffer_store_dwordx4 v[184:187], v23, s[64:67], s74 offen offset:1024 sc1\n"
"v_mfma_f32_32x32x16_bf16 v[144:159], a[204:207], v[124:127], v[144:159]\n"
"buffer_load_dwordx4 v4, s[52:55], s69 offen offset:3072 lds\n"
"ds_read_b64_tr_b16 a[220:221], v10 offset:55296\n"
"ds_read_b64_tr_b16 a[222:223], v10 offset:55808\n"
"v_xor_b32 v27, 3, v29\n"
"s_waitcnt lgkmcnt(4)\n"
"v_mfma_f32_32x32x16_bf16 v[128:143], a[208:211], v[208:211], v[128:143]\n"
"s_waitcnt vmcnt(14)\n"
"s_barrier\n"
"v_xor_b32 v9, 0x10000, v9\n"
"ds_read_b64_tr_b16 a[192:193], v9 offset:0\n"
"ds_read_b64_tr_b16 a[194:195], v9 offset:512\n"
"v_mfma_f32_32x32x16_bf16 v[144:159], a[212:215], v[208:211], v[144:159]\n"
"s_add_u32 m0, s68, 81920\n"
"s_mov_b32 s69, 57856\n"
"buffer_load_dwordx4 v1, s[52:55], s69 offen offset:0 lds\n"
"v_xor_b32 v10, 0x10000, v10\n"
"ds_read_b64_tr_b16 a[196:197], v10 offset:0\n"
"ds_read_b64_tr_b16 a[198:199], v10 offset:512\n"
"v_lshl_add_u32 v8, v27, 6, v28\n"
"s_waitcnt lgkmcnt(4)\n"
"v_mfma_f32_32x32x16_bf16 v[128:143], a[216:219], v[212:215], v[128:143]\n"
"ds_read_b64_tr_b16 a[200:201], v9 offset:2048\n"
"ds_read_b64_tr_b16 a[202:203], v9 offset:2560\n"
"v_mfma_f32_32x32x16_bf16 v[144:159], a[220:223], v[212:215], v[144:159]\n"
"buffer_load_dwordx4 v2, s[52:55], s69 offen offset:1024 lds\n"
"ds_read_b64_tr_b16 a[204:205], v10 offset:2048\n"
"ds_read_b64_tr_b16 a[206:207], v10 offset:2560\n"
"s_waitcnt lgkmcnt(4)\n"
"v_mfma_f32_32x32x16_bf16 v[160:175], a[192:195], v[32:35], 0\n"
"ds_read_b64_tr_b16 a[208:209], v9 offset:4096\n"
"ds_read_b64_tr_b16 a[210:211], v9 offset:4608\n"
"v_mfma_f32_32x32x16_bf16 v[176:191], a[196:199], v[32:35], 0\n"
"buffer_load_dwordx4 v3, s[52:55], s69 offen offset:2048 lds\n"
"ds_read_b64_tr_b16 a[212:213], v10 offset:4096\n"
"ds_read_b64_tr_b16 a[214:215], v10 offset:4608\n"
"v_cvt_pk_bf16_f32 v128, v128, v129\n"
"s_waitcnt lgkmcnt(4)\n"
"v_mfma_f32_32x32x16_bf16 v[160:175], a[200:203], v[36:39], v[160:175]\n"
"ds_read_b64_tr_b16 a[216:217], v9 offset:6144\n"
"ds_read_b64_tr_b16 a[218:219], v9 offset:6656\n"
"v_cvt_pk_bf16_f32 v129, v130, v131\n"
"v_mfma_f32_32x32x16_bf16 v[176:191], a[204:207], v[36:39], v[176:191]\n"
"buffer_load_dwordx4 v4, s[52:55], s69 offen offset:3072 lds\n"
"ds_read_b64_tr_b16 a[220:221], v10 offset:6144\n"
"ds_read_b64_tr_b16 a[222:223], v10 offset:6656\n"
"v_cvt_pk_bf16_f32 v130, v132, v133\n"
"v_accvgpr_read_b32 v248, a160\n"
"s_waitcnt lgkmcnt(4)\n"
"v_mfma_f32_32x32x16_bf16 v[160:175], a[208:211], v[48:51], v[160:175]\n"
"ds_read_b64_tr_b16 a[192:193], v9 offset:16384\n"
"ds_read_b64_tr_b16 a[194:195], v9 offset:16896\n"
"v_cvt_pk_bf16_f32 v131, v134, v135\n"
"v_mfma_f32_32x32x16_bf16 v[176:191], a[212:215], v[48:51], v[176:191]\n"
"s_add_u32 m0, s68, 98304\n"
"s_mov_b32 s69, 115200\n"
"buffer_load_dwordx4 v1, s[52:55], s69 offen offset:0 lds\n"
"ds_read_b64_tr_b16 a[196:197], v10 offset:16384\n"
"ds_read_b64_tr_b16 a[198:199], v10 offset:16896\n"
"ds_write_b128 v17, v[128:131] offset:0\n"
"v_cvt_pk_bf16_f32 v136, v136, v137\n"
"s_waitcnt lgkmcnt(5)\n"
"v_mfma_f32_32x32x16_bf16 v[160:175], a[216:219], v[52:55], v[160:175]\n"
"ds_read_b64_tr_b16 a[200:201], v9 offset:18432\n"
"ds_read_b64_tr_b16 a[202:203], v9 offset:18944\n"
"v_cvt_pk_bf16_f32 v137, v138, v139\n"
"v_mfma_f32_32x32x16_bf16 v[176:191], a[220:223], v[52:55], v[176:191]\n"
"buffer_load_dwordx4 v2, s[52:55], s69 offen offset:1024 lds\n"
"ds_read_b64_tr_b16 a[204:205], v10 offset:18432\n"
"ds_read_b64_tr_b16 a[206:207], v10 offset:18944\n"
"v_cvt_pk_bf16_f32 v138, v140, v141\n"
"v_accvgpr_read_b32 v249, a161\n"
"s_waitcnt lgkmcnt(5)\n"
"v_mfma_f32_32x32x16_bf16 v[160:175], a[192:195], v[64:67], v[160:175]\n"
"ds_read_b64_tr_b16 a[208:209], v9 offset:20480\n"
"ds_read_b64_tr_b16 a[210:211], v9 offset:20992\n"
"v_cvt_pk_bf16_f32 v139, v142, v143\n"
"v_mfma_f32_32x32x16_bf16 v[176:191], a[196:199], v[64:67], v[176:191]\n"
"buffer_load_dwordx4 v3, s[52:55], s69 offen offset:2048 lds\n"
"ds_read_b64_tr_b16 a[212:213], v10 offset:20480\n"
"ds_read_b64_tr_b16 a[214:215], v10 offset:20992\n"
"ds_write_b128 v17, v[136:139] offset:1024\n"
"s_waitcnt lgkmcnt(5)\n"
"v_mfma_f32_32x32x16_bf16 v[160:175], a[200:203], v[68:71], v[160:175]\n"
"ds_read_b64_tr_b16 a[216:217], v9 offset:22528\n"
"ds_read_b64_tr_b16 a[218:219], v9 offset:23040\n"
"v_accvgpr_read_b32 v250, a162\n"
"v_mfma_f32_32x32x16_bf16 v[176:191], a[204:207], v[68:71], v[176:191]\n"
"buffer_load_dwordx4 v4, s[52:55], s69 offen offset:3072 lds\n"
"ds_read_b64_tr_b16 a[220:221], v10 offset:22528\n"
"ds_read_b64_tr_b16 a[222:223], v10 offset:23040\n"
"s_waitcnt lgkmcnt(5)\n"
"v_mfma_f32_32x32x16_bf16 v[160:175], a[208:211], v[80:83], v[160:175]\n"
"s_waitcnt vmcnt(14) lgkmcnt(4)\n"
"s_barrier\n"
"ds_read_b64_tr_b16 a[192:193], v9 offset:32768\n"
"ds_read_b64_tr_b16 a[194:195], v9 offset:33280\n"
"ds_read_b128 a[48:51], v24 offset:0\n"
"ds_read_b128 a[52:55], v24 offset:1024\n"
"v_mfma_f32_32x32x16_bf16 v[176:191], a[212:215], v[80:83], v[176:191]\n"
"s_add_u32 m0, s68, 114688\n"
"s_mov_b32 s69, 172544\n"
"buffer_load_dwordx4 v1, s[52:55], s69 offen offset:0 lds\n"
"ds_read_b64_tr_b16 a[196:197], v10 offset:32768\n"
"ds_read_b64_tr_b16 a[198:199], v10 offset:33280\n"
"v_accvgpr_read_b32 v251, a163\n"
"s_waitcnt lgkmcnt(6)\n"
"v_mfma_f32_32x32x16_bf16 v[160:175], a[216:219], v[84:87], v[160:175]\n"
"ds_read_b64_tr_b16 a[200:201], v9 offset:34816\n"
"ds_read_b64_tr_b16 a[202:203], v9 offset:35328\n"
"v_mfma_f32_32x32x16_bf16 v[176:191], a[220:223], v[84:87], v[176:191]\n"
"buffer_load_dwordx4 v2, s[52:55], s69 offen offset:1024 lds\n"
"ds_read_b64_tr_b16 a[204:205], v10 offset:34816\n"
"ds_read_b64_tr_b16 a[206:207], v10 offset:35328\n"
"s_waitcnt lgkmcnt(7)\n"
"v_mfma_f32_32x32x16_bf16 v[144:159], a[240:243], a[48:51], v[144:159]\n"
"s_waitcnt lgkmcnt(6)\n"
"v_mfma_f32_32x32x16_bf16 v[144:159], a[244:247], a[52:55], v[144:159]\n"
"s_waitcnt lgkmcnt(4)\n"
"v_mfma_f32_32x32x16_bf16 v[160:175], a[192:195], v[96:99], v[160:175]\n"
"ds_read_b64_tr_b16 a[208:209], v9 offset:36864\n"
"ds_read_b64_tr_b16 a[210:211], v9 offset:37376\n"
"v_mfma_f32_32x32x16_bf16 v[176:191], a[196:199], v[96:99], v[176:191]\n"
"buffer_load_dwordx4 v3, s[52:55], s69 offen offset:2048 lds\n"
"ds_read_b64_tr_b16 a[212:213], v10 offset:36864\n"
"ds_read_b64_tr_b16 a[214:215], v10 offset:37376\n"
"v_accvgpr_read_b32 v252, a164\n"
"s_waitcnt lgkmcnt(4)\n"
"v_mfma_f32_32x32x16_bf16 v[160:175], a[200:203], v[100:103], v[160:175]\n"
"ds_read_b64_tr_b16 a[216:217], v9 offset:38912\n"
"ds_read_b64_tr_b16 a[218:219], v9 offset:39424\n"
"s_mov_b32 s74, 8192\n"
"v_mfma_f32_32x32x16_bf16 v[176:191], a[204:207], v[100:103], v[176:191]\n"
"buffer_load_dwordx4 v4, s[52:55], s69 offen offset:3072 lds\n"
"ds_read_b64_tr_b16 a[220:221], v10 offset:38912\n"
"ds_read_b64_tr_b16 a[222:223], v10 offset:39424\n"
"v_cvt_pk_bf16_f32 v144, v144, v145\n"
"s_waitcnt lgkmcnt(4)\n"
"v_mfma_f32_32x32x16_bf16 v[160:175], a[208:211], v[112:115], v[160:175]\n"
"ds_read_b64_tr_b16 a[192:193], v9 offset:49152\n"
"ds_read_b64_tr_b16 a[194:195], v9 offset:49664\n"
"v_cvt_pk_bf16_f32 v145, v146, v147\n"
"v_cvt_pk_bf16_f32 v146, v148, v149\n"
"v_accvgpr_read_b32 v253, a165\n"
"v_mfma_f32_32x32x16_bf16 v[176:191], a[212:215], v[112:115], v[176:191]\n"
"s_add_u32 m0, s68, 0\n"
"s_mov_b32 s69, 640\n"
"buffer_load_dwordx4 v1, s[52:55], s69 offen offset:0 lds\n"
"ds_read_b64_tr_b16 a[196:197], v10 offset:49152\n"
"ds_read_b64_tr_b16 a[198:199], v10 offset:49664\n"
"v_cvt_pk_bf16_f32 v147, v150, v151\n"
"s_waitcnt lgkmcnt(4)\n"
"v_mfma_f32_32x32x16_bf16 v[160:175], a[216:219], v[116:119], v[160:175]\n"
"ds_read_b64_tr_b16 a[200:201], v9 offset:51200\n"
"ds_read_b64_tr_b16 a[202:203], v9 offset:51712\n"
"buffer_store_dwordx4 v[144:147], v23, s[64:67], s74 offen offset:0 sc1\n"
"v_mfma_f32_32x32x16_bf16 v[176:191], a[220:223], v[116:119], v[176:191]\n"
"buffer_load_dwordx4 v2, s[52:55], s69 offen offset:1024 lds\n"
"ds_read_b64_tr_b16 a[204:205], v10 offset:51200\n"
"ds_read_b64_tr_b16 a[206:207], v10 offset:51712\n"
"v_cvt_pk_bf16_f32 v152, v152, v153\n"
"v_accvgpr_read_b32 v254, a166\n"
"s_waitcnt lgkmcnt(4)\n"
"v_mfma_f32_32x32x16_bf16 v[160:175], a[192:195], v[120:123], v[160:175]\n"
"ds_read_b64_tr_b16 a[208:209], v9 offset:53248\n"
"ds_read_b64_tr_b16 a[210:211], v9 offset:53760\n"
"v_cvt_pk_bf16_f32 v153, v154, v155\n"
"v_cvt_pk_bf16_f32 v154, v156, v157\n"
"v_mfma_f32_32x32x16_bf16 v[176:191], a[196:199], v[120:123], v[176:191]\n"
"buffer_load_dwordx4 v3, s[52:55], s69 offen offset:2048 lds\n"
"ds_read_b64_tr_b16 a[212:213], v10 offset:53248\n"
"ds_read_b64_tr_b16 a[214:215], v10 offset:53760\n"
"v_cvt_pk_bf16_f32 v155, v158, v159\n"
"s_waitcnt lgkmcnt(4)\n"
"v_mfma_f32_32x32x16_bf16 v[160:175], a[200:203], v[124:127], v[160:175]\n"
"ds_read_b64_tr_b16 a[216:217], v9 offset:55296\n"
"ds_read_b64_tr_b16 a[218:219], v9 offset:55808\n"
"buffer_store_dwordx4 v[152:155], v23, s[64:67], s74 offen offset:1024 sc1\n"
"v_mfma_f32_32x32x16_bf16 v[176:191], a[204:207], v[124:127], v[176:191]\n"
"buffer_load_dwordx4 v4, s[52:55], s69 offen offset:3072 lds\n"
"ds_read_b64_tr_b16 a[220:221], v10 offset:55296\n"
"ds_read_b64_tr_b16 a[222:223], v10 offset:55808\n"
"v_accvgpr_read_b32 v255, a167\n"
"s_waitcnt lgkmcnt(4)\n"
"v_mfma_f32_32x32x16_bf16 v[160:175], a[208:211], v[208:211], v[160:175]\n"
"s_waitcnt vmcnt(14)\n"
"s_barrier\n"
"v_xor_b32 v9, 0x10000, v9\n"
"ds_read_b64_tr_b16 a[192:193], v9 offset:0\n"
"ds_read_b64_tr_b16 a[194:195], v9 offset:512\n"
"v_mfma_f32_32x32x16_bf16 v[176:191], a[212:215], v[208:211], v[176:191]\n"
"s_add_u32 m0, s68, 16384\n"
"s_mov_b32 s69, 57984\n"
"buffer_load_dwordx4 v1, s[52:55], s69 offen offset:0 lds\n"
"v_xor_b32 v10, 0x10000, v10\n"
"ds_read_b64_tr_b16 a[196:197], v10 offset:0\n"
"ds_read_b64_tr_b16 a[198:199], v10 offset:512\n"
"s_waitcnt lgkmcnt(4)\n"
"v_mfma_f32_32x32x16_bf16 v[160:175], a[216:219], v[212:215], v[160:175]\n"
"ds_read_b64_tr_b16 a[200:201], v9 offset:2048\n"
"ds_read_b64_tr_b16 a[202:203], v9 offset:2560\n"
"v_mfma_f32_32x32x16_bf16 v[176:191], a[220:223], v[212:215], v[176:191]\n"
"buffer_load_dwordx4 v2, s[52:55], s69 offen offset:1024 lds\n"
"ds_read_b64_tr_b16 a[204:205], v10 offset:2048\n"
"ds_read_b64_tr_b16 a[206:207], v10 offset:2560\n"
"s_waitcnt lgkmcnt(4)\n"
"v_mfma_f32_32x32x16_bf16 v[128:143], a[192:195], v[32:35], 0\n"
"ds_read_b64_tr_b16 a[208:209], v9 offset:4096\n"
"ds_read_b64_tr_b16 a[210:211], v9 offset:4608\n"
"v_mfma_f32_32x32x16_bf16 v[144:159], a[196:199], v[32:35], 0\n"
"buffer_load_dwordx4 v3, s[52:55], s69 offen offset:2048 lds\n"
"ds_read_b64_tr_b16 a[212:213], v10 offset:4096\n"
"ds_read_b64_tr_b16 a[214:215], v10 offset:4608\n"
"v_cvt_pk_bf16_f32 v160, v160, v161\n"
"s_waitcnt lgkmcnt(4)\n"
"v_mfma_f32_32x32x16_bf16 v[128:143], a[200:203], v[36:39], v[128:143]\n"
"ds_read_b64_tr_b16 a[216:217], v9 offset:6144\n"
"ds_read_b64_tr_b16 a[218:219], v9 offset:6656\n"
"v_cvt_pk_bf16_f32 v161, v162, v163\n"
"v_mfma_f32_32x32x16_bf16 v[144:159], a[204:207], v[36:39], v[144:159]\n"
"buffer_load_dwordx4 v4, s[52:55], s69 offen offset:3072 lds\n"
"ds_read_b64_tr_b16 a[220:221], v10 offset:6144\n"
"ds_read_b64_tr_b16 a[222:223], v10 offset:6656\n"
"v_cvt_pk_bf16_f32 v162, v164, v165\n"
"s_waitcnt lgkmcnt(4)\n"
"v_mfma_f32_32x32x16_bf16 v[128:143], a[208:211], v[48:51], v[128:143]\n"
"ds_read_b64_tr_b16 a[192:193], v9 offset:16384\n"
"ds_read_b64_tr_b16 a[194:195], v9 offset:16896\n"
"v_cvt_pk_bf16_f32 v163, v166, v167\n"
"v_mfma_f32_32x32x16_bf16 v[144:159], a[212:215], v[48:51], v[144:159]\n"
"s_add_u32 m0, s68, 32768\n"
"s_mov_b32 s69, 115328\n"
"buffer_load_dwordx4 v1, s[52:55], s69 offen offset:0 lds\n"
"ds_read_b64_tr_b16 a[196:197], v10 offset:16384\n"
"ds_read_b64_tr_b16 a[198:199], v10 offset:16896\n"
"ds_write_b128 v17, v[160:163] offset:2048\n"
"v_cvt_pk_bf16_f32 v168, v168, v169\n"
"s_waitcnt lgkmcnt(5)\n"
"v_mfma_f32_32x32x16_bf16 v[128:143], a[216:219], v[52:55], v[128:143]\n"
"ds_read_b64_tr_b16 a[200:201], v9 offset:18432\n"
"ds_read_b64_tr_b16 a[202:203], v9 offset:18944\n"
"v_cvt_pk_bf16_f32 v169, v170, v171\n"
"v_mfma_f32_32x32x16_bf16 v[144:159], a[220:223], v[52:55], v[144:159]\n"
"buffer_load_dwordx4 v2, s[52:55], s69 offen offset:1024 lds\n"
"ds_read_b64_tr_b16 a[204:205], v10 offset:18432\n"
"ds_read_b64_tr_b16 a[206:207], v10 offset:18944\n"
"v_cvt_pk_bf16_f32 v170, v172, v173\n"
"s_waitcnt lgkmcnt(5)\n"
"v_mfma_f32_32x32x16_bf16 v[128:143], a[192:195], v[64:67], v[128:143]\n"
"ds_read_b64_tr_b16 a[208:209], v9 offset:20480\n"
"ds_read_b64_tr_b16 a[210:211], v9 offset:20992\n"
"v_cvt_pk_bf16_f32 v171, v174, v175\n"
"v_mfma_f32_32x32x16_bf16 v[144:159], a[196:199], v[64:67], v[144:159]\n"
"buffer_load_dwordx4 v3, s[52:55], s69 offen offset:2048 lds\n"
"ds_read_b64_tr_b16 a[212:213], v10 offset:20480\n"
"ds_read_b64_tr_b16 a[214:215], v10 offset:20992\n"
"ds_write_b128 v17, v[168:171] offset:3072\n"
"s_waitcnt lgkmcnt(5)\n"
"v_mfma_f32_32x32x16_bf16 v[128:143], a[200:203], v[68:71], v[128:143]\n"
"ds_read_b64_tr_b16 a[216:217], v9 offset:22528\n"
"ds_read_b64_tr_b16 a[218:219], v9 offset:23040\n"
"v_mfma_f32_32x32x16_bf16 v[144:159], a[204:207], v[68:71], v[144:159]\n"
"buffer_load_dwordx4 v4, s[52:55], s69 offen offset:3072 lds\n"
"ds_read_b64_tr_b16 a[220:221], v10 offset:22528\n"
"ds_read_b64_tr_b16 a[222:223], v10 offset:23040\n"
"s_waitcnt lgkmcnt(5)\n"
"v_mfma_f32_32x32x16_bf16 v[128:143], a[208:211], v[80:83], v[128:143]\n"
"s_waitcnt vmcnt(14) lgkmcnt(4)\n"
"s_barrier\n"
"ds_read_b64_tr_b16 a[192:193], v9 offset:32768\n"
"ds_read_b64_tr_b16 a[194:195], v9 offset:33280\n"
"ds_read_b128 a[48:51], v24 offset:2048\n"
"ds_read_b128 a[52:55], v24 offset:3072\n"
"v_mfma_f32_32x32x16_bf16 v[144:159], a[212:215], v[80:83], v[144:159]\n"
"s_add_u32 m0, s68, 49152\n"
"s_mov_b32 s69, 172672\n"
"buffer_load_dwordx4 v1, s[52:55], s69 offen offset:0 lds\n"
"ds_read_b64_tr_b16 a[196:197], v10 offset:32768\n"
"ds_read_b64_tr_b16 a[198:199], v10 offset:33280\n"
"s_waitcnt lgkmcnt(6)\n"
"v_mfma_f32_32x32x16_bf16 v[128:143], a[216:219], v[84:87], v[128:143]\n"
"ds_read_b64_tr_b16 a[200:201], v9 offset:34816\n"
"ds_read_b64_tr_b16 a[202:203], v9 offset:35328\n"
"v_mfma_f32_32x32x16_bf16 v[144:159], a[220:223], v[84:87], v[144:159]\n"
"buffer_load_dwordx4 v2, s[52:55], s69 offen offset:1024 lds\n"
"ds_read_b64_tr_b16 a[204:205], v10 offset:34816\n"
"ds_read_b64_tr_b16 a[206:207], v10 offset:35328\n"
"s_waitcnt lgkmcnt(7)\n"
"v_mfma_f32_32x32x16_bf16 v[176:191], a[240:243], a[48:51], v[176:191]\n"
"s_waitcnt lgkmcnt(6)\n"
"v_mfma_f32_32x32x16_bf16 v[176:191], a[244:247], a[52:55], v[176:191]\n"
"s_waitcnt lgkmcnt(4)\n"
"v_mfma_f32_32x32x16_bf16 v[128:143], a[192:195], v[96:99], v[128:143]\n"
"ds_read_b64_tr_b16 a[208:209], v9 offset:36864\n"
"ds_read_b64_tr_b16 a[210:211], v9 offset:37376\n"
"v_mfma_f32_32x32x16_bf16 v[144:159], a[196:199], v[96:99], v[144:159]\n"
"buffer_load_dwordx4 v3, s[52:55], s69 offen offset:2048 lds\n"
"ds_read_b64_tr_b16 a[212:213], v10 offset:36864\n"
"ds_read_b64_tr_b16 a[214:215], v10 offset:37376\n"
"s_waitcnt lgkmcnt(4)\n"
"v_mfma_f32_32x32x16_bf16 v[128:143], a[200:203], v[100:103], v[128:143]\n"
"ds_read_b64_tr_b16 a[216:217], v9 offset:38912\n"
"ds_read_b64_tr_b16 a[218:219], v9 offset:39424\n"
"s_mov_b32 s74, 12288\n"
"v_mfma_f32_32x32x16_bf16 v[144:159], a[204:207], v[100:103], v[144:159]\n"
"buffer_load_dwordx4 v4, s[52:55], s69 offen offset:3072 lds\n"
"ds_read_b64_tr_b16 a[220:221], v10 offset:38912\n"
"ds_read_b64_tr_b16 a[222:223], v10 offset:39424\n"
"v_cvt_pk_bf16_f32 v176, v176, v177\n"
"s_waitcnt lgkmcnt(4)\n"
"v_mfma_f32_32x32x16_bf16 v[128:143], a[208:211], v[112:115], v[128:143]\n"
"ds_read_b64_tr_b16 a[192:193], v9 offset:49152\n"
"ds_read_b64_tr_b16 a[194:195], v9 offset:49664\n"
"v_cvt_pk_bf16_f32 v177, v178, v179\n"
"v_cvt_pk_bf16_f32 v178, v180, v181\n"
"v_mfma_f32_32x32x16_bf16 v[144:159], a[212:215], v[112:115], v[144:159]\n"
"s_add_u32 m0, s68, 65536\n"
"s_mov_b32 s69, 768\n"
"buffer_load_dwordx4 v1, s[52:55], s69 offen offset:0 lds\n"
"ds_read_b64_tr_b16 a[196:197], v10 offset:49152\n"
"ds_read_b64_tr_b16 a[198:199], v10 offset:49664\n"
"v_cvt_pk_bf16_f32 v179, v182, v183\n"
"s_waitcnt lgkmcnt(4)\n"
"v_mfma_f32_32x32x16_bf16 v[128:143], a[216:219], v[116:119], v[128:143]\n"
"ds_read_b64_tr_b16 a[200:201], v9 offset:51200\n"
"ds_read_b64_tr_b16 a[202:203], v9 offset:51712\n"
"buffer_store_dwordx4 v[176:179], v23, s[64:67], s74 offen offset:0 sc1\n"
"v_mfma_f32_32x32x16_bf16 v[144:159], a[220:223], v[116:119], v[144:159]\n"
"buffer_load_dwordx4 v2, s[52:55], s69 offen offset:1024 lds\n"
"ds_read_b64_tr_b16 a[204:205], v10 offset:51200\n"
"ds_read_b64_tr_b16 a[206:207], v10 offset:51712\n"
"v_cvt_pk_bf16_f32 v184, v184, v185\n"
"s_waitcnt lgkmcnt(4)\n"
"v_mfma_f32_32x32x16_bf16 v[128:143], a[192:195], v[120:123], v[128:143]\n"
"ds_read_b64_tr_b16 a[208:209], v9 offset:53248\n"
"ds_read_b64_tr_b16 a[210:211], v9 offset:53760\n"
"v_cvt_pk_bf16_f32 v185, v186, v187\n"
"v_cvt_pk_bf16_f32 v186, v188, v189\n"
"v_mfma_f32_32x32x16_bf16 v[144:159], a[196:199], v[120:123], v[144:159]\n"
"buffer_load_dwordx4 v3, s[52:55], s69 offen offset:2048 lds\n"
"ds_read_b64_tr_b16 a[212:213], v10 offset:53248\n"
"ds_read_b64_tr_b16 a[214:215], v10 offset:53760\n"
"v_cvt_pk_bf16_f32 v187, v190, v191\n"
"s_waitcnt lgkmcnt(4)\n"
"v_mfma_f32_32x32x16_bf16 v[128:143], a[200:203], v[124:127], v[128:143]\n"
"ds_read_b64_tr_b16 a[216:217], v9 offset:55296\n"
"ds_read_b64_tr_b16 a[218:219], v9 offset:55808\n"
"buffer_store_dwordx4 v[184:187], v23, s[64:67], s74 offen offset:1024 sc1\n"
"v_mfma_f32_32x32x16_bf16 v[144:159], a[204:207], v[124:127], v[144:159]\n"
"buffer_load_dwordx4 v4, s[52:55], s69 offen offset:3072 lds\n"
"ds_read_b64_tr_b16 a[220:221], v10 offset:55296\n"
"ds_read_b64_tr_b16 a[222:223], v10 offset:55808\n"
"s_waitcnt lgkmcnt(4)\n"
"v_mfma_f32_32x32x16_bf16 v[128:143], a[208:211], v[208:211], v[128:143]\n"
"s_waitcnt vmcnt(14)\n"
"s_barrier\n"
"v_xor_b32 v9, 0x10000, v9\n"
"ds_read_b64_tr_b16 a[192:193], v9 offset:0\n"
"ds_read_b64_tr_b16 a[194:195], v9 offset:512\n"
"v_mfma_f32_32x32x16_bf16 v[144:159], a[212:215], v[208:211], v[144:159]\n"
"s_add_u32 m0, s68, 81920\n"
"s_mov_b32 s69, 58112\n"
"buffer_load_dwordx4 v1, s[52:55], s69 offen offset:0 lds\n"
"v_xor_b32 v10, 0x10000, v10\n"
"ds_read_b64_tr_b16 a[196:197], v10 offset:0\n"
"ds_read_b64_tr_b16 a[198:199], v10 offset:512\n"
"s_waitcnt lgkmcnt(4)\n"
"v_mfma_f32_32x32x16_bf16 v[128:143], a[216:219], v[212:215], v[128:143]\n"
"ds_read_b64_tr_b16 a[200:201], v9 offset:2048\n"
"ds_read_b64_tr_b16 a[202:203], v9 offset:2560\n"
"v_mfma_f32_32x32x16_bf16 v[144:159], a[220:223], v[212:215], v[144:159]\n"
"buffer_load_dwordx4 v2, s[52:55], s69 offen offset:1024 lds\n"
"ds_read_b64_tr_b16 a[204:205], v10 offset:2048\n"
"ds_read_b64_tr_b16 a[206:207], v10 offset:2560\n"
"s_waitcnt lgkmcnt(4)\n"
"v_mfma_f32_32x32x16_bf16 v[160:175], a[192:195], v[32:35], 0\n"
"ds_read_b64_tr_b16 a[208:209], v9 offset:4096\n"
"ds_read_b64_tr_b16 a[210:211], v9 offset:4608\n"
"v_mfma_f32_32x32x16_bf16 v[176:191], a[196:199], v[32:35], 0\n"
"buffer_load_dwordx4 v3, s[52:55], s69 offen offset:2048 lds\n"
"ds_read_b64_tr_b16 a[212:213], v10 offset:4096\n"
"ds_read_b64_tr_b16 a[214:215], v10 offset:4608\n"
"v_cvt_pk_bf16_f32 v128, v128, v129\n"
"s_waitcnt lgkmcnt(4)\n"
"v_mfma_f32_32x32x16_bf16 v[160:175], a[200:203], v[36:39], v[160:175]\n"
"ds_read_b64_tr_b16 a[216:217], v9 offset:6144\n"
"ds_read_b64_tr_b16 a[218:219], v9 offset:6656\n"
"v_cvt_pk_bf16_f32 v129, v130, v131\n"
"v_mfma_f32_32x32x16_bf16 v[176:191], a[204:207], v[36:39], v[176:191]\n"
"buffer_load_dwordx4 v4, s[52:55], s69 offen offset:3072 lds\n"
"ds_read_b64_tr_b16 a[220:221], v10 offset:6144\n"
"ds_read_b64_tr_b16 a[222:223], v10 offset:6656\n"
"v_cvt_pk_bf16_f32 v130, v132, v133\n"
"s_waitcnt lgkmcnt(4)\n"
"v_mfma_f32_32x32x16_bf16 v[160:175], a[208:211], v[48:51], v[160:175]\n"
"ds_read_b64_tr_b16 a[192:193], v9 offset:16384\n"
"ds_read_b64_tr_b16 a[194:195], v9 offset:16896\n"
"v_cvt_pk_bf16_f32 v131, v134, v135\n"
"v_mfma_f32_32x32x16_bf16 v[176:191], a[212:215], v[48:51], v[176:191]\n"
"s_add_u32 m0, s68, 98304\n"
"s_mov_b32 s69, 115456\n"
"buffer_load_dwordx4 v1, s[52:55], s69 offen offset:0 lds\n"
"ds_read_b64_tr_b16 a[196:197], v10 offset:16384\n"
"ds_read_b64_tr_b16 a[198:199], v10 offset:16896\n"
"ds_write_b128 v17, v[128:131] offset:0\n"
"v_cvt_pk_bf16_f32 v136, v136, v137\n"
"s_waitcnt lgkmcnt(5)\n"
"v_mfma_f32_32x32x16_bf16 v[160:175], a[216:219], v[52:55], v[160:175]\n"
"ds_read_b64_tr_b16 a[200:201], v9 offset:18432\n"
"ds_read_b64_tr_b16 a[202:203], v9 offset:18944\n"
"v_cvt_pk_bf16_f32 v137, v138, v139\n"
"v_mfma_f32_32x32x16_bf16 v[176:191], a[220:223], v[52:55], v[176:191]\n"
"buffer_load_dwordx4 v2, s[52:55], s69 offen offset:1024 lds\n"
"ds_read_b64_tr_b16 a[204:205], v10 offset:18432\n"
"ds_read_b64_tr_b16 a[206:207], v10 offset:18944\n"
"v_cvt_pk_bf16_f32 v138, v140, v141\n"
"s_waitcnt lgkmcnt(5)\n"
"v_mfma_f32_32x32x16_bf16 v[160:175], a[192:195], v[64:67], v[160:175]\n"
"ds_read_b64_tr_b16 a[208:209], v9 offset:20480\n"
"ds_read_b64_tr_b16 a[210:211], v9 offset:20992\n"
"v_cvt_pk_bf16_f32 v139, v142, v143\n"
"v_mfma_f32_32x32x16_bf16 v[176:191], a[196:199], v[64:67], v[176:191]\n"
"buffer_load_dwordx4 v3, s[52:55], s69 offen offset:2048 lds\n"
"ds_read_b64_tr_b16 a[212:213], v10 offset:20480\n"
"ds_read_b64_tr_b16 a[214:215], v10 offset:20992\n"
"ds_write_b128 v17, v[136:139] offset:1024\n"
"s_waitcnt lgkmcnt(5)\n"
"v_mfma_f32_32x32x16_bf16 v[160:175], a[200:203], v[68:71], v[160:175]\n"
"ds_read_b64_tr_b16 a[216:217], v9 offset:22528\n"
"ds_read_b64_tr_b16 a[218:219], v9 offset:23040\n"
"v_mfma_f32_32x32x16_bf16 v[176:191], a[204:207], v[68:71], v[176:191]\n"
"buffer_load_dwordx4 v4, s[52:55], s69 offen offset:3072 lds\n"
"ds_read_b64_tr_b16 a[220:221], v10 offset:22528\n"
"ds_read_b64_tr_b16 a[222:223], v10 offset:23040\n"
"s_waitcnt lgkmcnt(5)\n"
"v_mfma_f32_32x32x16_bf16 v[160:175], a[208:211], v[80:83], v[160:175]\n"
"s_waitcnt vmcnt(14) lgkmcnt(4)\n"
"s_barrier\n"
"ds_read_b64_tr_b16 a[192:193], v9 offset:32768\n"
"ds_read_b64_tr_b16 a[194:195], v9 offset:33280\n"
"ds_read_b128 a[48:51], v24 offset:0\n"
"ds_read_b128 a[52:55], v24 offset:1024\n"
"s_waitcnt vmcnt(9)\n"
"s_mov_b32 s75, 4096\n"
"buffer_store_dword v11, v12, s[60:63], s75 offen sc1\n"
"v_mfma_f32_32x32x16_bf16 v[176:191], a[212:215], v[80:83], v[176:191]\n"
"s_add_u32 m0, s68, 114688\n"
"s_mov_b32 s69, 172800\n"
"buffer_load_dwordx4 v1, s[52:55], s69 offen offset:0 lds\n"
"ds_read_b64_tr_b16 a[196:197], v10 offset:32768\n"
"ds_read_b64_tr_b16 a[198:199], v10 offset:33280\n"
"s_waitcnt lgkmcnt(6)\n"
"v_mfma_f32_32x32x16_bf16 v[160:175], a[216:219], v[84:87], v[160:175]\n"
"ds_read_b64_tr_b16 a[200:201], v9 offset:34816\n"
"ds_read_b64_tr_b16 a[202:203], v9 offset:35328\n"
"v_mfma_f32_32x32x16_bf16 v[176:191], a[220:223], v[84:87], v[176:191]\n"
"buffer_load_dwordx4 v2, s[52:55], s69 offen offset:1024 lds\n"
"ds_read_b64_tr_b16 a[204:205], v10 offset:34816\n"
"ds_read_b64_tr_b16 a[206:207], v10 offset:35328\n"
"s_waitcnt lgkmcnt(7)\n"
"v_mfma_f32_32x32x16_bf16 v[144:159], a[240:243], a[48:51], v[144:159]\n"
"s_waitcnt lgkmcnt(6)\n"
"v_mfma_f32_32x32x16_bf16 v[144:159], a[244:247], a[52:55], v[144:159]\n"
"s_waitcnt lgkmcnt(4)\n"
"v_mfma_f32_32x32x16_bf16 v[160:175], a[192:195], v[96:99], v[160:175]\n"
"ds_read_b64_tr_b16 a[208:209], v9 offset:36864\n"
"ds_read_b64_tr_b16 a[210:211], v9 offset:37376\n"
"v_mfma_f32_32x32x16_bf16 v[176:191], a[196:199], v[96:99], v[176:191]\n"
"buffer_load_dwordx4 v3, s[52:55], s69 offen offset:2048 lds\n"
"ds_read_b64_tr_b16 a[212:213], v10 offset:36864\n"
"ds_read_b64_tr_b16 a[214:215], v10 offset:37376\n"
"s_waitcnt lgkmcnt(4)\n"
"v_mfma_f32_32x32x16_bf16 v[160:175], a[200:203], v[100:103], v[160:175]\n"
"ds_read_b64_tr_b16 a[216:217], v9 offset:38912\n"
"ds_read_b64_tr_b16 a[218:219], v9 offset:39424\n"
"s_mov_b32 s74, 16384\n"
"v_mfma_f32_32x32x16_bf16 v[176:191], a[204:207], v[100:103], v[176:191]\n"
"buffer_load_dwordx4 v4, s[52:55], s69 offen offset:3072 lds\n"
"ds_read_b64_tr_b16 a[220:221], v10 offset:38912\n"
"ds_read_b64_tr_b16 a[222:223], v10 offset:39424\n"
"v_cvt_pk_bf16_f32 v144, v144, v145\n"
"s_waitcnt lgkmcnt(4)\n"
"v_mfma_f32_32x32x16_bf16 v[160:175], a[208:211], v[112:115], v[160:175]\n"
"v_lshrrev_b32 v28, 5, v0\n"
"s_lshl_b32 s75, s71, 3\n"
"v_add_u32 v28, s75, v28\n"
"v_and_b32 v29, 31, v0\n"
"v_add_u32 v26, 0, v28\n"
"v_and_b32 v27, 3, v26\n"
"v_lshlrev_b32 v27, 2, v27\n"
"v_xor_b32 v27, v27, v29\n"
"v_lshlrev_b32 v27, 4, v27\n"
"v_mul_u32_u24 v26, 3584, v26\n"
"v_add_u32 v27, v26, v27\n"
"v_subrev_u32 v1, 0, v27\n"
"v_add_u32 v26, 2, v28\n"
"v_and_b32 v27, 3, v26\n"
"v_lshlrev_b32 v27, 2, v27\n"
"v_xor_b32 v27, v27, v29\n"
"v_lshlrev_b32 v27, 4, v27\n"
"v_mul_u32_u24 v26, 3584, v26\n"
"ds_read_b64_tr_b16 a[192:193], v9 offset:49152\n"
"ds_read_b64_tr_b16 a[194:195], v9 offset:49664\n"
"v_cvt_pk_bf16_f32 v145, v146, v147\n"
"v_cvt_pk_bf16_f32 v146, v148, v149\n"
"v_mfma_f32_32x32x16_bf16 v[176:191], a[212:215], v[112:115], v[176:191]\n"
"v_add_u32 v27, v26, v27\n"
"v_subrev_u32 v2, 1024, v27\n"
"v_add_u32 v26, 4, v28\n"
"v_and_b32 v27, 3, v26\n"
"v_lshlrev_b32 v27, 2, v27\n"
"v_xor_b32 v27, v27, v29\n"
"v_lshlrev_b32 v27, 4, v27\n"
"v_mul_u32_u24 v26, 3584, v26\n"
"v_add_u32 v27, v26, v27\n"
"v_subrev_u32 v3, 2048, v27\n"
"v_add_u32 v26, 6, v28\n"
"v_and_b32 v27, 3, v26\n"
"v_lshlrev_b32 v27, 2, v27\n"
"v_xor_b32 v27, v27, v29\n"
"v_lshlrev_b32 v27, 4, v27\n"
"v_mul_u32_u24 v26, 3584, v26\n"
"v_add_u32 v27, v26, v27\n"
"v_subrev_u32 v4, 3072, v27\n"
"ds_read_b64_tr_b16 a[196:197], v10 offset:49152\n"
"ds_read_b64_tr_b16 a[198:199], v10 offset:49664\n"
"v_cvt_pk_bf16_f32 v147, v150, v151\n"
"s_waitcnt lgkmcnt(4)\n"
"v_mfma_f32_32x32x16_bf16 v[160:175], a[216:219], v[116:119], v[160:175]\n"
"ds_read_b64_tr_b16 a[200:201], v9 offset:51200\n"
"ds_read_b64_tr_b16 a[202:203], v9 offset:51712\n"
"buffer_store_dwordx4 v[144:147], v23, s[64:67], s74 offen offset:0 sc1\n"
"v_mfma_f32_32x32x16_bf16 v[176:191], a[220:223], v[116:119], v[176:191]\n"
"ds_read_b64_tr_b16 a[204:205], v10 offset:51200\n"
"ds_read_b64_tr_b16 a[206:207], v10 offset:51712\n"
"v_cvt_pk_bf16_f32 v152, v152, v153\n"
"s_waitcnt lgkmcnt(4)\n"
"v_mfma_f32_32x32x16_bf16 v[160:175], a[192:195], v[120:123], v[160:175]\n"
"ds_read_b64_tr_b16 a[208:209], v9 offset:53248\n"
"ds_read_b64_tr_b16 a[210:211], v9 offset:53760\n"
"v_cvt_pk_bf16_f32 v153, v154, v155\n"
"v_cvt_pk_bf16_f32 v154, v156, v157\n"
"v_mfma_f32_32x32x16_bf16 v[176:191], a[196:199], v[120:123], v[176:191]\n"
"ds_read_b64_tr_b16 a[212:213], v10 offset:53248\n"
"ds_read_b64_tr_b16 a[214:215], v10 offset:53760\n"
"v_cvt_pk_bf16_f32 v155, v158, v159\n"
"s_waitcnt lgkmcnt(4)\n"
"v_mfma_f32_32x32x16_bf16 v[160:175], a[200:203], v[124:127], v[160:175]\n"
"ds_read_b64_tr_b16 a[216:217], v9 offset:55296\n"
"ds_read_b64_tr_b16 a[218:219], v9 offset:55808\n"
"buffer_store_dwordx4 v[152:155], v23, s[64:67], s74 offen offset:1024 sc1\n"
"v_mfma_f32_32x32x16_bf16 v[176:191], a[204:207], v[124:127], v[176:191]\n"
"ds_read_b64_tr_b16 a[220:221], v10 offset:55296\n"
"ds_read_b64_tr_b16 a[222:223], v10 offset:55808\n"
"s_waitcnt lgkmcnt(4)\n"
"v_mfma_f32_32x32x16_bf16 v[160:175], a[208:211], v[208:211], v[160:175]\n"
"s_waitcnt vmcnt(11)\n"
"s_barrier\n"
"v_xor_b32 v9, 0x10000, v9\n"
"ds_read_b64_tr_b16 a[192:193], v9 offset:0\n"
"ds_read_b64_tr_b16 a[194:195], v9 offset:512\n"
"v_mfma_f32_32x32x16_bf16 v[176:191], a[212:215], v[208:211], v[176:191]\n"
"v_xor_b32 v10, 0x10000, v10\n"
"ds_read_b64_tr_b16 a[196:197], v10 offset:0\n"
"ds_read_b64_tr_b16 a[198:199], v10 offset:512\n"
"s_waitcnt lgkmcnt(4)\n"
"v_mfma_f32_32x32x16_bf16 v[160:175], a[216:219], v[212:215], v[160:175]\n"
"ds_read_b64_tr_b16 a[200:201], v9 offset:2048\n"
"ds_read_b64_tr_b16 a[202:203], v9 offset:2560\n"
"s_mov_b32 s75, 4096\n"
"buffer_load_dword v31, v13, s[60:63], s75 offen sc1\n"
"v_mfma_f32_32x32x16_bf16 v[176:191], a[220:223], v[212:215], v[176:191]\n"
"ds_read_b64_tr_b16 a[204:205], v10 offset:2048\n"
"ds_read_b64_tr_b16 a[206:207], v10 offset:2560\n"
"s_waitcnt lgkmcnt(4)\n"
"v_mfma_f32_32x32x16_bf16 v[128:143], a[192:195], v[32:35], 0\n"
"ds_read_b64_tr_b16 a[208:209], v9 offset:4096\n"
"ds_read_b64_tr_b16 a[210:211], v9 offset:4608\n"
"v_mfma_f32_32x32x16_bf16 v[144:159], a[196:199], v[32:35], 0\n"
"ds_read_b64_tr_b16 a[212:213], v10 offset:4096\n"
"ds_read_b64_tr_b16 a[214:215], v10 offset:4608\n"
"v_cvt_pk_bf16_f32 v160, v160, v161\n"
"s_waitcnt lgkmcnt(4)\n"
"v_mfma_f32_32x32x16_bf16 v[128:143], a[200:203], v[36:39], v[128:143]\n"
"ds_read_b64_tr_b16 a[216:217], v9 offset:6144\n"
"ds_read_b64_tr_b16 a[218:219], v9 offset:6656\n"
"v_cvt_pk_bf16_f32 v161, v162, v163\n"
"v_mfma_f32_32x32x16_bf16 v[144:159], a[204:207], v[36:39], v[144:159]\n"
"ds_read_b64_tr_b16 a[220:221], v10 offset:6144\n"
"ds_read_b64_tr_b16 a[222:223], v10 offset:6656\n"
"v_cvt_pk_bf16_f32 v162, v164, v165\n"
"s_waitcnt lgkmcnt(4)\n"
"v_mfma_f32_32x32x16_bf16 v[128:143], a[208:211], v[48:51], v[128:143]\n"
"ds_read_b64_tr_b16 a[192:193], v9 offset:16384\n"
"ds_read_b64_tr_b16 a[194:195], v9 offset:16896\n"
"v_cvt_pk_bf16_f32 v163, v166, v167\n"
"v_mfma_f32_32x32x16_bf16 v[144:159], a[212:215], v[48:51], v[144:159]\n"
"ds_read_b64_tr_b16 a[196:197], v10 offset:16384\n"
"ds_read_b64_tr_b16 a[198:199], v10 offset:16896\n"
"ds_write_b128 v17, v[160:163] offset:2048\n"
"v_cvt_pk_bf16_f32 v168, v168, v169\n"
"s_waitcnt lgkmcnt(5)\n"
"v_mfma_f32_32x32x16_bf16 v[128:143], a[216:219], v[52:55], v[128:143]\n"
"ds_read_b64_tr_b16 a[200:201], v9 offset:18432\n"
"ds_read_b64_tr_b16 a[202:203], v9 offset:18944\n"
"v_cvt_pk_bf16_f32 v169, v170, v171\n"
"v_mfma_f32_32x32x16_bf16 v[144:159], a[220:223], v[52:55], v[144:159]\n"
"ds_read_b64_tr_b16 a[204:205], v10 offset:18432\n"
"ds_read_b64_tr_b16 a[206:207], v10 offset:18944\n"
"v_cvt_pk_bf16_f32 v170, v172, v173\n"
"s_waitcnt lgkmcnt(5)\n"
"v_mfma_f32_32x32x16_bf16 v[128:143], a[192:195], v[64:67], v[128:143]\n"
"ds_read_b64_tr_b16 a[208:209], v9 offset:20480\n"
"ds_read_b64_tr_b16 a[210:211], v9 offset:20992\n"
"v_cvt_pk_bf16_f32 v171, v174, v175\n"
"v_mfma_f32_32x32x16_bf16 v[144:159], a[196:199], v[64:67], v[144:159]\n"
"ds_read_b64_tr_b16 a[212:213], v10 offset:20480\n"
"ds_read_b64_tr_b16 a[214:215], v10 offset:20992\n"
"ds_write_b128 v17, v[168:171] offset:3072\n"
"s_waitcnt lgkmcnt(5)\n"
"v_mfma_f32_32x32x16_bf16 v[128:143], a[200:203], v[68:71], v[128:143]\n"
"ds_read_b64_tr_b16 a[216:217], v9 offset:22528\n"
"ds_read_b64_tr_b16 a[218:219], v9 offset:23040\n"
"v_mfma_f32_32x32x16_bf16 v[144:159], a[204:207], v[68:71], v[144:159]\n"
"ds_read_b64_tr_b16 a[220:221], v10 offset:22528\n"
"ds_read_b64_tr_b16 a[222:223], v10 offset:23040\n"
"s_waitcnt lgkmcnt(5)\n"
"v_mfma_f32_32x32x16_bf16 v[128:143], a[208:211], v[80:83], v[128:143]\n"
"s_waitcnt vmcnt(3) lgkmcnt(4)\n"
"s_barrier\n"
"ds_read_b64_tr_b16 a[192:193], v9 offset:32768\n"
"ds_read_b64_tr_b16 a[194:195], v9 offset:33280\n"
"ds_read_b128 a[48:51], v24 offset:2048\n"
"ds_read_b128 a[52:55], v24 offset:3072\n"
"v_mfma_f32_32x32x16_bf16 v[144:159], a[212:215], v[80:83], v[144:159]\n"
"ds_read_b64_tr_b16 a[196:197], v10 offset:32768\n"
"ds_read_b64_tr_b16 a[198:199], v10 offset:33280\n"
"s_waitcnt lgkmcnt(6)\n"
"v_mfma_f32_32x32x16_bf16 v[128:143], a[216:219], v[84:87], v[128:143]\n"
"ds_read_b64_tr_b16 a[200:201], v9 offset:34816\n"
"ds_read_b64_tr_b16 a[202:203], v9 offset:35328\n"
"v_mfma_f32_32x32x16_bf16 v[144:159], a[220:223], v[84:87], v[144:159]\n"
"ds_read_b64_tr_b16 a[204:205], v10 offset:34816\n"
"ds_read_b64_tr_b16 a[206:207], v10 offset:35328\n"
"s_waitcnt lgkmcnt(7)\n"
"v_mfma_f32_32x32x16_bf16 v[176:191], a[240:243], a[48:51], v[176:191]\n"
"s_waitcnt lgkmcnt(6)\n"
"v_mfma_f32_32x32x16_bf16 v[176:191], a[244:247], a[52:55], v[176:191]\n"
"s_waitcnt lgkmcnt(4)\n"
"v_mfma_f32_32x32x16_bf16 v[128:143], a[192:195], v[96:99], v[128:143]\n"
"ds_read_b64_tr_b16 a[208:209], v9 offset:36864\n"
"ds_read_b64_tr_b16 a[210:211], v9 offset:37376\n"
"s_waitcnt vmcnt(0)\n"
"v_cmp_ne_u32 vcc, s72, v31\n"
"s_cmp_eq_u64 vcc, 0\n"
"s_cbranch_scc1 .Lok2_%=\n"
"s_movk_i32 s84, 0x7000\n"
".Lsp2_%=:\n"
"s_mov_b32 s75, 4096\n"
"buffer_load_dword v31, v13, s[60:63], s75 offen sc1\n"
"s_waitcnt vmcnt(0)\n"
"v_cmp_ne_u32 vcc, s72, v31\n"
"s_cmp_eq_u64 vcc, 0\n"
"s_cbranch_scc1 .Lok2_%=\n"
"s_sub_u32 s84, s84, 1\n"
"s_cmp_lg_u32 s84, 0\n"
"s_cbranch_scc1 .Lsp2_%=\n"
".Lok2_%=:\n"
"v_mfma_f32_32x32x16_bf16 v[144:159], a[196:199], v[96:99], v[144:159]\n"
"ds_read_b64_tr_b16 a[212:213], v10 offset:36864\n"
"ds_read_b64_tr_b16 a[214:215], v10 offset:37376\n"
"s_waitcnt lgkmcnt(4)\n"
"v_mfma_f32_32x32x16_bf16 v[128:143], a[200:203], v[100:103], v[128:143]\n"
"ds_read_b64_tr_b16 a[216:217], v9 offset:38912\n"
"ds_read_b64_tr_b16 a[218:219], v9 offset:39424\n"
"s_mov_b32 s74, 20480\n"
"s_mov_b32 s70, 0\n"
"buffer_load_dwordx4 a[64:67], v16, s[76:79], s70 offen sc1\n"
"s_mov_b32 s70, 7340032\n"
"buffer_load_dwordx4 a[68:71], v16, s[76:79], s70 offen sc1\n"
"v_mfma_f32_32x32x16_bf16 v[144:159], a[204:207], v[100:103], v[144:159]\n"
"ds_read_b64_tr_b16 a[220:221], v10 offset:38912\n"
"ds_read_b64_tr_b16 a[222:223], v10 offset:39424\n"
"v_cvt_pk_bf16_f32 v176, v176, v177\n"
"s_waitcnt lgkmcnt(4)\n"
"v_mfma_f32_32x32x16_bf16 v[128:143], a[208:211], v[112:115], v[128:143]\n"
"ds_read_b64_tr_b16 a[192:193], v9 offset:49152\n"
"ds_read_b64_tr_b16 a[194:195], v9 offset:49664\n"
"v_cvt_pk_bf16_f32 v177, v178, v179\n"
"v_cvt_pk_bf16_f32 v178, v180, v181\n"
"v_mfma_f32_32x32x16_bf16 v[144:159], a[212:215], v[112:115], v[144:159]\n"
"ds_read_b64_tr_b16 a[196:197], v10 offset:49152\n"
"ds_read_b64_tr_b16 a[198:199], v10 offset:49664\n"
"v_cvt_pk_bf16_f32 v179, v182, v183\n"
"s_waitcnt lgkmcnt(4)\n"
"v_mfma_f32_32x32x16_bf16 v[128:143], a[216:219], v[116:119], v[128:143]\n"
"ds_read_b64_tr_b16 a[200:201], v9 offset:51200\n"
"ds_read_b64_tr_b16 a[202:203], v9 offset:51712\n"
"buffer_store_dwordx4 v[176:179], v23, s[64:67], s74 offen offset:0 sc1\n"
"s_mov_b32 s70, 4096\n"
"buffer_load_dwordx4 a[72:75], v16, s[76:79], s70 offen sc1\n"
"s_mov_b32 s70, 7344128\n"
"buffer_load_dwordx4 a[76:79], v16, s[76:79], s70 offen sc1\n"
"v_mfma_f32_32x32x16_bf16 v[144:159], a[220:223], v[116:119], v[144:159]\n"
"ds_read_b64_tr_b16 a[204:205], v10 offset:51200\n"
"ds_read_b64_tr_b16 a[206:207], v10 offset:51712\n"
"v_cvt_pk_bf16_f32 v184, v184, v185\n"
"s_waitcnt lgkmcnt(4)\n"
"v_mfma_f32_32x32x16_bf16 v[128:143], a[192:195], v[120:123], v[128:143]\n"
"ds_read_b64_tr_b16 a[208:209], v9 offset:53248\n"
"ds_read_b64_tr_b16 a[210:211], v9 offset:53760\n"
"v_cvt_pk_bf16_f32 v185, v186, v187\n"
"v_cvt_pk_bf16_f32 v186, v188, v189\n"
"v_mfma_f32_32x32x16_bf16 v[144:159], a[196:199], v[120:123], v[144:159]\n"
"ds_read_b64_tr_b16 a[212:213], v10 offset:53248\n"
"ds_read_b64_tr_b16 a[214:215], v10 offset:53760\n"
"v_cvt_pk_bf16_f32 v187, v190, v191\n"
"s_waitcnt lgkmcnt(4)\n"
"v_mfma_f32_32x32x16_bf16 v[128:143], a[200:203], v[124:127], v[128:143]\n"
"ds_read_b64_tr_b16 a[216:217], v9 offset:55296\n"
"ds_read_b64_tr_b16 a[218:219], v9 offset:55808\n"
"buffer_store_dwordx4 v[184:187], v23, s[64:67], s74 offen offset:1024 sc1\n"
"s_mov_b32 s70, 8192\n"
"buffer_load_dwordx4 a[80:83], v16, s[76:79], s70 offen sc1\n"
"s_mov_b32 s70, 7348224\n"
"buffer_load_dwordx4 a[84:87], v16, s[76:79], s70 offen sc1\n"
"v_mfma_f32_32x32x16_bf16 v[144:159], a[204:207], v[124:127], v[144:159]\n"
"ds_read_b64_tr_b16 a[220:221], v10 offset:55296\n"
"ds_read_b64_tr_b16 a[222:223], v10 offset:55808\n"
"s_waitcnt lgkmcnt(4)\n"
"v_mfma_f32_32x32x16_bf16 v[128:143], a[208:211], v[208:211], v[128:143]\n"
"s_barrier\n"
"v_mfma_f32_32x32x16_bf16 v[144:159], a[212:215], v[208:211], v[144:159]\n"
"s_waitcnt lgkmcnt(0)\n"
"v_mfma_f32_32x32x16_bf16 v[128:143], a[216:219], v[212:215], v[128:143]\n"
"s_mov_b32 s70, 12288\n"
"buffer_load_dwordx4 a[88:91], v16, s[76:79], s70 offen sc1\n"
"s_mov_b32 s70, 7352320\n"
"buffer_load_dwordx4 a[92:95], v16, s[76:79], s70 offen sc1\n"
"v_mfma_f32_32x32x16_bf16 v[144:159], a[220:223], v[212:215], v[144:159]\n"
"s_nop 4\n"
"v_cvt_pk_bf16_f32 v128, v128, v129\n"
"v_cvt_pk_bf16_f32 v129, v130, v131\n"
"v_cvt_pk_bf16_f32 v130, v132, v133\n"
"v_cvt_pk_bf16_f32 v131, v134, v135\n"
"ds_write_b128 v17, v[128:131] offset:0\n"
"v_cvt_pk_bf16_f32 v136, v136, v137\n"
"v_cvt_pk_bf16_f32 v137, v138, v139\n"
"v_cvt_pk_bf16_f32 v138, v140, v141\n"
"v_cvt_pk_bf16_f32 v139, v142, v143\n"
"ds_write_b128 v17, v[136:139] offset:1024\n"
"s_waitcnt lgkmcnt(0)\n"
"s_barrier\n"
"ds_read_b128 a[48:51], v24 offset:0\n"
"ds_read_b128 a[52:55], v24 offset:1024\n"
"s_waitcnt lgkmcnt(1)\n"
"v_mfma_f32_32x32x16_bf16 v[144:159], a[240:243], a[48:51], v[144:159]\n"
"s_waitcnt lgkmcnt(0)\n"
"v_mfma_f32_32x32x16_bf16 v[144:159], a[244:247], a[52:55], v[144:159]\n"
"s_mov_b32 s74, 24576\n"
"s_nop 10\n"
"v_cvt_pk_bf16_f32 v144, v144, v145\n"
"v_cvt_pk_bf16_f32 v145, v146, v147\n"
"v_cvt_pk_bf16_f32 v146, v148, v149\n"
"v_cvt_pk_bf16_f32 v147, v150, v151\n"
"buffer_store_dwordx4 v[144:147], v23, s[64:67], s74 offen offset:0 sc1\n"
"v_cvt_pk_bf16_f32 v152, v152, v153\n"
"v_cvt_pk_bf16_f32 v153, v154, v155\n"
"v_cvt_pk_bf16_f32 v154, v156, v157\n"
"v_cvt_pk_bf16_f32 v155, v158, v159\n"
"buffer_store_dwordx4 v[152:155], v23, s[64:67], s74 offen offset:1024 sc1\n"
"s_mov_b32 s70, 16384\n"
"buffer_load_dwordx4 v[160:163], v16, s[76:79], s70 offen sc1\n"
"s_mov_b32 s70, 7356416\n"
"buffer_load_dwordx4 v[164:167], v16, s[76:79], s70 offen sc1\n"
"s_mov_b32 s70, 20480\n"
"buffer_load_dwordx4 v[168:171], v16, s[76:79], s70 offen sc1\n"
"s_mov_b32 s70, 7360512\n"
"buffer_load_dwordx4 v[172:175], v16, s[76:79], s70 offen sc1\n"
"s_mov_b32 s70, 24576\n"
"buffer_load_dwordx4 v[176:179], v16, s[76:79], s70 offen sc1\n"
"s_mov_b32 s70, 7364608\n"
"buffer_load_dwordx4 v[180:183], v16, s[76:79], s70 offen sc1\n"
"s_add_u32 m0, s68, 0\n"
"s_mov_b32 s69, 0\n"
"buffer_load_dwordx4 v1, s[40:43], s69 offen offset:0 lds\n"
"buffer_load_dwordx4 v2, s[40:43], s69 offen offset:1024 lds\n"
"buffer_load_dwordx4 v3, s[40:43], s69 offen offset:2048 lds\n"
"buffer_load_dwordx4 v4, s[40:43], s69 offen offset:3072 lds\n"
"s_add_u32 m0, s68, 16384\n"
"s_mov_b32 s69, 114688\n"
"buffer_load_dwordx4 v1, s[40:43], s69 offen offset:0 lds\n"
"buffer_load_dwordx4 v2, s[40:43], s69 offen offset:1024 lds\n"
"buffer_load_dwordx4 v3, s[40:43], s69 offen offset:2048 lds\n"
"buffer_load_dwordx4 v4, s[40:43], s69 offen offset:3072 lds\n"
"s_add_u32 m0, s68, 32768\n"
"s_mov_b32 s69, 229376\n"
"buffer_load_dwordx4 v1, s[40:43], s69 offen offset:0 lds\n"
"buffer_load_dwordx4 v2, s[40:43], s69 offen offset:1024 lds\n"
"buffer_load_dwordx4 v3, s[40:43], s69 offen offset:2048 lds\n"
"buffer_load_dwordx4 v4, s[40:43], s69 offen offset:3072 lds\n"
"s_add_u32 m0, s68, 49152\n"
"s_mov_b32 s69, 344064\n"
"buffer_load_dwordx4 v1, s[40:43], s69 offen offset:0 lds\n"
"buffer_load_dwordx4 v2, s[40:43], s69 offen offset:1024 lds\n"
"buffer_load_dwordx4 v3, s[40:43], s69 offen offset:2048 lds\n"
"buffer_load_dwordx4 v4, s[40:43], s69 offen offset:3072 lds\n"
"ds_read_b128 v[32:35], v22 offset:0\n"
"ds_read_b128 v[36:39], v22 offset:512\n"
"ds_read_b128 v[40:43], v22 offset:1024\n"
"ds_read_b128 v[44:47], v22 offset:1536\n"
"v_lshlrev_b32 v96, 16, v248\n"
"v_lshlrev_b32 v98, 16, v249\n"
"v_lshlrev_b32 v100, 16, v250\n"
"v_lshlrev_b32 v102, 16, v251\n"
"v_lshlrev_b32 v104, 16, v252\n"
"v_lshlrev_b32 v106, 16, v253\n"
"v_lshlrev_b32 v108, 16, v254\n"
"v_lshlrev_b32 v110, 16, v255\n"
"v_and_b32 v97, 0xffff0000, v248\n"
"v_and_b32 v99, 0xffff0000, v249\n"
"v_and_b32 v101, 0xffff0000, v250\n"
"v_and_b32 v103, 0xffff0000, v251\n"
"v_and_b32 v105, 0xffff0000, v252\n"
"v_and_b32 v107, 0xffff0000, v253\n"
"v_and_b32 v109, 0xffff0000, v254\n"
"v_and_b32 v111, 0xffff0000, v255\n"
"ds_read_b128 v[48:51], v22 offset:1792\n"
"ds_read_b128 v[52:55], v22 offset:2304\n"
"ds_read_b128 v[56:59], v22 offset:2816\n"
"ds_read_b128 v[60:63], v22 offset:3328\n"
"ds_read_b128 v[64:67], v22 offset:3584\n"
"ds_read_b128 v[68:71], v22 offset:4096\n"
"ds_read_b128 v[72:75], v22 offset:4608\n"
"ds_read_b128 v[76:79], v22 offset:5120\n"
"s_lshl_b32 s75, s71, 3\n"
"s_add_u32 s75, s75, 144384\n"
"v_and_b32 v26, 7, v0\n"
"v_lshlrev_b32 v26, 5, v26\n"
"v_add_u32 v27, s75, v26\n"
"v_add_u32 v26, 144384, v26\n"
"s_waitcnt vmcnt(33)\n"
"v_mfma_f32_16x16x32_bf16 v[224:227], a[180:183], a[64:67], 0\n"
"s_waitcnt vmcnt(32)\n"
"v_mfma_f32_16x16x32_bf16 v[224:227], a[180:183], a[68:71], v[224:227]\n"
"s_waitcnt vmcnt(30)\n"
"v_mfma_f32_16x16x32_bf16 v[224:227], a[184:187], a[72:75], v[224:227]\n"
"s_waitcnt vmcnt(29)\n"
"v_mfma_f32_16x16x32_bf16 v[224:227], a[184:187], a[76:79], v[224:227]\n"
"s_waitcnt vmcnt(27)\n"
"v_mfma_f32_16x16x32_bf16 v[228:231], a[180:183], a[80:83], 0\n"
"s_waitcnt vmcnt(26)\n"
"v_mfma_f32_16x16x32_bf16 v[228:231], a[180:183], a[84:87], v[228:231]\n"
"s_waitcnt vmcnt(25)\n"
"v_mfma_f32_16x16x32_bf16 v[228:231], a[184:187], a[88:91], v[228:231]\n"
"s_waitcnt vmcnt(24)\n"
"v_mfma_f32_16x16x32_bf16 v[228:231], a[184:187], a[92:95], v[228:231]\n"
"s_waitcnt vmcnt(21)\n"
"v_max_u32 v25, v160, v161\n"
"v_max3_u32 v25, v162, v163, v25\n"
"s_waitcnt vmcnt(20)\n"
"v_max3_u32 v25, v164, v165, v25\n"
"v_max3_u32 v25, v166, v167, v25\n"
"s_waitcnt vmcnt(19)\n"
"v_max3_u32 v25, v168, v169, v25\n"
"v_max3_u32 v25, v170, v171, v25\n"
"s_waitcnt vmcnt(18)\n"
"v_max3_u32 v25, v172, v173, v25\n"
"v_max3_u32 v25, v174, v175, v25\n"
"s_waitcnt vmcnt(17)\n"
"v_max3_u32 v25, v176, v177, v25\n"
"v_max3_u32 v25, v178, v179, v25\n"
"s_waitcnt vmcnt(16)\n"
"v_max3_u32 v25, v180, v181, v25\n"
"v_max3_u32 v25, v182, v183, v25\n"
"v_cmp_eq_u32 vcc, -1, v25\n"
"s_cmp_eq_u64 vcc, 0\n"
"s_cbranch_scc1 .Lok3_%=\n"
"s_movk_i32 s84, 0x7000\n"
".Lsp3_%=:\n"
"s_mov_b32 s70, 16384\n"
"buffer_load_dwordx4 v[160:163], v16, s[76:79], s70 offen sc1\n"
"s_mov_b32 s70, 7356416\n"
"buffer_load_dwordx4 v[164:167], v16, s[76:79], s70 offen sc1\n"
"s_mov_b32 s70, 20480\n"
"buffer_load_dwordx4 v[168:171], v16, s[76:79], s70 offen sc1\n"
"s_mov_b32 s70, 7360512\n"
"buffer_load_dwordx4 v[172:175], v16, s[76:79], s70 offen sc1\n"
"s_mov_b32 s70, 24576\n"
"buffer_load_dwordx4 v[176:179], v16, s[76:79], s70 offen sc1\n"
"s_mov_b32 s70, 7364608\n"
"buffer_load_dwordx4 v[180:183], v16, s[76:79], s70 offen sc1\n"
"s_waitcnt vmcnt(0)\n"
"v_max_u32 v25, v160, v161\n"
"v_max3_u32 v25, v162, v163, v25\n"
"v_max3_u32 v25, v164, v165, v25\n"
"v_max3_u32 v25, v166, v167, v25\n"
"v_max3_u32 v25, v168, v169, v25\n"
"v_max3_u32 v25, v170, v171, v25\n"
"v_max3_u32 v25, v172, v173, v25\n"
"v_max3_u32 v25, v174, v175, v25\n"
"v_max3_u32 v25, v176, v177, v25\n"
"v_max3_u32 v25, v178, v179, v25\n"
"v_max3_u32 v25, v180, v181, v25\n"
"v_max3_u32 v25, v182, v183, v25\n"
"v_cmp_eq_u32 vcc, -1, v25\n"
"s_cmp_eq_u64 vcc, 0\n"
"s_cbranch_scc1 .Lok3_%=\n"
"s_sub_u32 s84, s84, 1\n"
"s_cmp_lg_u32 s84, 0\n"
"s_cbranch_scc1 .Lsp3_%=\n"
".Lok3_%=:\n"
"v_mfma_f32_16x16x32_bf16 v[232:235], a[180:183], v[160:163], 0\n"
"v_mfma_f32_16x16x32_bf16 v[232:235], a[180:183], v[164:167], v[232:235]\n"
"v_mfma_f32_16x16x32_bf16 v[232:235], a[184:187], v[168:171], v[232:235]\n"
"v_mfma_f32_16x16x32_bf16 v[232:235], a[184:187], v[172:175], v[232:235]\n"
"v_mfma_f32_16x16x32_bf16 v[236:239], a[180:183], v[176:179], 0\n"
"v_mfma_f32_16x16x32_bf16 v[236:239], a[180:183], v[180:183], v[236:239]\n"
"v_cmp_gt_u32 vcc, 32, v0\n"
"s_waitcnt lgkmcnt(11)\n"
"v_fma_f32 v224, v224, v32, v96\n"
"v_fma_f32 v225, v225, v33, v97\n"
"v_fma_f32 v226, v226, v34, v98\n"
"v_fma_f32 v227, v227, v35, v99\n"
"s_waitcnt lgkmcnt(10)\n"
"v_fma_f32 v228, v228, v36, v100\n"
"v_fma_f32 v229, v229, v37, v101\n"
"v_fma_f32 v230, v230, v38, v102\n"
"v_fma_f32 v231, v231, v39, v103\n"
"s_waitcnt lgkmcnt(9)\n"
"v_fma_f32 v232, v232, v40, v104\n"
"v_fma_f32 v233, v233, v41, v105\n"
"v_fma_f32 v234, v234, v42, v106\n"
"v_fma_f32 v235, v235, v43, v107\n"
"s_waitcnt lgkmcnt(8)\n"
"v_fma_f32 v236, v236, v44, v108\n"
"v_fma_f32 v237, v237, v45, v109\n"
"v_fma_f32 v238, v238, v46, v110\n"
"v_fma_f32 v239, v239, v47, v111\n"
"v_cndmask_b32 v236, 0, v236, vcc\n"
"v_cndmask_b32 v237, 0, v237, vcc\n"
"v_cndmask_b32 v238, 0, v238, vcc\n"
"v_cndmask_b32 v239, 0, v239, vcc\n"
"v_cvt_pk_bf16_f32 v96, v224, v225\n"
"v_cvt_pk_bf16_f32 v97, v226, v227\n"
"v_cvt_pk_bf16_f32 v98, v228, v229\n"
"v_cvt_pk_bf16_f32 v99, v230, v231\n"
"v_cvt_pk_bf16_f32 v100, v232, v233\n"
"v_cvt_pk_bf16_f32 v101, v234, v235\n"
"v_cvt_pk_bf16_f32 v102, v236, v237\n"
"v_cvt_pk_bf16_f32 v103, v238, v239\n"
"v_lshlrev_b32 v224, 16, v96\n"
"v_lshlrev_b32 v226, 16, v97\n"
"v_lshlrev_b32 v228, 16, v98\n"
"v_lshlrev_b32 v230, 16, v99\n"
"v_lshlrev_b32 v232, 16, v100\n"
"v_lshlrev_b32 v234, 16, v101\n"
"v_lshlrev_b32 v236, 16, v102\n"
"v_lshlrev_b32 v238, 16, v103\n"
"v_and_b32 v225, 0xffff0000, v96\n"
"v_and_b32 v227, 0xffff0000, v97\n"
"v_and_b32 v229, 0xffff0000, v98\n"
"v_and_b32 v231, 0xffff0000, v99\n"
"v_and_b32 v233, 0xffff0000, v100\n"
"v_and_b32 v235, 0xffff0000, v101\n"
"v_and_b32 v237, 0xffff0000, v102\n"
"v_and_b32 v239, 0xffff0000, v103\n"
"v_add_f32 v28, v224, v225\n"
"v_mul_f32 v29, v224, v224\n"
"v_fmac_f32 v29, v225, v225\n"
"v_add_f32 v28, v28, v226\n"
"v_fmac_f32 v29, v226, v226\n"
"v_add_f32 v28, v28, v227\n"
"v_fmac_f32 v29, v227, v227\n"
"v_add_f32 v28, v28, v228\n"
"v_fmac_f32 v29, v228, v228\n"
"v_add_f32 v28, v28, v229\n"
"v_fmac_f32 v29, v229, v229\n"
"v_add_f32 v28, v28, v230\n"
"v_fmac_f32 v29, v230, v230\n"
"v_add_f32 v28, v28, v231\n"
"v_fmac_f32 v29, v231, v231\n"
"v_add_f32 v28, v28, v232\n"
"v_fmac_f32 v29, v232, v232\n"
"v_add_f32 v28, v28, v233\n"
"v_fmac_f32 v29, v233, v233\n"
"v_add_f32 v28, v28, v234\n"
"v_fmac_f32 v29, v234, v234\n"
"v_add_f32 v28, v28, v235\n"
"v_fmac_f32 v29, v235, v235\n"
"v_add_f32 v28, v28, v236\n"
"v_fmac_f32 v29, v236, v236\n"
"v_add_f32 v28, v28, v237\n"
"v_fmac_f32 v29, v237, v237\n"
"v_add_f32 v28, v28, v238\n"
"v_fmac_f32 v29, v238, v238\n"
"v_add_f32 v28, v28, v239\n"
"v_fmac_f32 v29, v239, v239\n"
"s_nop 0\n"
"v_add_f32 v28, v28, v28 row_ror:8 row_mask:0xf bank_mask:0xf\n"
"v_add_f32 v29, v29, v29 row_ror:8 row_mask:0xf bank_mask:0xf\n"
"v_mov_b32 v30, v28\n"
"v_mov_b32 v25, v29\n"
"s_nop 0\n"
"v_permlane16_swap_b32 v28, v30\n"
"v_permlane16_swap_b32 v29, v25\n"
"v_add_f32 v28, v28, v30\n"
"v_add_f32 v29, v29, v25\n"
"v_mov_b32 v30, v28\n"
"v_mov_b32 v25, v29\n"
"s_nop 0\n"
"v_permlane32_swap_b32 v28, v30\n"
"v_permlane32_swap_b32 v29, v25\n"
"v_add_f32 v28, v28, v30\n"
"v_add_f32 v29, v29, v25\n"
"ds_write_b64 v27, v[28:29]\n"
"s_waitcnt vmcnt(12) lgkmcnt(0)\n"
"s_barrier\n"
"ds_read_b128 v[96:99], v26\n"
"ds_read_b128 v[100:103], v26 offset:16\n"
"s_waitcnt lgkmcnt(1)\n"
"v_add_f32 v28, v96, v98\n"
"v_add_f32 v29, v97, v99\n"
"s_waitcnt lgkmcnt(0)\n"
"v_add_f32 v28, v28, v100\n"
"v_add_f32 v29, v29, v101\n"
"v_add_f32 v28, v28, v102\n"
"v_add_f32 v29, v29, v103\n"
"v_mul_f32 v28, 0x3b124925, v28\n"
"v_mul_f32 v30, v28, v28\n"
"v_mul_f32 v29, 0x3b124925, v29\n"
"v_sub_f32 v29, v29, v30\n"
"v_max_f32 v29, 0, v29\n"
"v_add_f32 v29, 0x3727c5ac, v29\n"
"v_rsq_f32 v29, v29\n"
"s_nop 0\n"
"v_mul_f32 v28, v28, v29\n"
"v_sub_f32 v28, 0, v28\n"
"v_fma_f32 v96, v224, v29, v28\n"
"v_fma_f32 v97, v225, v29, v28\n"
"v_fma_f32 v98, v226, v29, v28\n"
"v_fma_f32 v99, v227, v29, v28\n"
"v_fma_f32 v100, v228, v29, v28\n"
"v_fma_f32 v101, v229, v29, v28\n"
"v_fma_f32 v102, v230, v29, v28\n"
"v_fma_f32 v103, v231, v29, v28\n"
"v_fma_f32 v104, v232, v29, v28\n"
"v_fma_f32 v105, v233, v29, v28\n"
"v_fma_f32 v106, v234, v29, v28\n"
"v_fma_f32 v107, v235, v29, v28\n"
"v_fma_f32 v108, v236, v29, v28\n"
"v_fma_f32 v109, v237, v29, v28\n"
"v_fma_f32 v110, v238, v29, v28\n"
"v_fma_f32 v111, v239, v29, v28\n"
"v_fma_f32 v96, v96, v64, v48\n"
"v_fma_f32 v97, v97, v65, v49\n"
"v_fma_f32 v98, v98, v66, v50\n"
"v_fma_f32 v99, v99, v67, v51\n"
"v_fma_f32 v100, v100, v68, v52\n"
"v_fma_f32 v101, v101, v69, v53\n"
"v_fma_f32 v102, v102, v70, v54\n"
"v_fma_f32 v103, v103, v71, v55\n"
"v_fma_f32 v104, v104, v72, v56\n"
"v_fma_f32 v105, v105, v73, v57\n"
"v_fma_f32 v106, v106, v74, v58\n"
"v_fma_f32 v107, v107, v75, v59\n"
"v_fma_f32 v108, v108, v76, v60\n"
"v_fma_f32 v109, v109, v77, v61\n"
"v_fma_f32 v110, v110, v78, v62\n"
"v_fma_f32 v111, v111, v79, v63\n"
"v_cvt_pk_bf16_f32 v96, v96, v97\n"
"v_cvt_pk_bf16_f32 v97, v98, v99\n"
"v_cvt_pk_bf16_f32 v98, v100, v101\n"
"v_cvt_pk_bf16_f32 v99, v102, v103\n"
"v_cvt_pk_bf16_f32 v100, v104, v105\n"
"v_cvt_pk_bf16_f32 v101, v106, v107\n"
"v_cvt_pk_bf16_f32 v102, v108, v109\n"
"v_cvt_pk_bf16_f32 v103, v110, v111\n"
"s_movk_i32 s74, 0\n"
"buffer_store_dwordx2 v[96:97], v21, s[48:51], s74 offen sc1\n"
"s_movk_i32 s74, 8192\n"
"buffer_store_dwordx2 v[98:99], v21, s[48:51], s74 offen sc1\n"
"s_movk_i32 s74, 16384\n"
"buffer_store_dwordx2 v[100:101], v21, s[48:51], s74 offen sc1\n"
"s_movk_i32 s74, 24576\n"
"buffer_store_dwordx2 v[102:103], v21, s[48:51], s74 offen sc1\n"
"s_lshl_b32 s85, %[T], 1\n"
"s_add_u32 s85, s85, %[rr]\n"
"s_mul_i32 s85, s85, 28672\n"
"s_add_u32 s85, s85, 131072\n"
"s_mov_b64 s[56:57], %[ws]\n"
"s_add_u32 s56, s56, s85\n"
"s_addc_u32 s57, s57, 0\n"
"s_mov_b32 s58, %[nx]\n"
"s_mov_b32 s59, 0x00020000\n"
"s_cmp_eq_u32 %[nx], 0\n"
"s_cselect_b32 s66, 0, s66\n"
"s_sub_u32 s78, s78, 16515072\n"
"s_cmp_lg_u32 %[nx], 0\n"
"s_cbranch_scc1 .Lffn_%=\n"
"s_lshl_b32 s85, %[T], 3\n"
"s_add_u32 s85, s85, %[g]\n"
"s_mul_i32 s85, s85, 7168\n"
"s_waitcnt vmcnt(0) lgkmcnt(0)\n"
"s_mov_b64 s[48:49], %[out]\n"
"s_add_u32 s48, s48, s85\n"
"s_addc_u32 s49, s49, 0\n"
"s_movk_i32 s50, 7168\n"
"s_mov_b32 s75, 69632\n"
"buffer_store_dword v11, v12, s[60:63], s75 offen sc1\n"
"s_sleep 127\n"
"s_mov_b32 s75, 69632\n"
"buffer_load_dword v31, v13, s[60:63], s75 offen sc1\n"
"s_waitcnt vmcnt(0)\n"
"s_branch .Lr2_%=\n"
".Lffn_%=:\n"
"s_movk_i32 s70, 0\n"
"buffer_load_dwordx4 v[32:35], v23, s[56:59], s70 offen offset:0 sc1\n"
"buffer_load_dwordx4 v[36:39], v23, s[56:59], s70 offen offset:1024 sc1\n"
"buffer_load_dwordx4 v[40:43], v23, s[56:59], s70 offen offset:2048 sc1\n"
"buffer_load_dwordx4 v[44:47], v23, s[56:59], s70 offen offset:3072 sc1\n"
"s_movk_i32 s70, 4096\n"
"buffer_load_dwordx4 v[48:51], v23, s[56:59], s70 offen offset:0 sc1\n"
"buffer_load_dwordx4 v[52:55], v23, s[56:59], s70 offen offset:1024 sc1\n"
"buffer_load_dwordx4 v[56:59], v23, s[56:59], s70 offen offset:2048 sc1\n"
"buffer_load_dwordx4 v[60:63], v23, s[56:59], s70 offen offset:3072 sc1\n"
"s_movk_i32 s70, 8192\n"
"buffer_load_dwordx4 v[64:67], v23, s[56:59], s70 offen offset:0 sc1\n"
"buffer_load_dwordx4 v[68:71], v23, s[56:59], s70 offen offset:1024 sc1\n"
"buffer_load_dwordx4 v[72:75], v23, s[56:59], s70 offen offset:2048 sc1\n"
"buffer_load_dwordx4 v[76:79], v23, s[56:59], s70 offen offset:3072 sc1\n"
"s_add_u32 m0, s68, 65536\n"
"s_mov_b32 s69, 458752\n"
"buffer_load_dwordx4 v1, s[40:43], s69 offen offset:0 lds\n"
"buffer_load_dwordx4 v2, s[40:43], s69 offen offset:1024 lds\n"
"buffer_load_dwordx4 v3, s[40:43], s69 offen offset:2048 lds\n"
"buffer_load_dwordx4 v4, s[40:43], s69 offen offset:3072 lds\n"
"s_add_u32 m0, s68, 81920\n"
"s_mov_b32 s69, 573440\n"
"buffer_load_dwordx4 v1, s[40:43], s69 offen offset:0 lds\n"
"buffer_load_dwordx4 v2, s[40:43], s69 offen offset:1024 lds\n"
"buffer_load_dwordx4 v3, s[40:43], s69 offen offset:2048 lds\n"
"buffer_load_dwordx4 v4, s[40:43], s69 offen offset:3072 lds\n"
"s_add_u32 m0, s68, 98304\n"
"s_mov_b32 s69, 688128\n"
"buffer_load_dwordx4 v1, s[40:43], s69 offen offset:0 lds\n"
"buffer_load_dwordx4 v2, s[40:43], s69 offen offset:1024 lds\n"
"buffer_load_dwordx4 v3, s[40:43], s69 offen offset:2048 lds\n"
"buffer_load_dwordx4 v4, s[40:43], s69 offen offset:3072 lds\n"
"ds_read_b64_tr_b16 a[192:193], v5 offset:0\n"
"ds_read_b64_tr_b16 a[194:195], v5 offset:2048\n"
"ds_read_b64_tr_b16 a[196:197], v6 offset:0\n"
"ds_read_b64_tr_b16 a[198:199], v6 offset:2048\n"
"ds_read_b64_tr_b16 a[200:201], v7 offset:0\n"
"ds_read_b64_tr_b16 a[202:203], v7 offset:2048\n"
"ds_read_b64_tr_b16 a[204:205], v8 offset:0\n"
"ds_read_b64_tr_b16 a[206:207], v8 offset:2048\n"
"s_waitcnt vmcnt(23)\n"
"v_max_u32 v25, v32, v33\n"
"v_max3_u32 v25, v34, v35, v25\n"
"s_waitcnt vmcnt(22)\n"
"v_max3_u32 v25, v36, v37, v25\n"
"v_max3_u32 v25, v38, v39, v25\n"
"s_waitcnt vmcnt(21)\n"
"v_max3_u32 v25, v40, v41, v25\n"
"v_max3_u32 v25, v42, v43, v25\n"
"s_waitcnt vmcnt(20)\n"
"v_max3_u32 v25, v44, v45, v25\n"
"v_max3_u32 v25, v46, v47, v25\n"
"s_waitcnt vmcnt(19)\n"
"v_max3_u32 v25, v48, v49, v25\n"
"v_max3_u32 v25, v50, v51, v25\n"
"s_waitcnt vmcnt(18)\n"
"v_max3_u32 v25, v52, v53, v25\n"
"v_max3_u32 v25, v54, v55, v25\n"
"s_waitcnt vmcnt(17)\n"
"v_max3_u32 v25, v56, v57, v25\n"
"v_max3_u32 v25, v58, v59, v25\n"
"s_waitcnt vmcnt(16)\n"
"v_max3_u32 v25, v60, v61, v25\n"
"v_max3_u32 v25, v62, v63, v25\n"
"s_waitcnt vmcnt(15)\n"
"v_max3_u32 v25, v64, v65, v25\n"
"v_max3_u32 v25, v66, v67, v25\n"
"s_waitcnt vmcnt(14)\n"
"v_max3_u32 v25, v68, v69, v25\n"
"v_max3_u32 v25, v70, v71, v25\n"
"s_waitcnt vmcnt(13)\n"
"v_max3_u32 v25, v72, v73, v25\n"
"v_max3_u32 v25, v74, v75, v25\n"
"s_waitcnt vmcnt(12)\n"
"v_max3_u32 v25, v76, v77, v25\n"
"v_max3_u32 v25, v78, v79, v25\n"
"v_cmp_eq_u32 vcc, -1, v25\n"
"s_cmp_eq_u64 vcc, 0\n"
"s_cbranch_scc1 .Lok4_%=\n"
"s_movk_i32 s84, 0x7000\n"
".Lsp4_%=:\n"
"s_movk_i32 s70, 0\n"
"buffer_load_dwordx4 v[32:35], v23, s[56:59], s70 offen offset:0 sc1\n"
"buffer_load_dwordx4 v[36:39], v23, s[56:59], s70 offen offset:1024 sc1\n"
"buffer_load_dwordx4 v[40:43], v23, s[56:59], s70 offen offset:2048 sc1\n"
"buffer_load_dwordx4 v[44:47], v23, s[56:59], s70 offen offset:3072 sc1\n"
"s_movk_i32 s70, 4096\n"
"buffer_load_dwordx4 v[48:51], v23, s[56:59], s70 offen offset:0 sc1\n"
"buffer_load_dwordx4 v[52:55], v23, s[56:59], s70 offen offset:1024 sc1\n"
"buffer_load_dwordx4 v[56:59], v23, s[56:59], s70 offen offset:2048 sc1\n"
"buffer_load_dwordx4 v[60:63], v23, s[56:59], s70 offen offset:3072 sc1\n"
"s_movk_i32 s70, 8192\n"
"buffer_load_dwordx4 v[64:67], v23, s[56:59], s70 offen offset:0 sc1\n"
"buffer_load_dwordx4 v[68:71], v23, s[56:59], s70 offen offset:1024 sc1\n"
"buffer_load_dwordx4 v[72:75], v23, s[56:59], s70 offen offset:2048 sc1\n"
"buffer_load_dwordx4 v[76:79], v23, s[56:59], s70 offen offset:3072 sc1\n"
"s_waitcnt vmcnt(0)\n"
"v_max_u32 v25, v32, v33\n"
"v_max3_u32 v25, v34, v35, v25\n"
"v_max3_u32 v25, v36, v37, v25\n"
"v_max3_u32 v25, v38, v39, v25\n"
"v_max3_u32 v25, v40, v41, v25\n"
"v_max3_u32 v25, v42, v43, v25\n"
"v_max3_u32 v25, v44, v45, v25\n"
"v_max3_u32 v25, v46, v47, v25\n"
"v_max3_u32 v25, v48, v49, v25\n"
"v_max3_u32 v25, v50, v51, v25\n"
"v_max3_u32 v25, v52, v53, v25\n"
"v_max3_u32 v25, v54, v55, v25\n"
"v_max3_u32 v25, v56, v57, v25\n"
"v_max3_u32 v25, v58, v59, v25\n"
"v_max3_u32 v25, v60, v61, v25\n"
"v_max3_u32 v25, v62, v63, v25\n"
"v_max3_u32 v25, v64, v65, v25\n"
"v_max3_u32 v25, v66, v67, v25\n"
"v_max3_u32 v25, v68, v69, v25\n"
"v_max3_u32 v25, v70, v71, v25\n"
"v_max3_u32 v25, v72, v73, v25\n"
"v_max3_u32 v25, v74, v75, v25\n"
"v_max3_u32 v25, v76, v77, v25\n"
"v_max3_u32 v25, v78, v79, v25\n"
"v_cmp_eq_u32 vcc, -1, v25\n"
"s_cmp_eq_u64 vcc, 0\n"
"s_cbranch_scc1 .Lok4_%=\n"
"s_sub_u32 s84, s84, 1\n"
"s_cmp_lg_u32 s84, 0\n"
"s_cbranch_scc1 .Lsp4_%=\n"
".Lok4_%=:\n"
"s_movk_i32 s70, 12288\n"
"buffer_load_dwordx4 v[80:83], v23, s[56:59], s70 offen offset:0 sc1\n"
"buffer_load_dwordx4 v[84:87], v23, s[56:59], s70 offen offset:1024 sc1\n"
"buffer_load_dwordx4 v[88:91], v23, s[56:59], s70 offen offset:2048 sc1\n"
"buffer_load_dwordx4 v[92:95], v23, s[56:59], s70 offen offset:3072 sc1\n"
"s_movk_i32 s70, 16384\n"
"buffer_load_dwordx4 v[96:99], v23, s[56:59], s70 offen offset:0 sc1\n"
"buffer_load_dwordx4 v[100:103], v23, s[56:59], s70 offen offset:1024 sc1\n"
"buffer_load_dwordx4 v[104:107], v23, s[56:59], s70 offen offset:2048 sc1\n"
"buffer_load_dwordx4 v[108:111], v23, s[56:59], s70 offen offset:3072 sc1\n"
"s_movk_i32 s70, 20480\n"
"buffer_load_dwordx4 v[112:115], v23, s[56:59], s70 offen offset:0 sc1\n"
"buffer_load_dwordx4 v[116:119], v23, s[56:59], s70 offen offset:1024 sc1\n"
"buffer_load_dwordx4 v[120:123], v23, s[56:59], s70 offen offset:2048 sc1\n"
"buffer_load_dwordx4 v[124:127], v23, s[56:59], s70 offen offset:3072 sc1\n"
"s_movk_i32 s70, 24576\n"
"buffer_load_dwordx4 v[192:195], v23, s[56:59], s70 offen offset:0 sc1\n"
"buffer_load_dwordx4 v[196:199], v23, s[56:59], s70 offen offset:1024 sc1\n"
"buffer_load_dwordx4 v[200:203], v23, s[56:59], s70 offen offset:2048 sc1\n"
"buffer_load_dwordx4 v[204:207], v23, s[56:59], s70 offen offset:3072 sc1\n"
"s_waitcnt lgkmcnt(4)\n"
"v_mfma_f32_32x32x16_bf16 v[128:143], a[192:195], v[32:35], 0\n"
"ds_read_b64_tr_b16 a[208:209], v5 offset:8192\n"
"ds_read_b64_tr_b16 a[210:211], v5 offset:10240\n"
"v_mfma_f32_32x32x16_bf16 v[144:159], a[196:199], v[32:35], 0\n"
"ds_read_b64_tr_b16 a[212:213], v6 offset:8192\n"
"ds_read_b64_tr_b16 a[214:215], v6 offset:10240\n"
"s_waitcnt lgkmcnt(4)\n"
"v_mfma_f32_32x32x16_bf16 v[160:175], a[200:203], v[32:35], 0\n"
"ds_read_b64_tr_b16 a[216:217], v7 offset:8192\n"
"ds_read_b64_tr_b16 a[218:219], v7 offset:10240\n"
"v_mfma_f32_32x32x16_bf16 v[176:191], a[204:207], v[32:35], 0\n"
"ds_read_b64_tr_b16 a[220:221], v8 offset:8192\n"
"ds_read_b64_tr_b16 a[222:223], v8 offset:10240\n"
"s_waitcnt lgkmcnt(4)\n"
"v_mfma_f32_32x32x16_bf16 v[128:143], a[208:211], v[36:39], v[128:143]\n"
"s_barrier\n"
"ds_read_b64_tr_b16 a[192:193], v5 offset:16384\n"
"ds_read_b64_tr_b16 a[194:195], v5 offset:18432\n"
"v_mfma_f32_32x32x16_bf16 v[144:159], a[212:215], v[36:39], v[144:159]\n"
"s_add_u32 m0, s68, 114688\n"
"s_mov_b32 s69, 802816\n"
"buffer_load_dwordx4 v1, s[40:43], s69 offen offset:0 lds\n"
"ds_read_b64_tr_b16 a[196:197], v6 offset:16384\n"
"ds_read_b64_tr_b16 a[198:199], v6 offset:18432\n"
"s_waitcnt lgkmcnt(4)\n"
"v_mfma_f32_32x32x16_bf16 v[160:175], a[216:219], v[36:39], v[160:175]\n"
"ds_read_b64_tr_b16 a[200:201], v7 offset:16384\n"
"ds_read_b64_tr_b16 a[202:203], v7 offset:18432\n"
"v_mfma_f32_32x32x16_bf16 v[176:191], a[220:223], v[36:39], v[176:191]\n"
"buffer_load_dwordx4 v2, s[40:43], s69 offen offset:1024 lds\n"
"ds_read_b64_tr_b16 a[204:205], v8 offset:16384\n"
"ds_read_b64_tr_b16 a[206:207], v8 offset:18432\n"
"s_waitcnt lgkmcnt(4)\n"
"v_mfma_f32_32x32x16_bf16 v[128:143], a[192:195], v[40:43], v[128:143]\n"
"ds_read_b64_tr_b16 a[208:209], v5 offset:24576\n"
"ds_read_b64_tr_b16 a[210:211], v5 offset:26624\n"
"v_mfma_f32_32x32x16_bf16 v[144:159], a[196:199], v[40:43], v[144:159]\n"
"buffer_load_dwordx4 v3, s[40:43], s69 offen offset:2048 lds\n"
"ds_read_b64_tr_b16 a[212:213], v6 offset:24576\n"
"ds_read_b64_tr_b16 a[214:215], v6 offset:26624\n"
"s_waitcnt lgkmcnt(4)\n"
"v_mfma_f32_32x32x16_bf16 v[160:175], a[200:203], v[40:43], v[160:175]\n"
"ds_read_b64_tr_b16 a[216:217], v7 offset:24576\n"
"ds_read_b64_tr_b16 a[218:219], v7 offset:26624\n"
"v_mfma_f32_32x32x16_bf16 v[176:191], a[204:207], v[40:43], v[176:191]\n"
"buffer_load_dwordx4 v4, s[40:43], s69 offen offset:3072 lds\n"
"ds_read_b64_tr_b16 a[220:221], v8 offset:24576\n"
"ds_read_b64_tr_b16 a[222:223], v8 offset:26624\n"
"s_waitcnt lgkmcnt(4)\n"
"v_mfma_f32_32x32x16_bf16 v[128:143], a[208:211], v[44:47], v[128:143]\n"
"s_barrier\n"
"ds_read_b64_tr_b16 a[192:193], v5 offset:32768\n"
"ds_read_b64_tr_b16 a[194:195], v5 offset:34816\n"
"v_mfma_f32_32x32x16_bf16 v[144:159], a[212:215], v[44:47], v[144:159]\n"
"s_add_u32 m0, s68, 0\n"
"s_mov_b32 s69, 917504\n"
"buffer_load_dwordx4 v1, s[40:43], s69 offen offset:0 lds\n"
"ds_read_b64_tr_b16 a[196:197], v6 offset:32768\n"
"ds_read_b64_tr_b16 a[198:199], v6 offset:34816\n"
"s_waitcnt lgkmcnt(4)\n"
"v_mfma_f32_32x32x16_bf16 v[160:175], a[216:219], v[44:47], v[160:175]\n"
"ds_read_b64_tr_b16 a[200:201], v7 offset:32768\n"
"ds_read_b64_tr_b16 a[202:203], v7 offset:34816\n"
"v_mfma_f32_32x32x16_bf16 v[176:191], a[220:223], v[44:47], v[176:191]\n"
"buffer_load_dwordx4 v2, s[40:43], s69 offen offset:1024 lds\n"
"ds_read_b64_tr_b16 a[204:205], v8 offset:32768\n"
"ds_read_b64_tr_b16 a[206:207], v8 offset:34816\n"
"s_waitcnt lgkmcnt(4)\n"
"v_mfma_f32_32x32x16_bf16 v[128:143], a[192:195], v[48:51], v[128:143]\n"
"ds_read_b64_tr_b16 a[208:209], v5 offset:40960\n"
"ds_read_b64_tr_b16 a[210:211], v5 offset:43008\n"
"v_mfma_f32_32x32x16_bf16 v[144:159], a[196:199], v[48:51], v[144:159]\n"
"buffer_load_dwordx4 v3, s[40:43], s69 offen offset:2048 lds\n"
"ds_read_b64_tr_b16 a[212:213], v6 offset:40960\n"
"ds_read_b64_tr_b16 a[214:215], v6 offset:43008\n"
"s_waitcnt lgkmcnt(4)\n"
"v_mfma_f32_32x32x16_bf16 v[160:175], a[200:203], v[48:51], v[160:175]\n"
"ds_read_b64_tr_b16 a[216:217], v7 offset:40960\n"
"ds_read_b64_tr_b16 a[218:219], v7 offset:43008\n"
"v_mfma_f32_32x32x16_bf16 v[176:191], a[204:207], v[48:51], v[176:191]\n"
"buffer_load_dwordx4 v4, s[40:43], s69 offen offset:3072 lds\n"
"ds_read_b64_tr_b16 a[220:221], v8 offset:40960\n"
"ds_read_b64_tr_b16 a[222:223], v8 offset:43008\n"
"s_waitcnt lgkmcnt(4)\n"
"v_mfma_f32_32x32x16_bf16 v[128:143], a[208:211], v[52:55], v[128:143]\n"
"s_barrier\n"
"ds_read_b64_tr_b16 a[192:193], v5 offset:49152\n"
"ds_read_b64_tr_b16 a[194:195], v5 offset:51200\n"
"v_mfma_f32_32x32x16_bf16 v[144:159], a[212:215], v[52:55], v[144:159]\n"
"s_add_u32 m0, s68, 16384\n"
"s_mov_b32 s69, 1032192\n"
"buffer_load_dwordx4 v1, s[40:43], s69 offen offset:0 lds\n"
"ds_read_b64_tr_b16 a[196:197], v6 offset:49152\n"
"ds_read_b64_tr_b16 a[198:199], v6 offset:51200\n"
"s_waitcnt lgkmcnt(4)\n"
"v_mfma_f32_32x32x16_bf16 v[160:175], a[216:219], v[52:55], v[160:175]\n"
"ds_read_b64_tr_b16 a[200:201], v7 offset:49152\n"
"ds_read_b64_tr_b16 a[202:203], v7 offset:51200\n"
"v_mfma_f32_32x32x16_bf16 v[176:191], a[220:223], v[52:55], v[176:191]\n"
"buffer_load_dwordx4 v2, s[40:43], s69 offen offset:1024 lds\n"
"ds_read_b64_tr_b16 a[204:205], v8 offset:49152\n"
"ds_read_b64_tr_b16 a[206:207], v8 offset:51200\n"
"s_waitcnt lgkmcnt(4)\n"
"v_mfma_f32_32x32x16_bf16 v[128:143], a[192:195], v[56:59], v[128:143]\n"
"ds_read_b64_tr_b16 a[208:209], v5 offset:57344\n"
"ds_read_b64_tr_b16 a[210:211], v5 offset:59392\n"
"v_mfma_f32_32x32x16_bf16 v[144:159], a[196:199], v[56:59], v[144:159]\n"
"buffer_load_dwordx4 v3, s[40:43], s69 offen offset:2048 lds\n"
"ds_read_b64_tr_b16 a[212:213], v6 offset:57344\n"
"ds_read_b64_tr_b16 a[214:215], v6 offset:59392\n"
"s_waitcnt lgkmcnt(4)\n"
"v_mfma_f32_32x32x16_bf16 v[160:175], a[200:203], v[56:59], v[160:175]\n"
"ds_read_b64_tr_b16 a[216:217], v7 offset:57344\n"
"ds_read_b64_tr_b16 a[218:219], v7 offset:59392\n"
"v_mfma_f32_32x32x16_bf16 v[176:191], a[204:207], v[56:59], v[176:191]\n"
"buffer_load_dwordx4 v4, s[40:43], s69 offen offset:3072 lds\n"
"ds_read_b64_tr_b16 a[220:221], v8 offset:57344\n"
"ds_read_b64_tr_b16 a[222:223], v8 offset:59392\n"
"s_waitcnt lgkmcnt(4)\n"
"v_mfma_f32_32x32x16_bf16 v[128:143], a[208:211], v[60:63], v[128:143]\n"
"s_waitcnt vmcnt(36)\n"
"s_barrier\n"
"v_xor_b32 v5, 0x10000, v5\n"
"ds_read_b64_tr_b16 a[192:193], v5 offset:0\n"
"ds_read_b64_tr_b16 a[194:195], v5 offset:2048\n"
"v_mfma_f32_32x32x16_bf16 v[144:159], a[212:215], v[60:63], v[144:159]\n"
"s_add_u32 m0, s68, 32768\n"
"s_mov_b32 s69, 1146880\n"
"buffer_load_dwordx4 v1, s[40:43], s69 offen offset:0 lds\n"
"v_xor_b32 v6, 0x10000, v6\n"
"ds_read_b64_tr_b16 a[196:197], v6 offset:0\n"
"ds_read_b64_tr_b16 a[198:199], v6 offset:2048\n"
"s_waitcnt lgkmcnt(4)\n"
"v_mfma_f32_32x32x16_bf16 v[160:175], a[216:219], v[60:63], v[160:175]\n"
"v_xor_b32 v7, 0x10000, v7\n"
"ds_read_b64_tr_b16 a[200:201], v7 offset:0\n"
"ds_read_b64_tr_b16 a[202:203], v7 offset:2048\n"
"v_mfma_f32_32x32x16_bf16 v[176:191], a[220:223], v[60:63], v[176:191]\n"
"buffer_load_dwordx4 v2, s[40:43], s69 offen offset:1024 lds\n"
"v_xor_b32 v8, 0x10000, v8\n"
"ds_read_b64_tr_b16 a[204:205], v8 offset:0\n"
"ds_read_b64_tr_b16 a[206:207], v8 offset:2048\n"
"s_waitcnt lgkmcnt(4)\n"
"v_mfma_f32_32x32x16_bf16 v[128:143], a[192:195], v[64:67], v[128:143]\n"
"ds_read_b64_tr_b16 a[208:209], v5 offset:8192\n"
"ds_read_b64_tr_b16 a[210:211], v5 offset:10240\n"
"v_mfma_f32_32x32x16_bf16 v[144:159], a[196:199], v[64:67], v[144:159]\n"
"buffer_load_dwordx4 v3, s[40:43], s69 offen offset:2048 lds\n"
"ds_read_b64_tr_b16 a[212:213], v6 offset:8192\n"
"ds_read_b64_tr_b16 a[214:215], v6 offset:10240\n"
"s_waitcnt lgkmcnt(4)\n"
"v_mfma_f32_32x32x16_bf16 v[160:175], a[200:203], v[64:67], v[160:175]\n"
"ds_read_b64_tr_b16 a[216:217], v7 offset:8192\n"
"ds_read_b64_tr_b16 a[218:219], v7 offset:10240\n"
"v_mfma_f32_32x32x16_bf16 v[176:191], a[204:207], v[64:67], v[176:191]\n"
"buffer_load_dwordx4 v4, s[40:43], s69 offen offset:3072 lds\n"
"ds_read_b64_tr_b16 a[220:221], v8 offset:8192\n"
"ds_read_b64_tr_b16 a[222:223], v8 offset:10240\n"
"s_waitcnt lgkmcnt(4)\n"
"v_mfma_f32_32x32x16_bf16 v[128:143], a[208:211], v[68:71], v[128:143]\n"
"s_waitcnt vmcnt(36)\n"
"s_barrier\n"
"ds_read_b64_tr_b16 a[192:193], v5 offset:16384\n"
"ds_read_b64_tr_b16 a[194:195], v5 offset:18432\n"
"v_mfma_f32_32x32x16_bf16 v[144:159], a[212:215], v[68:71], v[144:159]\n"
"s_add_u32 m0, s68, 49152\n"
"s_mov_b32 s69, 1261568\n"
"buffer_load_dwordx4 v1, s[40:43], s69 offen offset:0 lds\n"
"ds_read_b64_tr_b16 a[196:197], v6 offset:16384\n"
"ds_read_b64_tr_b16 a[198:199], v6 offset:18432\n"
"s_waitcnt lgkmcnt(4)\n"
"v_mfma_f32_32x32x16_bf16 v[160:175], a[216:219], v[68:71], v[160:175]\n"
"ds_read_b64_tr_b16 a[200:201], v7 offset:16384\n"
"ds_read_b64_tr_b16 a[202:203], v7 offset:18432\n"
"v_mfma_f32_32x32x16_bf16 v[176:191], a[220:223], v[68:71], v[176:191]\n"
"buffer_load_dwordx4 v2, s[40:43], s69 offen offset:1024 lds\n"
"ds_read_b64_tr_b16 a[204:205], v8 offset:16384\n"
"ds_read_b64_tr_b16 a[206:207], v8 offset:18432\n"
"s_waitcnt lgkmcnt(4)\n"
"v_mfma_f32_32x32x16_bf16 v[128:143], a[192:195], v[72:75], v[128:143]\n"
"ds_read_b64_tr_b16 a[208:209], v5 offset:24576\n"
"ds_read_b64_tr_b16 a[210:211], v5 offset:26624\n"
"s_lshl_b32 s85, %[T], 3\n"
"s_add_u32 s85, s85, %[g]\n"
"s_mul_i32 s85, s85, 7168\n"
"s_mov_b64 s[48:49], %[out]\n"
"s_add_u32 s48, s48, s85\n"
"s_addc_u32 s49, s49, 0\n"
"s_mov_b32 s50, 7168\n"
"s_mov_b32 s51, 0x00020000\n"
"v_mfma_f32_32x32x16_bf16 v[144:159], a[196:199], v[72:75], v[144:159]\n"
"buffer_load_dwordx4 v3, s[40:43], s69 offen offset:2048 lds\n"
"ds_read_b64_tr_b16 a[212:213], v6 offset:24576\n"
"ds_read_b64_tr_b16 a[214:215], v6 offset:26624\n"
"s_waitcnt lgkmcnt(4)\n"
"v_mfma_f32_32x32x16_bf16 v[160:175], a[200:203], v[72:75], v[160:175]\n"
"ds_read_b64_tr_b16 a[216:217], v7 offset:24576\n"
"ds_read_b64_tr_b16 a[218:219], v7 offset:26624\n"
"v_mfma_f32_32x32x16_bf16 v[176:191], a[204:207], v[72:75], v[176:191]\n"
"buffer_load_dwordx4 v4, s[40:43], s69 offen offset:3072 lds\n"
"ds_read_b64_tr_b16 a[220:221], v8 offset:24576\n"
"ds_read_b64_tr_b16 a[222:223], v8 offset:26624\n"
"s_waitcnt lgkmcnt(4)\n"
"v_mfma_f32_32x32x16_bf16 v[128:143], a[208:211], v[76:79], v[128:143]\n"
"s_waitcnt vmcnt(36)\n"
"s_barrier\n"
"ds_read_b64_tr_b16 a[192:193], v5 offset:32768\n"
"ds_read_b64_tr_b16 a[194:195], v5 offset:34816\n"
"v_mfma_f32_32x32x16_bf16 v[144:159], a[212:215], v[76:79], v[144:159]\n"
"s_add_u32 m0, s68, 65536\n"
"s_mov_b32 s69, 1376256\n"
"buffer_load_dwordx4 v1, s[40:43], s69 offen offset:0 lds\n"
"ds_read_b64_tr_b16 a[196:197], v6 offset:32768\n"
"ds_read_b64_tr_b16 a[198:199], v6 offset:34816\n"
"s_waitcnt lgkmcnt(4)\n"
"v_mfma_f32_32x32x16_bf16 v[160:175], a[216:219], v[76:79], v[160:175]\n"
"ds_read_b64_tr_b16 a[200:201], v7 offset:32768\n"
"ds_read_b64_tr_b16 a[202:203], v7 offset:34816\n"
"s_waitcnt vmcnt(36)\n"
"v_max_u32 v25, v80, v81\n"
"v_max3_u32 v25, v82, v83, v25\n"
"s_waitcnt vmcnt(35)\n"
"v_max3_u32 v25, v84, v85, v25\n"
"v_max3_u32 v25, v86, v87, v25\n"
"s_waitcnt vmcnt(34)\n"
"v_max3_u32 v25, v88, v89, v25\n"
"v_max3_u32 v25, v90, v91, v25\n"
"s_waitcnt vmcnt(33)\n"
"v_max3_u32 v25, v92, v93, v25\n"
"v_max3_u32 v25, v94, v95, v25\n"
"v_cmp_eq_u32 vcc, -1, v25\n"
"s_cmp_eq_u64 vcc, 0\n"
"s_cbranch_scc1 .Lok5_%=\n"
"s_movk_i32 s84, 0x7000\n"
".Lsp5_%=:\n"
"s_movk_i32 s70, 12288\n"
"buffer_load_dwordx4 v[80:83], v23, s[56:59], s70 offen offset:0 sc1\n"
"buffer_load_dwordx4 v[84:87], v23, s[56:59], s70 offen offset:1024 sc1\n"
"buffer_load_dwordx4 v[88:91], v23, s[56:59], s70 offen offset:2048 sc1\n"
"buffer_load_dwordx4 v[92:95], v23, s[56:59], s70 offen offset:3072 sc1\n"
"s_waitcnt vmcnt(0)\n"
"v_max_u32 v25, v80, v81\n"
"v_max3_u32 v25, v82, v83, v25\n"
"v_max3_u32 v25, v84, v85, v25\n"
"v_max3_u32 v25, v86, v87, v25\n"
"v_max3_u32 v25, v88, v89, v25\n"
"v_max3_u32 v25, v90, v91, v25\n"
"v_max3_u32 v25, v92, v93, v25\n"
"v_max3_u32 v25, v94, v95, v25\n"
"v_cmp_eq_u32 vcc, -1, v25\n"
"s_cmp_eq_u64 vcc, 0\n"
"s_cbranch_scc1 .Lok5_%=\n"
"s_sub_u32 s84, s84, 1\n"
"s_cmp_lg_u32 s84, 0\n"
"s_cbranch_scc1 .Lsp5_%=\n"
".Lok5_%=:\n"
"v_mfma_f32_32x32x16_bf16 v[176:191], a[220:223], v[76:79], v[176:191]\n"
"buffer_load_dwordx4 v2, s[40:43], s69 offen offset:1024 lds\n"
"ds_read_b64_tr_b16 a[204:205], v8 offset:32768\n"
"ds_read_b64_tr_b16 a[206:207], v8 offset:34816\n"
"s_waitcnt lgkmcnt(4)\n"
"v_mfma_f32_32x32x16_bf16 v[128:143], a[192:195], v[80:83], v[128:143]\n"
"ds_read_b64_tr_b16 a[208:209], v5 offset:40960\n"
"ds_read_b64_tr_b16 a[210:211], v5 offset:43008\n"
"v_mfma_f32_32x32x16_bf16 v[144:159], a[196:199], v[80:83], v[144:159]\n"
"buffer_load_dwordx4 v3, s[40:43], s69 offen offset:2048 lds\n"
"ds_read_b64_tr_b16 a[212:213], v6 offset:40960\n"
"ds_read_b64_tr_b16 a[214:215], v6 offset:43008\n"
"s_waitcnt lgkmcnt(4)\n"
"v_mfma_f32_32x32x16_bf16 v[160:175], a[200:203], v[80:83], v[160:175]\n"
"ds_read_b64_tr_b16 a[216:217], v7 offset:40960\n"
"ds_read_b64_tr_b16 a[218:219], v7 offset:43008\n"
"v_mfma_f32_32x32x16_bf16 v[176:191], a[204:207], v[80:83], v[176:191]\n"
"buffer_load_dwordx4 v4, s[40:43], s69 offen offset:3072 lds\n"
"ds_read_b64_tr_b16 a[220:221], v8 offset:40960\n"
"ds_read_b64_tr_b16 a[222:223], v8 offset:43008\n"
"s_waitcnt lgkmcnt(4)\n"
"v_mfma_f32_32x32x16_bf16 v[128:143], a[208:211], v[84:87], v[128:143]\n"
"s_waitcnt vmcnt(20)\n"
"s_barrier\n"
"ds_read_b64_tr_b16 a[192:193], v5 offset:49152\n"
"ds_read_b64_tr_b16 a[194:195], v5 offset:51200\n"
"v_mfma_f32_32x32x16_bf16 v[144:159], a[212:215], v[84:87], v[144:159]\n"
"s_add_u32 m0, s68, 81920\n"
"s_mov_b32 s69, 1490944\n"
"buffer_load_dwordx4 v1, s[40:43], s69 offen offset:0 lds\n"
"ds_read_b64_tr_b16 a[196:197], v6 offset:49152\n"
"ds_read_b64_tr_b16 a[198:199], v6 offset:51200\n"
"s_waitcnt lgkmcnt(4)\n"
"v_mfma_f32_32x32x16_bf16 v[160:175], a[216:219], v[84:87], v[160:175]\n"
"ds_read_b64_tr_b16 a[200:201], v7 offset:49152\n"
"ds_read_b64_tr_b16 a[202:203], v7 offset:51200\n"
"v_mfma_f32_32x32x16_bf16 v[176:191], a[220:223], v[84:87], v[176:191]\n"
"buffer_load_dwordx4 v2, s[40:43], s69 offen offset:1024 lds\n"
"ds_read_b64_tr_b16 a[204:205], v8 offset:49152\n"
"ds_read_b64_tr_b16 a[206:207], v8 offset:51200\n"
"s_waitcnt lgkmcnt(4)\n"
"v_mfma_f32_32x32x16_bf16 v[128:143], a[192:195], v[88:91], v[128:143]\n"
"ds_read_b64_tr_b16 a[208:209], v5 offset:57344\n"
"ds_read_b64_tr_b16 a[210:211], v5 offset:59392\n"
"v_mfma_f32_32x32x16_bf16 v[144:159], a[196:199], v[88:91], v[144:159]\n"
"buffer_load_dwordx4 v3, s[40:43], s69 offen offset:2048 lds\n"
"ds_read_b64_tr_b16 a[212:213], v6 offset:57344\n"
"ds_read_b64_tr_b16 a[214:215], v6 offset:59392\n"
"s_waitcnt lgkmcnt(4)\n"
"v_mfma_f32_32x32x16_bf16 v[160:175], a[200:203], v[88:91], v[160:175]\n"
"ds_read_b64_tr_b16 a[216:217], v7 offset:57344\n"
"ds_read_b64_tr_b16 a[218:219], v7 offset:59392\n"
"v_mfma_f32_32x32x16_bf16 v[176:191], a[204:207], v[88:91], v[176:191]\n"
"buffer_load_dwordx4 v4, s[40:43], s69 offen offset:3072 lds\n"
"ds_read_b64_tr_b16 a[220:221], v8 offset:57344\n"
"ds_read_b64_tr_b16 a[222:223], v8 offset:59392\n"
"s_waitcnt lgkmcnt(4)\n"
"v_mfma_f32_32x32x16_bf16 v[128:143], a[208:211], v[92:95], v[128:143]\n"
"s_waitcnt vmcnt(20)\n"
"s_barrier\n"
"v_lshrrev_b32 v28, 3, v0\n"
"s_lshl_b32 s75, %[ss], 5\n"
"v_add_u32 v28, s75, v28\n"
"s_lshl_b32 s75, %[rr], 7\n"
"v_add_u32 v28, s75, v28\n"
"v_bfe_u32 v29, v0, 4, 1\n"
"v_lshlrev_b32 v29, 2, v29\n"
"v_and_b32 v26, 7, v0\n"
"v_xor_b32 v29, v29, v26\n"
"v_xor_b32 v5, 0x10000, v5\n"
"ds_read_b64_tr_b16 a[192:193], v5 offset:0\n"
"ds_read_b64_tr_b16 a[194:195], v5 offset:2048\n"
"v_mfma_f32_32x32x16_bf16 v[144:159], a[212:215], v[92:95], v[144:159]\n"
"v_lshlrev_b32 v29, 4, v29\n"
"v_mul_u32_u24 v28, 896, v28\n"
"v_add_u32 v1, v28, v29\n"
"v_add_u32 v2, 6144, v1\n"
"v_add_u32 v3, 12288, v1\n"
"v_add_u32 v4, 18432, v1\n"
"s_add_u32 m0, s68, 98304\n"
"s_mov_b32 s69, 0\n"
"buffer_load_dwordx4 v1, s[44:47], s69 offen offset:0 lds\n"
"v_xor_b32 v6, 0x10000, v6\n"
"ds_read_b64_tr_b16 a[196:197], v6 offset:0\n"
"ds_read_b64_tr_b16 a[198:199], v6 offset:2048\n"
"s_waitcnt lgkmcnt(4)\n"
"v_mfma_f32_32x32x16_bf16 v[160:175], a[216:219], v[92:95], v[160:175]\n"
"v_xor_b32 v7, 0x10000, v7\n"
"ds_read_b64_tr_b16 a[200:201], v7 offset:0\n"
"ds_read_b64_tr_b16 a[202:203], v7 offset:2048\n"
"v_max_u32 v25, v96, v97\n"
"v_max3_u32 v25, v98, v99, v25\n"
"v_max3_u32 v25, v100, v101, v25\n"
"v_max3_u32 v25, v102, v103, v25\n"
"v_max3_u32 v25, v104, v105, v25\n"
"v_max3_u32 v25, v106, v107, v25\n"
"v_max3_u32 v25, v108, v109, v25\n"
"v_max3_u32 v25, v110, v111, v25\n"
"v_cmp_eq_u32 vcc, -1, v25\n"
"s_cmp_eq_u64 vcc, 0\n"
"s_cbranch_scc1 .Lok6_%=\n"
"s_movk_i32 s84, 0x7000\n"
".Lsp6_%=:\n"
"s_movk_i32 s70, 16384\n"
"buffer_load_dwordx4 v[96:99], v23, s[56:59], s70 offen offset:0 sc1\n"
"buffer_load_dwordx4 v[100:103], v23, s[56:59], s70 offen offset:1024 sc1\n"
"buffer_load_dwordx4 v[104:107], v23, s[56:59], s70 offen offset:2048 sc1\n"
"buffer_load_dwordx4 v[108:111], v23, s[56:59], s70 offen offset:3072 sc1\n"
"s_waitcnt vmcnt(0)\n"
"v_max_u32 v25, v96, v97\n"
"v_max3_u32 v25, v98, v99, v25\n"
"v_max3_u32 v25, v100, v101, v25\n"
"v_max3_u32 v25, v102, v103, v25\n"
"v_max3_u32 v25, v104, v105, v25\n"
"v_max3_u32 v25, v106, v107, v25\n"
"v_max3_u32 v25, v108, v109, v25\n"
"v_max3_u32 v25, v110, v111, v25\n"
"v_cmp_eq_u32 vcc, -1, v25\n"
"s_cmp_eq_u64 vcc, 0\n"
"s_cbranch_scc1 .Lok6_%=\n"
"s_sub_u32 s84, s84, 1\n"
"s_cmp_lg_u32 s84, 0\n"
"s_cbranch_scc1 .Lsp6_%=\n"
".Lok6_%=:\n"
"v_mfma_f32_32x32x16_bf16 v[176:191], a[220:223], v[92:95], v[176:191]\n"
"buffer_load_dwordx4 v2, s[44:47], s69 offen offset:1024 lds\n"
"v_xor_b32 v8, 0x10000, v8\n"
"ds_read_b64_tr_b16 a[204:205], v8 offset:0\n"
"ds_read_b64_tr_b16 a[206:207], v8 offset:2048\n"
"s_waitcnt lgkmcnt(4)\n"
"v_mfma_f32_32x32x16_bf16 v[128:143], a[192:195], v[96:99], v[128:143]\n"
"ds_read_b64_tr_b16 a[208:209], v5 offset:8192\n"
"ds_read_b64_tr_b16 a[210:211], v5 offset:10240\n"
"v_mfma_f32_32x32x16_bf16 v[144:159], a[196:199], v[96:99], v[144:159]\n"
"buffer_load_dwordx4 v3, s[44:47], s69 offen offset:2048 lds\n"
"ds_read_b64_tr_b16 a[212:213], v6 offset:8192\n"
"ds_read_b64_tr_b16 a[214:215], v6 offset:10240\n"
"s_waitcnt lgkmcnt(4)\n"
"v_mfma_f32_32x32x16_bf16 v[160:175], a[200:203], v[96:99], v[160:175]\n"
"ds_read_b64_tr_b16 a[216:217], v7 offset:8192\n"
"ds_read_b64_tr_b16 a[218:219], v7 offset:10240\n"
"v_mfma_f32_32x32x16_bf16 v[176:191], a[204:207], v[96:99], v[176:191]\n"
"buffer_load_dwordx4 v4, s[44:47], s69 offen offset:3072 lds\n"
"ds_read_b64_tr_b16 a[220:221], v8 offset:8192\n"
"ds_read_b64_tr_b16 a[222:223], v8 offset:10240\n"
"s_waitcnt lgkmcnt(4)\n"
"v_mfma_f32_32x32x16_bf16 v[128:143], a[208:211], v[100:103], v[128:143]\n"
"s_waitcnt vmcnt(20)\n"
"s_barrier\n"
"ds_read_b64_tr_b16 a[192:193], v5 offset:16384\n"
"ds_read_b64_tr_b16 a[194:195], v5 offset:18432\n"
"v_mfma_f32_32x32x16_bf16 v[144:159], a[212:215], v[100:103], v[144:159]\n"
"s_add_u32 m0, s68, 114688\n"
"s_mov_b32 s69, 57344\n"
"buffer_load_dwordx4 v1, s[44:47], s69 offen offset:0 lds\n"
"ds_read_b64_tr_b16 a[196:197], v6 offset:16384\n"
"ds_read_b64_tr_b16 a[198:199], v6 offset:18432\n"
"s_waitcnt lgkmcnt(4)\n"
"v_mfma_f32_32x32x16_bf16 v[160:175], a[216:219], v[100:103], v[160:175]\n"
"ds_read_b64_tr_b16 a[200:201], v7 offset:16384\n"
"ds_read_b64_tr_b16 a[202:203], v7 offset:18432\n"
"v_mfma_f32_32x32x16_bf16 v[176:191], a[220:223], v[100:103], v[176:191]\n"
"buffer_load_dwordx4 v2, s[44:47], s69 offen offset:1024 lds\n"
"ds_read_b64_tr_b16 a[204:205], v8 offset:16384\n"
"ds_read_b64_tr_b16 a[206:207], v8 offset:18432\n"
"s_waitcnt lgkmcnt(4)\n"
"v_mfma_f32_32x32x16_bf16 v[128:143], a[192:195], v[104:107], v[128:143]\n"
"ds_read_b64_tr_b16 a[208:209], v5 offset:24576\n"
"ds_read_b64_tr_b16 a[210:211], v5 offset:26624\n"
"v_mfma_f32_32x32x16_bf16 v[144:159], a[196:199], v[104:107], v[144:159]\n"
"buffer_load_dwordx4 v3, s[44:47], s69 offen offset:2048 lds\n"
"ds_read_b64_tr_b16 a[212:213], v6 offset:24576\n"
"ds_read_b64_tr_b16 a[214:215], v6 offset:26624\n"
"s_waitcnt lgkmcnt(4)\n"
"v_mfma_f32_32x32x16_bf16 v[160:175], a[200:203], v[104:107], v[160:175]\n"
"ds_read_b64_tr_b16 a[216:217], v7 offset:24576\n"
"ds_read_b64_tr_b16 a[218:219], v7 offset:26624\n"
"v_mfma_f32_32x32x16_bf16 v[176:191], a[204:207], v[104:107], v[176:191]\n"
"buffer_load_dwordx4 v4, s[44:47], s69 offen offset:3072 lds\n"
"ds_read_b64_tr_b16 a[220:221], v8 offset:24576\n"
"ds_read_b64_tr_b16 a[222:223], v8 offset:26624\n"
"s_waitcnt lgkmcnt(4)\n"
"v_mfma_f32_32x32x16_bf16 v[128:143], a[208:211], v[108:111], v[128:143]\n"
"s_waitcnt vmcnt(20)\n"
"s_barrier\n"
"ds_read_b64_tr_b16 a[192:193], v5 offset:32768\n"
"ds_read_b64_tr_b16 a[194:195], v5 offset:34816\n"
"v_mfma_f32_32x32x16_bf16 v[144:159], a[212:215], v[108:111], v[144:159]\n"
"s_add_u32 m0, s68, 0\n"
"s_mov_b32 s69, 128\n"
"buffer_load_dwordx4 v1, s[44:47], s69 offen offset:0 lds\n"
"ds_read_b64_tr_b16 a[196:197], v6 offset:32768\n"
"ds_read_b64_tr_b16 a[198:199], v6 offset:34816\n"
"s_waitcnt lgkmcnt(4)\n"
"v_mfma_f32_32x32x16_bf16 v[160:175], a[216:219], v[108:111], v[160:175]\n"
"ds_read_b64_tr_b16 a[200:201], v7 offset:32768\n"
"ds_read_b64_tr_b16 a[202:203], v7 offset:34816\n"
"v_max_u32 v25, v112, v113\n"
"v_max3_u32 v25, v114, v115, v25\n"
"v_max3_u32 v25, v116, v117, v25\n"
"v_max3_u32 v25, v118, v119, v25\n"
"v_max3_u32 v25, v120, v121, v25\n"
"v_max3_u32 v25, v122, v123, v25\n"
"v_max3_u32 v25, v124, v125, v25\n"
"v_max3_u32 v25, v126, v127, v25\n"
"v_cmp_eq_u32 vcc, -1, v25\n"
"s_cmp_eq_u64 vcc, 0\n"
"s_cbranch_scc1 .Lok7_%=\n"
"s_movk_i32 s84, 0x7000\n"
".Lsp7_%=:\n"
"s_movk_i32 s70, 20480\n"
"buffer_load_dwordx4 v[112:115], v23, s[56:59], s70 offen offset:0 sc1\n"
"buffer_load_dwordx4 v[116:119], v23, s[56:59], s70 offen offset:1024 sc1\n"
"buffer_load_dwordx4 v[120:123], v23, s[56:59], s70 offen offset:2048 sc1\n"
"buffer_load_dwordx4 v[124:127], v23, s[56:59], s70 offen offset:3072 sc1\n"
"s_waitcnt vmcnt(0)\n"
"v_max_u32 v25, v112, v113\n"
"v_max3_u32 v25, v114, v115, v25\n"
"v_max3_u32 v25, v116, v117, v25\n"
"v_max3_u32 v25, v118, v119, v25\n"
"v_max3_u32 v25, v120, v121, v25\n"
"v_max3_u32 v25, v122, v123, v25\n"
"v_max3_u32 v25, v124, v125, v25\n"
"v_max3_u32 v25, v126, v127, v25\n"
"v_cmp_eq_u32 vcc, -1, v25\n"
"s_cmp_eq_u64 vcc, 0\n"
"s_cbranch_scc1 .Lok7_%=\n"
"s_sub_u32 s84, s84, 1\n"
"s_cmp_lg_u32 s84, 0\n"
"s_cbranch_scc1 .Lsp7_%=\n"
".Lok7_%=:\n"
"v_mfma_f32_32x32x16_bf16 v[176:191], a[220:223], v[108:111], v[176:191]\n"
"buffer_load_dwordx4 v2, s[44:47], s69 offen offset:1024 lds\n"
"ds_read_b64_tr_b16 a[204:205], v8 offset:32768\n"
"ds_read_b64_tr_b16 a[206:207], v8 offset:34816\n"
"s_waitcnt lgkmcnt(4)\n"
"v_mfma_f32_32x32x16_bf16 v[128:143], a[192:195], v[112:115], v[128:143]\n"
"ds_read_b64_tr_b16 a[208:209], v5 offset:40960\n"
"ds_read_b64_tr_b16 a[210:211], v5 offset:43008\n"
"v_mfma_f32_32x32x16_bf16 v[144:159], a[196:199], v[112:115], v[144:159]\n"
"buffer_load_dwordx4 v3, s[44:47], s69 offen offset:2048 lds\n"
"ds_read_b64_tr_b16 a[212:213], v6 offset:40960\n"
"ds_read_b64_tr_b16 a[214:215], v6 offset:43008\n"
"s_waitcnt lgkmcnt(4)\n"
"v_mfma_f32_32x32x16_bf16 v[160:175], a[200:203], v[112:115], v[160:175]\n"
"ds_read_b64_tr_b16 a[216:217], v7 offset:40960\n"
"ds_read_b64_tr_b16 a[218:219], v7 offset:43008\n"
"v_mfma_f32_32x32x16_bf16 v[176:191], a[204:207], v[112:115], v[176:191]\n"
"buffer_load_dwordx4 v4, s[44:47], s69 offen offset:3072 lds\n"
"ds_read_b64_tr_b16 a[220:221], v8 offset:40960\n"
"ds_read_b64_tr_b16 a[222:223], v8 offset:43008\n"
"s_waitcnt lgkmcnt(4)\n"
"v_mfma_f32_32x32x16_bf16 v[128:143], a[208:211], v[116:119], v[128:143]\n"
"s_waitcnt vmcnt(20)\n"
"s_barrier\n"
"ds_read_b64_tr_b16 a[192:193], v5 offset:49152\n"
"ds_read_b64_tr_b16 a[194:195], v5 offset:51200\n"
"v_mfma_f32_32x32x16_bf16 v[144:159], a[212:215], v[116:119], v[144:159]\n"
"s_add_u32 m0, s68, 16384\n"
"s_mov_b32 s69, 57472\n"
"buffer_load_dwordx4 v1, s[44:47], s69 offen offset:0 lds\n"
"ds_read_b64_tr_b16 a[196:197], v6 offset:49152\n"
"ds_read_b64_tr_b16 a[198:199], v6 offset:51200\n"
"s_waitcnt lgkmcnt(4)\n"
"v_mfma_f32_32x32x16_bf16 v[160:175], a[216:219], v[116:119], v[160:175]\n"
"ds_read_b64_tr_b16 a[200:201], v7 offset:49152\n"
"ds_read_b64_tr_b16 a[202:203], v7 offset:51200\n"
"v_mfma_f32_32x32x16_bf16 v[176:191], a[220:223], v[116:119], v[176:191]\n"
"buffer_load_dwordx4 v2, s[44:47], s69 offen offset:1024 lds\n"
"ds_read_b64_tr_b16 a[204:205], v8 offset:49152\n"
"ds_read_b64_tr_b16 a[206:207], v8 offset:51200\n"
"s_waitcnt lgkmcnt(4)\n"
"v_mfma_f32_32x32x16_bf16 v[128:143], a[192:195], v[120:123], v[128:143]\n"
"ds_read_b64_tr_b16 a[208:209], v5 offset:57344\n"
"ds_read_b64_tr_b16 a[210:211], v5 offset:59392\n"
"v_mfma_f32_32x32x16_bf16 v[144:159], a[196:199], v[120:123], v[144:159]\n"
"buffer_load_dwordx4 v3, s[44:47], s69 offen offset:2048 lds\n"
"ds_read_b64_tr_b16 a[212:213], v6 offset:57344\n"
"ds_read_b64_tr_b16 a[214:215], v6 offset:59392\n"
"s_waitcnt lgkmcnt(4)\n"
"v_mfma_f32_32x32x16_bf16 v[160:175], a[200:203], v[120:123], v[160:175]\n"
"ds_read_b64_tr_b16 a[216:217], v7 offset:57344\n"
"ds_read_b64_tr_b16 a[218:219], v7 offset:59392\n"
"v_mfma_f32_32x32x16_bf16 v[176:191], a[204:207], v[120:123], v[176:191]\n"
"buffer_load_dwordx4 v4, s[44:47], s69 offen offset:3072 lds\n"
"ds_read_b64_tr_b16 a[220:221], v8 offset:57344\n"
"ds_read_b64_tr_b16 a[222:223], v8 offset:59392\n"
"s_waitcnt lgkmcnt(4)\n"
"v_mfma_f32_32x32x16_bf16 v[128:143], a[208:211], v[124:127], v[128:143]\n"
"s_waitcnt vmcnt(20)\n"
"s_barrier\n"
"v_xor_b32 v5, 0x10000, v5\n"
"ds_read_b64_tr_b16 a[192:193], v5 offset:0\n"
"ds_read_b64_tr_b16 a[194:195], v5 offset:2048\n"
"v_mfma_f32_32x32x16_bf16 v[144:159], a[212:215], v[124:127], v[144:159]\n"
"s_add_u32 m0, s68, 32768\n"
"s_mov_b32 s69, 256\n"
"buffer_load_dwordx4 v1, s[44:47], s69 offen offset:0 lds\n"
"v_xor_b32 v6, 0x10000, v6\n"
"ds_read_b64_tr_b16 a[196:197], v6 offset:0\n"
"ds_read_b64_tr_b16 a[198:199], v6 offset:2048\n"
"s_waitcnt lgkmcnt(4)\n"
"v_mfma_f32_32x32x16_bf16 v[160:175], a[216:219], v[124:127], v[160:175]\n"
"v_xor_b32 v7, 0x10000, v7\n"
"ds_read_b64_tr_b16 a[200:201], v7 offset:0\n"
"ds_read_b64_tr_b16 a[202:203], v7 offset:2048\n"
"v_max_u32 v25, v192, v193\n"
"v_max3_u32 v25, v194, v195, v25\n"
"v_max3_u32 v25, v196, v197, v25\n"
"v_max3_u32 v25, v198, v199, v25\n"
"v_max3_u32 v25, v200, v201, v25\n"
"v_max3_u32 v25, v202, v203, v25\n"
"v_max3_u32 v25, v204, v205, v25\n"
"v_max3_u32 v25, v206, v207, v25\n"
"v_cmp_eq_u32 vcc, -1, v25\n"
"s_cmp_eq_u64 vcc, 0\n"
"s_cbranch_scc1 .Lok8_%=\n"
"s_movk_i32 s84, 0x7000\n"
".Lsp8_%=:\n"
"s_movk_i32 s70, 24576\n"
"buffer_load_dwordx4 v[192:195], v23, s[56:59], s70 offen offset:0 sc1\n"
"buffer_load_dwordx4 v[196:199], v23, s[56:59], s70 offen offset:1024 sc1\n"
"buffer_load_dwordx4 v[200:203], v23, s[56:59], s70 offen offset:2048 sc1\n"
"buffer_load_dwordx4 v[204:207], v23, s[56:59], s70 offen offset:3072 sc1\n"
"s_waitcnt vmcnt(0)\n"
"v_max_u32 v25, v192, v193\n"
"v_max3_u32 v25, v194, v195, v25\n"
"v_max3_u32 v25, v196, v197, v25\n"
"v_max3_u32 v25, v198, v199, v25\n"
"v_max3_u32 v25, v200, v201, v25\n"
"v_max3_u32 v25, v202, v203, v25\n"
"v_max3_u32 v25, v204, v205, v25\n"
"v_max3_u32 v25, v206, v207, v25\n"
"v_cmp_eq_u32 vcc, -1, v25\n"
"s_cmp_eq_u64 vcc, 0\n"
"s_cbranch_scc1 .Lok8_%=\n"
"s_sub_u32 s84, s84, 1\n"
"s_cmp_lg_u32 s84, 0\n"
"s_cbranch_scc1 .Lsp8_%=\n"
".Lok8_%=:\n"
"v_mfma_f32_32x32x16_bf16 v[176:191], a[220:223], v[124:127], v[176:191]\n"
"buffer_load_dwordx4 v2, s[44:47], s69 offen offset:1024 lds\n"
"v_xor_b32 v8, 0x10000, v8\n"
"ds_read_b64_tr_b16 a[204:205], v8 offset:0\n"
"ds_read_b64_tr_b16 a[206:207], v8 offset:2048\n"
"s_waitcnt lgkmcnt(4)\n"
"v_mfma_f32_32x32x16_bf16 v[128:143], a[192:195], v[192:195], v[128:143]\n"
"ds_read_b64_tr_b16 a[208:209], v5 offset:8192\n"
"ds_read_b64_tr_b16 a[210:211], v5 offset:10240\n"
"v_mfma_f32_32x32x16_bf16 v[144:159], a[196:199], v[192:195], v[144:159]\n"
"buffer_load_dwordx4 v3, s[44:47], s69 offen offset:2048 lds\n"
"ds_read_b64_tr_b16 a[212:213], v6 offset:8192\n"
"ds_read_b64_tr_b16 a[214:215], v6 offset:10240\n"
"s_waitcnt lgkmcnt(4)\n"
"v_mfma_f32_32x32x16_bf16 v[160:175], a[200:203], v[192:195], v[160:175]\n"
"ds_read_b64_tr_b16 a[216:217], v7 offset:8192\n"
"ds_read_b64_tr_b16 a[218:219], v7 offset:10240\n"
"v_mfma_f32_32x32x16_bf16 v[176:191], a[204:207], v[192:195], v[176:191]\n"
"buffer_load_dwordx4 v4, s[44:47], s69 offen offset:3072 lds\n"
"ds_read_b64_tr_b16 a[220:221], v8 offset:8192\n"
"ds_read_b64_tr_b16 a[222:223], v8 offset:10240\n"
"s_waitcnt lgkmcnt(4)\n"
"v_mfma_f32_32x32x16_bf16 v[128:143], a[208:211], v[196:199], v[128:143]\n"
"s_waitcnt vmcnt(20)\n"
"s_barrier\n"
"ds_read_b64_tr_b16 a[192:193], v5 offset:16384\n"
"ds_read_b64_tr_b16 a[194:195], v5 offset:18432\n"
"v_mfma_f32_32x32x16_bf16 v[144:159], a[212:215], v[196:199], v[144:159]\n"
"s_add_u32 m0, s68, 49152\n"
"s_mov_b32 s69, 57600\n"
"buffer_load_dwordx4 v1, s[44:47], s69 offen offset:0 lds\n"
"ds_read_b64_tr_b16 a[196:197], v6 offset:16384\n"
"ds_read_b64_tr_b16 a[198:199], v6 offset:18432\n"
"s_waitcnt lgkmcnt(4)\n"
"v_mfma_f32_32x32x16_bf16 v[160:175], a[216:219], v[196:199], v[160:175]\n"
"ds_read_b64_tr_b16 a[200:201], v7 offset:16384\n"
"ds_read_b64_tr_b16 a[202:203], v7 offset:18432\n"
"v_mfma_f32_32x32x16_bf16 v[176:191], a[220:223], v[196:199], v[176:191]\n"
"buffer_load_dwordx4 v2, s[44:47], s69 offen offset:1024 lds\n"
"ds_read_b64_tr_b16 a[204:205], v8 offset:16384\n"
"ds_read_b64_tr_b16 a[206:207], v8 offset:18432\n"
"s_waitcnt lgkmcnt(4)\n"
"v_mfma_f32_32x32x16_bf16 v[128:143], a[192:195], v[200:203], v[128:143]\n"
"ds_read_b64_tr_b16 a[208:209], v5 offset:24576\n"
"ds_read_b64_tr_b16 a[210:211], v5 offset:26624\n"
"v_mfma_f32_32x32x16_bf16 v[144:159], a[196:199], v[200:203], v[144:159]\n"
"buffer_load_dwordx4 v3, s[44:47], s69 offen offset:2048 lds\n"
"ds_read_b64_tr_b16 a[212:213], v6 offset:24576\n"
"ds_read_b64_tr_b16 a[214:215], v6 offset:26624\n"
"s_waitcnt lgkmcnt(4)\n"
"v_mfma_f32_32x32x16_bf16 v[160:175], a[200:203], v[200:203], v[160:175]\n"
"ds_read_b64_tr_b16 a[216:217], v7 offset:24576\n"
"ds_read_b64_tr_b16 a[218:219], v7 offset:26624\n"
"v_mfma_f32_32x32x16_bf16 v[176:191], a[204:207], v[200:203], v[176:191]\n"
"buffer_load_dwordx4 v4, s[44:47], s69 offen offset:3072 lds\n"
"ds_read_b64_tr_b16 a[220:221], v8 offset:24576\n"
"ds_read_b64_tr_b16 a[222:223], v8 offset:26624\n"
"s_waitcnt lgkmcnt(4)\n"
"v_mfma_f32_32x32x16_bf16 v[128:143], a[208:211], v[204:207], v[128:143]\n"
"s_waitcnt vmcnt(16)\n"
"s_barrier\n"
"ds_read_b64_tr_b16 a[192:193], v9 offset:32768\n"
"ds_read_b64_tr_b16 a[194:195], v9 offset:33280\n"
"v_mfma_f32_32x32x16_bf16 v[144:159], a[212:215], v[204:207], v[144:159]\n"
"s_add_u32 m0, s68, 65536\n"
"s_mov_b32 s69, 384\n"
"buffer_load_dwordx4 v1, s[44:47], s69 offen offset:0 lds\n"
"ds_read_b64_tr_b16 a[196:197], v10 offset:32768\n"
"ds_read_b64_tr_b16 a[198:199], v10 offset:33280\n"
"s_waitcnt lgkmcnt(4)\n"
"v_mfma_f32_32x32x16_bf16 v[160:175], a[216:219], v[204:207], v[160:175]\n"
"ds_read_b64_tr_b16 a[200:201], v9 offset:34816\n"
"ds_read_b64_tr_b16 a[202:203], v9 offset:35328\n"
"v_cvt_pk_bf16_f32 v128, v128, v129\n"
"v_cvt_pk_bf16_f32 v129, v130, v131\n"
"v_cvt_pk_bf16_f32 v130, v132, v133\n"
"v_cvt_pk_bf16_f32 v131, v134, v135\n"
"v_mfma_f32_32x32x16_bf16 v[176:191], a[220:223], v[204:207], v[176:191]\n"
"buffer_load_dwordx4 v2, s[44:47], s69 offen offset:1024 lds\n"
"ds_read_b64_tr_b16 a[204:205], v10 offset:34816\n"
"ds_read_b64_tr_b16 a[206:207], v10 offset:35328\n"
"v_pk_max_i16 v128, v128, 0\n"
"v_pk_max_i16 v129, v129, 0\n"
"v_pk_max_i16 v130, v130, 0\n"
"v_pk_max_i16 v131, v131, 0\n"
"v_cvt_pk_bf16_f32 v136, v136, v137\n"
"v_cvt_pk_bf16_f32 v137, v138, v139\n"
"v_cvt_pk_bf16_f32 v138, v140, v141\n"
"v_cvt_pk_bf16_f32 v139, v142, v143\n"
"s_waitcnt lgkmcnt(4)\n"
"v_mfma_f32_32x32x16_bf16 v[32:47], a[192:195], v[128:131], 0\n"
"ds_read_b64_tr_b16 a[208:209], v9 offset:36864\n"
"ds_read_b64_tr_b16 a[210:211], v9 offset:37376\n"
"v_pk_max_i16 v136, v136, 0\n"
"v_pk_max_i16 v137, v137, 0\n"
"v_pk_max_i16 v138, v138, 0\n"
"v_pk_max_i16 v139, v139, 0\n"
"v_mfma_f32_32x32x16_bf16 v[48:63], a[196:199], v[128:131], 0\n"
"buffer_load_dwordx4 v3, s[44:47], s69 offen offset:2048 lds\n"
"ds_read_b64_tr_b16 a[212:213], v10 offset:36864\n"
"ds_read_b64_tr_b16 a[214:215], v10 offset:37376\n"
"v_cvt_pk_bf16_f32 v144, v144, v145\n"
"v_cvt_pk_bf16_f32 v145, v146, v147\n"
"v_cvt_pk_bf16_f32 v146, v148, v149\n"
"v_cvt_pk_bf16_f32 v147, v150, v151\n"
"s_waitcnt lgkmcnt(4)\n"
"v_mfma_f32_32x32x16_bf16 v[32:47], a[200:203], v[136:139], v[32:47]\n"
"ds_read_b64_tr_b16 a[216:217], v9 offset:38912\n"
"ds_read_b64_tr_b16 a[218:219], v9 offset:39424\n"
"v_pk_max_i16 v144, v144, 0\n"
"v_pk_max_i16 v145, v145, 0\n"
"v_pk_max_i16 v146, v146, 0\n"
"v_pk_max_i16 v147, v147, 0\n"
"v_mfma_f32_32x32x16_bf16 v[48:63], a[204:207], v[136:139], v[48:63]\n"
"buffer_load_dwordx4 v4, s[44:47], s69 offen offset:3072 lds\n"
"ds_read_b64_tr_b16 a[220:221], v10 offset:38912\n"
"ds_read_b64_tr_b16 a[222:223], v10 offset:39424\n"
"v_cvt_pk_bf16_f32 v152, v152, v153\n"
"v_cvt_pk_bf16_f32 v153, v154, v155\n"
"v_cvt_pk_bf16_f32 v154, v156, v157\n"
"v_cvt_pk_bf16_f32 v155, v158, v159\n"
"s_waitcnt lgkmcnt(4)\n"
"v_mfma_f32_32x32x16_bf16 v[32:47], a[208:211], v[144:147], v[32:47]\n"
"ds_read_b64_tr_b16 a[192:193], v9 offset:49152\n"
"ds_read_b64_tr_b16 a[194:195], v9 offset:49664\n"
"v_pk_max_i16 v152, v152, 0\n"
"v_pk_max_i16 v153, v153, 0\n"
"v_pk_max_i16 v154, v154, 0\n"
"v_pk_max_i16 v155, v155, 0\n"
"v_mfma_f32_32x32x16_bf16 v[48:63], a[212:215], v[144:147], v[48:63]\n"
"ds_read_b64_tr_b16 a[196:197], v10 offset:49152\n"
"ds_read_b64_tr_b16 a[198:199], v10 offset:49664\n"
"v_cvt_pk_bf16_f32 v160, v160, v161\n"
"v_cvt_pk_bf16_f32 v161, v162, v163\n"
"v_cvt_pk_bf16_f32 v162, v164, v165\n"
"v_cvt_pk_bf16_f32 v163, v166, v167\n"
"s_waitcnt lgkmcnt(4)\n"
"v_mfma_f32_32x32x16_bf16 v[32:47], a[216:219], v[152:155], v[32:47]\n"
"ds_read_b64_tr_b16 a[200:201], v9 offset:51200\n"
"ds_read_b64_tr_b16 a[202:203], v9 offset:51712\n"
"v_pk_max_i16 v160, v160, 0\n"
"v_pk_max_i16 v161, v161, 0\n"
"v_pk_max_i16 v162, v162, 0\n"
"v_pk_max_i16 v163, v163, 0\n"
"v_mfma_f32_32x32x16_bf16 v[48:63], a[220:223], v[152:155], v[48:63]\n"
"ds_read_b64_tr_b16 a[204:205], v10 offset:51200\n"
"ds_read_b64_tr_b16 a[206:207], v10 offset:51712\n"
"v_cvt_pk_bf16_f32 v168, v168, v169\n"
"v_cvt_pk_bf16_f32 v169, v170, v171\n"
"v_cvt_pk_bf16_f32 v170, v172, v173\n"
"v_cvt_pk_bf16_f32 v171, v174, v175\n"
"s_waitcnt lgkmcnt(4)\n"
"v_mfma_f32_32x32x16_bf16 v[32:47], a[192:195], v[160:163], v[32:47]\n"
"ds_read_b64_tr_b16 a[208:209], v9 offset:53248\n"
"ds_read_b64_tr_b16 a[210:211], v9 offset:53760\n"
"v_pk_max_i16 v168, v168, 0\n"
"v_pk_max_i16 v169, v169, 0\n"
"v_pk_max_i16 v170, v170, 0\n"
"v_pk_max_i16 v171, v171, 0\n"
"v_mfma_f32_32x32x16_bf16 v[48:63], a[196:199], v[160:163], v[48:63]\n"
"ds_read_b64_tr_b16 a[212:213], v10 offset:53248\n"
"ds_read_b64_tr_b16 a[214:215], v10 offset:53760\n"
"v_cvt_pk_bf16_f32 v176, v176, v177\n"
"v_cvt_pk_bf16_f32 v177, v178, v179\n"
"v_cvt_pk_bf16_f32 v178, v180, v181\n"
"v_cvt_pk_bf16_f32 v179, v182, v183\n"
"s_waitcnt lgkmcnt(4)\n"
"v_mfma_f32_32x32x16_bf16 v[32:47], a[200:203], v[168:171], v[32:47]\n"
"ds_read_b64_tr_b16 a[216:217], v9 offset:55296\n"
"ds_read_b64_tr_b16 a[218:219], v9 offset:55808\n"
"v_pk_max_i16 v176, v176, 0\n"
"v_pk_max_i16 v177, v177, 0\n"
"v_pk_max_i16 v178, v178, 0\n"
"v_pk_max_i16 v179, v179, 0\n"
"v_mfma_f32_32x32x16_bf16 v[48:63], a[204:207], v[168:171], v[48:63]\n"
"ds_read_b64_tr_b16 a[220:221], v10 offset:55296\n"
"ds_read_b64_tr_b16 a[222:223], v10 offset:55808\n"
"v_cvt_pk_bf16_f32 v184, v184, v185\n"
"v_cvt_pk_bf16_f32 v185, v186, v187\n"
"v_cvt_pk_bf16_f32 v186, v188, v189\n"
"v_cvt_pk_bf16_f32 v187, v190, v191\n"
"s_waitcnt lgkmcnt(4)\n"
"v_mfma_f32_32x32x16_bf16 v[32:47], a[208:211], v[176:179], v[32:47]\n"
"s_waitcnt vmcnt(12)\n"
"s_barrier\n"
"v_xor_b32 v9, 0x10000, v9\n"
"ds_read_b64_tr_b16 a[192:193], v9 offset:0\n"
"ds_read_b64_tr_b16 a[194:195], v9 offset:512\n"
"v_pk_max_i16 v184, v184, 0\n"
"v_pk_max_i16 v185, v185, 0\n"
"v_pk_max_i16 v186, v186, 0\n"
"v_pk_max_i16 v187, v187, 0\n"
"v_mfma_f32_32x32x16_bf16 v[48:63], a[212:215], v[176:179], v[48:63]\n"
"s_add_u32 m0, s68, 81920\n"
"s_mov_b32 s69, 57728\n"
"buffer_load_dwordx4 v1, s[44:47], s69 offen offset:0 lds\n"
"v_xor_b32 v10, 0x10000, v10\n"
"ds_read_b64_tr_b16 a[196:197], v10 offset:0\n"
"ds_read_b64_tr_b16 a[198:199], v10 offset:512\n"
"s_waitcnt lgkmcnt(4)\n"
"v_mfma_f32_32x32x16_bf16 v[32:47], a[216:219], v[184:187], v[32:47]\n"
"ds_read_b64_tr_b16 a[200:201], v9 offset:2048\n"
"ds_read_b64_tr_b16 a[202:203], v9 offset:2560\n"
"v_mfma_f32_32x32x16_bf16 v[48:63], a[220:223], v[184:187], v[48:63]\n"
"buffer_load_dwordx4 v2, s[44:47], s69 offen offset:1024 lds\n"
"ds_read_b64_tr_b16 a[204:205], v10 offset:2048\n"
"ds_read_b64_tr_b16 a[206:207], v10 offset:2560\n"
"s_waitcnt lgkmcnt(4)\n"
"v_mfma_f32_32x32x16_bf16 v[64:79], a[192:195], v[128:131], 0\n"
"ds_read_b64_tr_b16 a[208:209], v9 offset:4096\n"
"ds_read_b64_tr_b16 a[210:211], v9 offset:4608\n"
"v_mfma_f32_32x32x16_bf16 v[80:95], a[196:199], v[128:131], 0\n"
"buffer_load_dwordx4 v3, s[44:47], s69 offen offset:2048 lds\n"
"ds_read_b64_tr_b16 a[212:213], v10 offset:4096\n"
"ds_read_b64_tr_b16 a[214:215], v10 offset:4608\n"
"v_cvt_pk_bf16_f32 v32, v32, v33\n"
"s_waitcnt lgkmcnt(4)\n"
"v_mfma_f32_32x32x16_bf16 v[64:79], a[200:203], v[136:139], v[64:79]\n"
"ds_read_b64_tr_b16 a[216:217], v9 offset:6144\n"
"ds_read_b64_tr_b16 a[218:219], v9 offset:6656\n"
"v_cvt_pk_bf16_f32 v33, v34, v35\n"
"v_mfma_f32_32x32x16_bf16 v[80:95], a[204:207], v[136:139], v[80:95]\n"
"buffer_load_dwordx4 v4, s[44:47], s69 offen offset:3072 lds\n"
"ds_read_b64_tr_b16 a[220:221], v10 offset:6144\n"
"ds_read_b64_tr_b16 a[222:223], v10 offset:6656\n"
"v_cvt_pk_bf16_f32 v34, v36, v37\n"
"v_cvt_pk_bf16_f32 v35, v38, v39\n"
"s_waitcnt lgkmcnt(4)\n"
"v_mfma_f32_32x32x16_bf16 v[64:79], a[208:211], v[144:147], v[64:79]\n"
"ds_read_b64_tr_b16 a[192:193], v9 offset:16384\n"
"ds_read_b64_tr_b16 a[194:195], v9 offset:16896\n"
"ds_write_b128 v17, v[32:35] offset:0\n"
"v_mfma_f32_32x32x16_bf16 v[80:95], a[212:215], v[144:147], v[80:95]\n"
"s_add_u32 m0, s68, 98304\n"
"s_mov_b32 s69, 512\n"
"buffer_load_dwordx4 v1, s[44:47], s69 offen offset:0 lds\n"
"ds_read_b64_tr_b16 a[196:197], v10 offset:16384\n"
"ds_read_b64_tr_b16 a[198:199], v10 offset:16896\n"
"v_cvt_pk_bf16_f32 v40, v40, v41\n"
"s_waitcnt lgkmcnt(5)\n"
"v_mfma_f32_32x32x16_bf16 v[64:79], a[216:219], v[152:155], v[64:79]\n"
"ds_read_b64_tr_b16 a[200:201], v9 offset:18432\n"
"ds_read_b64_tr_b16 a[202:203], v9 offset:18944\n"
"v_cvt_pk_bf16_f32 v41, v42, v43\n"
"v_mfma_f32_32x32x16_bf16 v[80:95], a[220:223], v[152:155], v[80:95]\n"
"buffer_load_dwordx4 v2, s[44:47], s69 offen offset:1024 lds\n"
"ds_read_b64_tr_b16 a[204:205], v10 offset:18432\n"
"ds_read_b64_tr_b16 a[206:207], v10 offset:18944\n"
"v_cvt_pk_bf16_f32 v42, v44, v45\n"
"v_cvt_pk_bf16_f32 v43, v46, v47\n"
"s_waitcnt lgkmcnt(4)\n"
"v_mfma_f32_32x32x16_bf16 v[64:79], a[192:195], v[160:163], v[64:79]\n"
"ds_read_b64_tr_b16 a[208:209], v9 offset:20480\n"
"ds_read_b64_tr_b16 a[210:211], v9 offset:20992\n"
"ds_write_b128 v17, v[40:43] offset:1024\n"
"v_mfma_f32_32x32x16_bf16 v[80:95], a[196:199], v[160:163], v[80:95]\n"
"buffer_load_dwordx4 v3, s[44:47], s69 offen offset:2048 lds\n"
"ds_read_b64_tr_b16 a[212:213], v10 offset:20480\n"
"ds_read_b64_tr_b16 a[214:215], v10 offset:20992\n"
"s_waitcnt lgkmcnt(5)\n"
"v_mfma_f32_32x32x16_bf16 v[64:79], a[200:203], v[168:171], v[64:79]\n"
"ds_read_b64_tr_b16 a[216:217], v9 offset:22528\n"
"ds_read_b64_tr_b16 a[218:219], v9 offset:23040\n"
"v_mfma_f32_32x32x16_bf16 v[80:95], a[204:207], v[168:171], v[80:95]\n"
"buffer_load_dwordx4 v4, s[44:47], s69 offen offset:3072 lds\n"
"ds_read_b64_tr_b16 a[220:221], v10 offset:22528\n"
"ds_read_b64_tr_b16 a[222:223], v10 offset:23040\n"
"s_waitcnt lgkmcnt(4)\n"
"v_mfma_f32_32x32x16_bf16 v[64:79], a[208:211], v[176:179], v[64:79]\n"
"s_waitcnt vmcnt(12)\n"
"s_barrier\n"
"ds_read_b64_tr_b16 a[192:193], v9 offset:32768\n"
"ds_read_b64_tr_b16 a[194:195], v9 offset:33280\n"
"ds_read_b128 a[48:51], v24 offset:0\n"
"ds_read_b128 a[52:55], v24 offset:1024\n"
"v_mfma_f32_32x32x16_bf16 v[80:95], a[212:215], v[176:179], v[80:95]\n"
"s_add_u32 m0, s68, 114688\n"
"s_mov_b32 s69, 57856\n"
"buffer_load_dwordx4 v1, s[44:47], s69 offen offset:0 lds\n"
"ds_read_b64_tr_b16 a[196:197], v10 offset:32768\n"
"ds_read_b64_tr_b16 a[198:199], v10 offset:33280\n"
"s_waitcnt lgkmcnt(6)\n"
"v_mfma_f32_32x32x16_bf16 v[64:79], a[216:219], v[184:187], v[64:79]\n"
"ds_read_b64_tr_b16 a[200:201], v9 offset:34816\n"
"ds_read_b64_tr_b16 a[202:203], v9 offset:35328\n"
"v_mfma_f32_32x32x16_bf16 v[80:95], a[220:223], v[184:187], v[80:95]\n"
"buffer_load_dwordx4 v2, s[44:47], s69 offen offset:1024 lds\n"
"ds_read_b64_tr_b16 a[204:205], v10 offset:34816\n"
"ds_read_b64_tr_b16 a[206:207], v10 offset:35328\n"
"s_waitcnt lgkmcnt(7)\n"
"v_mfma_f32_32x32x16_bf16 v[48:63], a[240:243], a[48:51], v[48:63]\n"
"s_waitcnt lgkmcnt(6)\n"
"v_mfma_f32_32x32x16_bf16 v[48:63], a[244:247], a[52:55], v[48:63]\n"
"s_waitcnt lgkmcnt(4)\n"
"v_mfma_f32_32x32x16_bf16 v[32:47], a[192:195], v[128:131], 0\n"
"ds_read_b64_tr_b16 a[208:209], v9 offset:36864\n"
"ds_read_b64_tr_b16 a[210:211], v9 offset:37376\n"
"v_mfma_f32_32x32x16_bf16 v[192:207], a[196:199], v[128:131], 0\n"
"buffer_load_dwordx4 v3, s[44:47], s69 offen offset:2048 lds\n"
"ds_read_b64_tr_b16 a[212:213], v10 offset:36864\n"
"ds_read_b64_tr_b16 a[214:215], v10 offset:37376\n"
"v_cvt_pk_bf16_f32 v64, v64, v65\n"
"s_waitcnt lgkmcnt(4)\n"
"v_mfma_f32_32x32x16_bf16 v[32:47], a[200:203], v[136:139], v[32:47]\n"
"ds_read_b64_tr_b16 a[216:217], v9 offset:38912\n"
"ds_read_b64_tr_b16 a[218:219], v9 offset:39424\n"
"s_mov_b32 s74, 0\n"
"v_cvt_pk_bf16_f32 v65, v66, v67\n"
"v_mfma_f32_32x32x16_bf16 v[192:207], a[204:207], v[136:139], v[192:207]\n"
"buffer_load_dwordx4 v4, s[44:47], s69 offen offset:3072 lds\n"
"ds_read_b64_tr_b16 a[220:221], v10 offset:38912\n"
"ds_read_b64_tr_b16 a[222:223], v10 offset:39424\n"
"v_cvt_pk_bf16_f32 v48, v48, v49\n"
"v_cvt_pk_bf16_f32 v49, v50, v51\n"
"v_cvt_pk_bf16_f32 v66, v68, v69\n"
"v_cvt_pk_bf16_f32 v67, v70, v71\n"
"s_waitcnt lgkmcnt(4)\n"
"v_mfma_f32_32x32x16_bf16 v[32:47], a[208:211], v[144:147], v[32:47]\n"
"ds_read_b64_tr_b16 a[192:193], v9 offset:49152\n"
"ds_read_b64_tr_b16 a[194:195], v9 offset:49664\n"
"v_cvt_pk_bf16_f32 v50, v52, v53\n"
"ds_write_b128 v17, v[64:67] offset:2048\n"
"v_mfma_f32_32x32x16_bf16 v[192:207], a[212:215], v[144:147], v[192:207]\n"
"s_add_u32 m0, s68, 0\n"
"s_mov_b32 s69, 640\n"
"buffer_load_dwordx4 v1, s[44:47], s69 offen offset:0 lds\n"
"ds_read_b64_tr_b16 a[196:197], v10 offset:49152\n"
"ds_read_b64_tr_b16 a[198:199], v10 offset:49664\n"
"v_cvt_pk_bf16_f32 v51, v54, v55\n"
"v_cvt_pk_bf16_f32 v72, v72, v73\n"
"s_waitcnt lgkmcnt(5)\n"
"v_mfma_f32_32x32x16_bf16 v[32:47], a[216:219], v[152:155], v[32:47]\n"
"ds_read_b64_tr_b16 a[200:201], v9 offset:51200\n"
"ds_read_b64_tr_b16 a[202:203], v9 offset:51712\n"
"buffer_store_dwordx4 v[48:51], v23, s[64:67], s74 offen offset:0 sc1\n"
"v_cvt_pk_bf16_f32 v56, v56, v57\n"
"v_cvt_pk_bf16_f32 v73, v74, v75\n"
"v_mfma_f32_32x32x16_bf16 v[192:207], a[220:223], v[152:155], v[192:207]\n"
"buffer_load_dwordx4 v2, s[44:47], s69 offen offset:1024 lds\n"
"ds_read_b64_tr_b16 a[204:205], v10 offset:51200\n"
"ds_read_b64_tr_b16 a[206:207], v10 offset:51712\n"
"v_cvt_pk_bf16_f32 v57, v58, v59\n"
"v_cvt_pk_bf16_f32 v74, v76, v77\n"
"v_cvt_pk_bf16_f32 v75, v78, v79\n"
"s_waitcnt lgkmcnt(4)\n"
"v_mfma_f32_32x32x16_bf16 v[32:47], a[192:195], v[160:163], v[32:47]\n"
"ds_read_b64_tr_b16 a[208:209], v9 offset:53248\n"
"ds_read_b64_tr_b16 a[210:211], v9 offset:53760\n"
"v_cvt_pk_bf16_f32 v58, v60, v61\n"
"v_cvt_pk_bf16_f32 v59, v62, v63\n"
"ds_write_b128 v17, v[72:75] offset:3072\n"
"v_mfma_f32_32x32x16_bf16 v[192:207], a[196:199], v[160:163], v[192:207]\n"
"buffer_load_dwordx4 v3, s[44:47], s69 offen offset:2048 lds\n"
"ds_read_b64_tr_b16 a[212:213], v10 offset:53248\n"
"ds_read_b64_tr_b16 a[214:215], v10 offset:53760\n"
"buffer_store_dwordx4 v[56:59], v23, s[64:67], s74 offen offset:1024 sc1\n"
"s_waitcnt lgkmcnt(5)\n"
"v_mfma_f32_32x32x16_bf16 v[32:47], a[200:203], v[168:171], v[32:47]\n"
"ds_read_b64_tr_b16 a[216:217], v9 offset:55296\n"
"ds_read_b64_tr_b16 a[218:219], v9 offset:55808\n"
"v_mfma_f32_32x32x16_bf16 v[192:207], a[204:207], v[168:171], v[192:207]\n"
"buffer_load_dwordx4 v4, s[44:47], s69 offen offset:3072 lds\n"
"ds_read_b64_tr_b16 a[220:221], v10 offset:55296\n"
"ds_read_b64_tr_b16 a[222:223], v10 offset:55808\n"
"s_waitcnt lgkmcnt(4)\n"
"v_mfma_f32_32x32x16_bf16 v[32:47], a[208:211], v[176:179], v[32:47]\n"
"s_waitcnt vmcnt(14)\n"
"s_barrier\n"
"v_xor_b32 v9, 0x10000, v9\n"
"ds_read_b64_tr_b16 a[192:193], v9 offset:0\n"
"ds_read_b64_tr_b16 a[194:195], v9 offset:512\n"
"ds_read_b128 a[48:51], v24 offset:2048\n"
"ds_read_b128 a[52:55], v24 offset:3072\n"
"v_mfma_f32_32x32x16_bf16 v[192:207], a[212:215], v[176:179], v[192:207]\n"
"s_add_u32 m0, s68, 16384\n"
"s_mov_b32 s69, 57984\n"
"buffer_load_dwordx4 v1, s[44:47], s69 offen offset:0 lds\n"
"v_xor_b32 v10, 0x10000, v10\n"
"ds_read_b64_tr_b16 a[196:197], v10 offset:0\n"
"ds_read_b64_tr_b16 a[198:199], v10 offset:512\n"
"s_waitcnt lgkmcnt(6)\n"
"v_mfma_f32_32x32x16_bf16 v[32:47], a[216:219], v[184:187], v[32:47]\n"
"ds_read_b64_tr_b16 a[200:201], v9 offset:2048\n"
"ds_read_b64_tr_b16 a[202:203], v9 offset:2560\n"
"v_mfma_f32_32x32x16_bf16 v[192:207], a[220:223], v[184:187], v[192:207]\n"
"buffer_load_dwordx4 v2, s[44:47], s69 offen offset:1024 lds\n"
"ds_read_b64_tr_b16 a[204:205], v10 offset:2048\n"
"ds_read_b64_tr_b16 a[206:207], v10 offset:2560\n"
"s_waitcnt lgkmcnt(7)\n"
"v_mfma_f32_32x32x16_bf16 v[80:95], a[240:243], a[48:51], v[80:95]\n"
"s_waitcnt lgkmcnt(6)\n"
"v_mfma_f32_32x32x16_bf16 v[80:95], a[244:247], a[52:55], v[80:95]\n"
"s_waitcnt lgkmcnt(4)\n"
"v_mfma_f32_32x32x16_bf16 v[64:79], a[192:195], v[128:131], 0\n"
"ds_read_b64_tr_b16 a[208:209], v9 offset:4096\n"
"ds_read_b64_tr_b16 a[210:211], v9 offset:4608\n"
"v_mfma_f32_32x32x16_bf16 v[48:63], a[196:199], v[128:131], 0\n"
"buffer_load_dwordx4 v3, s[44:47], s69 offen offset:2048 lds\n"
"ds_read_b64_tr_b16 a[212:213], v10 offset:4096\n"
"ds_read_b64_tr_b16 a[214:215], v10 offset:4608\n"
"v_cvt_pk_bf16_f32 v32, v32, v33\n"
"s_waitcnt lgkmcnt(4)\n"
"v_mfma_f32_32x32x16_bf16 v[64:79], a[200:203], v[136:139], v[64:79]\n"
"ds_read_b64_tr_b16 a[216:217], v9 offset:6144\n"
"ds_read_b64_tr_b16 a[218:219], v9 offset:6656\n"
"s_mov_b32 s74, 4096\n"
"v_cvt_pk_bf16_f32 v33, v34, v35\n"
"v_mfma_f32_32x32x16_bf16 v[48:63], a[204:207], v[136:139], v[48:63]\n"
"buffer_load_dwordx4 v4, s[44:47], s69 offen offset:3072 lds\n"
"ds_read_b64_tr_b16 a[220:221], v10 offset:6144\n"
"ds_read_b64_tr_b16 a[222:223], v10 offset:6656\n"
"v_cvt_pk_bf16_f32 v80, v80, v81\n"
"v_cvt_pk_bf16_f32 v81, v82, v83\n"
"v_cvt_pk_bf16_f32 v34, v36, v37\n"
"v_cvt_pk_bf16_f32 v35, v38, v39\n"
"s_waitcnt lgkmcnt(4)\n"
"v_mfma_f32_32x32x16_bf16 v[64:79], a[208:211], v[144:147], v[64:79]\n"
"ds_read_b64_tr_b16 a[192:193], v9 offset:16384\n"
"ds_read_b64_tr_b16 a[194:195], v9 offset:16896\n"
"v_cvt_pk_bf16_f32 v82, v84, v85\n"
"ds_write_b128 v17, v[32:35] offset:0\n"
"v_mfma_f32_32x32x16_bf16 v[48:63], a[212:215], v[144:147], v[48:63]\n"
"s_add_u32 m0, s68, 32768\n"
"s_mov_b32 s69, 768\n"
"buffer_load_dwordx4 v1, s[44:47], s69 offen offset:0 lds\n"
"ds_read_b64_tr_b16 a[196:197], v10 offset:16384\n"
"ds_read_b64_tr_b16 a[198:199], v10 offset:16896\n"
"v_cvt_pk_bf16_f32 v83, v86, v87\n"
"v_cvt_pk_bf16_f32 v40, v40, v41\n"
"s_waitcnt lgkmcnt(5)\n"
"v_mfma_f32_32x32x16_bf16 v[64:79], a[216:219], v[152:155], v[64:79]\n"
"ds_read_b64_tr_b16 a[200:201], v9 offset:18432\n"
"ds_read_b64_tr_b16 a[202:203], v9 offset:18944\n"
"buffer_store_dwordx4 v[80:83], v23, s[64:67], s74 offen offset:0 sc1\n"
"v_cvt_pk_bf16_f32 v88, v88, v89\n"
"v_cvt_pk_bf16_f32 v41, v42, v43\n"
"v_mfma_f32_32x32x16_bf16 v[48:63], a[220:223], v[152:155], v[48:63]\n"
"buffer_load_dwordx4 v2, s[44:47], s69 offen offset:1024 lds\n"
"ds_read_b64_tr_b16 a[204:205], v10 offset:18432\n"
"ds_read_b64_tr_b16 a[206:207], v10 offset:18944\n"
"v_cvt_pk_bf16_f32 v89, v90, v91\n"
"v_cvt_pk_bf16_f32 v42, v44, v45\n"
"v_cvt_pk_bf16_f32 v43, v46, v47\n"
"s_waitcnt lgkmcnt(4)\n"
"v_mfma_f32_32x32x16_bf16 v[64:79], a[192:195], v[160:163], v[64:79]\n"
"ds_read_b64_tr_b16 a[208:209], v9 offset:20480\n"
"ds_read_b64_tr_b16 a[210:211], v9 offset:20992\n"
"v_cvt_pk_bf16_f32 v90, v92, v93\n"
"v_cvt_pk_bf16_f32 v91, v94, v95\n"
"ds_write_b128 v17, v[40:43] offset:1024\n"
"v_mfma_f32_32x32x16_bf16 v[48:63], a[196:199], v[160:163], v[48:63]\n"
"buffer_load_dwordx4 v3, s[44:47], s69 offen offset:2048 lds\n"
"ds_read_b64_tr_b16 a[212:213], v10 offset:20480\n"
"ds_read_b64_tr_b16 a[214:215], v10 offset:20992\n"
"buffer_store_dwordx4 v[88:91], v23, s[64:67], s74 offen offset:1024 sc1\n"
"s_waitcnt lgkmcnt(5)\n"
"v_mfma_f32_32x32x16_bf16 v[64:79], a[200:203], v[168:171], v[64:79]\n"
"ds_read_b64_tr_b16 a[216:217], v9 offset:22528\n"
"ds_read_b64_tr_b16 a[218:219], v9 offset:23040\n"
"v_mfma_f32_32x32x16_bf16 v[48:63], a[204:207], v[168:171], v[48:63]\n"
"buffer_load_dwordx4 v4, s[44:47], s69 offen offset:3072 lds\n"
"ds_read_b64_tr_b16 a[220:221], v10 offset:22528\n"
"ds_read_b64_tr_b16 a[222:223], v10 offset:23040\n"
"s_waitcnt lgkmcnt(4)\n"
"v_mfma_f32_32x32x16_bf16 v[64:79], a[208:211], v[176:179], v[64:79]\n"
"s_waitcnt vmcnt(16)\n"
"s_barrier\n"
"ds_read_b64_tr_b16 a[192:193], v9 offset:32768\n"
"ds_read_b64_tr_b16 a[194:195], v9 offset:33280\n"
"ds_read_b128 a[48:51], v24 offset:0\n"
"ds_read_b128 a[52:55], v24 offset:1024\n"
"v_mfma_f32_32x32x16_bf16 v[48:63], a[212:215], v[176:179], v[48:63]\n"
"s_add_u32 m0, s68, 49152\n"
"s_mov_b32 s69, 58112\n"
"buffer_load_dwordx4 v1, s[44:47], s69 offen offset:0 lds\n"
"ds_read_b64_tr_b16 a[196:197], v10 offset:32768\n"
"ds_read_b64_tr_b16 a[198:199], v10 offset:33280\n"
"s_waitcnt lgkmcnt(6)\n"
"v_mfma_f32_32x32x16_bf16 v[64:79], a[216:219], v[184:187], v[64:79]\n"
"ds_read_b64_tr_b16 a[200:201], v9 offset:34816\n"
"ds_read_b64_tr_b16 a[202:203], v9 offset:35328\n"
"v_mfma_f32_32x32x16_bf16 v[48:63], a[220:223], v[184:187], v[48:63]\n"
"buffer_load_dwordx4 v2, s[44:47], s69 offen offset:1024 lds\n"
"ds_read_b64_tr_b16 a[204:205], v10 offset:34816\n"
"ds_read_b64_tr_b16 a[206:207], v10 offset:35328\n"
"s_waitcnt lgkmcnt(7)\n"
"v_mfma_f32_32x32x16_bf16 v[192:207], a[240:243], a[48:51], v[192:207]\n"
"s_waitcnt lgkmcnt(6)\n"
"v_mfma_f32_32x32x16_bf16 v[192:207], a[244:247], a[52:55], v[192:207]\n"
"s_waitcnt lgkmcnt(4)\n"
"v_mfma_f32_32x32x16_bf16 v[32:47], a[192:195], v[128:131], 0\n"
"ds_read_b64_tr_b16 a[208:209], v9 offset:36864\n"
"ds_read_b64_tr_b16 a[210:211], v9 offset:37376\n"
"v_mfma_f32_32x32x16_bf16 v[80:95], a[196:199], v[128:131], 0\n"
"buffer_load_dwordx4 v3, s[44:47], s69 offen offset:2048 lds\n"
"ds_read_b64_tr_b16 a[212:213], v10 offset:36864\n"
"ds_read_b64_tr_b16 a[214:215], v10 offset:37376\n"
"v_cvt_pk_bf16_f32 v64, v64, v65\n"
"s_waitcnt lgkmcnt(4)\n"
"v_mfma_f32_32x32x16_bf16 v[32:47], a[200:203], v[136:139], v[32:47]\n"
"ds_read_b64_tr_b16 a[216:217], v9 offset:38912\n"
"ds_read_b64_tr_b16 a[218:219], v9 offset:39424\n"
"s_mov_b32 s74, 8192\n"
"v_cvt_pk_bf16_f32 v65, v66, v67\n"
"v_mfma_f32_32x32x16_bf16 v[80:95], a[204:207], v[136:139], v[80:95]\n"
"buffer_load_dwordx4 v4, s[44:47], s69 offen offset:3072 lds\n"
"ds_read_b64_tr_b16 a[220:221], v10 offset:38912\n"
"ds_read_b64_tr_b16 a[222:223], v10 offset:39424\n"
"v_cvt_pk_bf16_f32 v192, v192, v193\n"
"v_cvt_pk_bf16_f32 v193, v194, v195\n"
"v_cvt_pk_bf16_f32 v66, v68, v69\n"
"v_cvt_pk_bf16_f32 v67, v70, v71\n"
"s_waitcnt lgkmcnt(4)\n"
"v_mfma_f32_32x32x16_bf16 v[32:47], a[208:211], v[144:147], v[32:47]\n"
"ds_read_b64_tr_b16 a[192:193], v9 offset:49152\n"
"ds_read_b64_tr_b16 a[194:195], v9 offset:49664\n"
"v_cvt_pk_bf16_f32 v194, v196, v197\n"
"ds_write_b128 v17, v[64:67] offset:2048\n"
"v_mfma_f32_32x32x16_bf16 v[80:95], a[212:215], v[144:147], v[80:95]\n"
"ds_read_b64_tr_b16 a[196:197], v10 offset:49152\n"
"ds_read_b64_tr_b16 a[198:199], v10 offset:49664\n"
"v_cvt_pk_bf16_f32 v195, v198, v199\n"
"v_cvt_pk_bf16_f32 v72, v72, v73\n"
"s_waitcnt lgkmcnt(5)\n"
"v_mfma_f32_32x32x16_bf16 v[32:47], a[216:219], v[152:155], v[32:47]\n"
"ds_read_b64_tr_b16 a[200:201], v9 offset:51200\n"
"ds_read_b64_tr_b16 a[202:203], v9 offset:51712\n"
"buffer_store_dwordx4 v[192:195], v23, s[64:67], s74 offen offset:0 sc1\n"
"v_cvt_pk_bf16_f32 v200, v200, v201\n"
"v_cvt_pk_bf16_f32 v73, v74, v75\n"
"v_mfma_f32_32x32x16_bf16 v[80:95], a[220:223], v[152:155], v[80:95]\n"
"ds_read_b64_tr_b16 a[204:205], v10 offset:51200\n"
"ds_read_b64_tr_b16 a[206:207], v10 offset:51712\n"
"v_cvt_pk_bf16_f32 v201, v202, v203\n"
"v_cvt_pk_bf16_f32 v74, v76, v77\n"
"v_cvt_pk_bf16_f32 v75, v78, v79\n"
"s_waitcnt lgkmcnt(4)\n"
"v_mfma_f32_32x32x16_bf16 v[32:47], a[192:195], v[160:163], v[32:47]\n"
"ds_read_b64_tr_b16 a[208:209], v9 offset:53248\n"
"ds_read_b64_tr_b16 a[210:211], v9 offset:53760\n"
"v_cvt_pk_bf16_f32 v202, v204, v205\n"
"v_cvt_pk_bf16_f32 v203, v206, v207\n"
"ds_write_b128 v17, v[72:75] offset:3072\n"
"v_mfma_f32_32x32x16_bf16 v[80:95], a[196:199], v[160:163], v[80:95]\n"
"ds_read_b64_tr_b16 a[212:213], v10 offset:53248\n"
"ds_read_b64_tr_b16 a[214:215], v10 offset:53760\n"
"buffer_store_dwordx4 v[200:203], v23, s[64:67], s74 offen offset:1024 sc1\n"
"s_waitcnt lgkmcnt(5)\n"
"v_mfma_f32_32x32x16_bf16 v[32:47], a[200:203], v[168:171], v[32:47]\n"
"ds_read_b64_tr_b16 a[216:217], v9 offset:55296\n"
"ds_read_b64_tr_b16 a[218:219], v9 offset:55808\n"
"v_mfma_f32_32x32x16_bf16 v[80:95], a[204:207], v[168:171], v[80:95]\n"
"ds_read_b64_tr_b16 a[220:221], v10 offset:55296\n"
"ds_read_b64_tr_b16 a[222:223], v10 offset:55808\n"
"s_waitcnt lgkmcnt(4)\n"
"v_mfma_f32_32x32x16_bf16 v[32:47], a[208:211], v[176:179], v[32:47]\n"
"s_waitcnt vmcnt(12)\n"
"s_barrier\n"
"v_xor_b32 v9, 0x10000, v9\n"
"ds_read_b64_tr_b16 a[192:193], v9 offset:0\n"
"ds_read_b64_tr_b16 a[194:195], v9 offset:512\n"
"ds_read_b128 a[48:51], v24 offset:2048\n"
"ds_read_b128 a[52:55], v24 offset:3072\n"
"v_mfma_f32_32x32x16_bf16 v[80:95], a[212:215], v[176:179], v[80:95]\n"
"v_xor_b32 v10, 0x10000, v10\n"
"ds_read_b64_tr_b16 a[196:197], v10 offset:0\n"
"ds_read_b64_tr_b16 a[198:199], v10 offset:512\n"
"s_waitcnt lgkmcnt(6)\n"
"v_mfma_f32_32x32x16_bf16 v[32:47], a[216:219], v[184:187], v[32:47]\n"
"ds_read_b64_tr_b16 a[200:201], v9 offset:2048\n"
"ds_read_b64_tr_b16 a[202:203], v9 offset:2560\n"
"v_mfma_f32_32x32x16_bf16 v[80:95], a[220:223], v[184:187], v[80:95]\n"
"ds_read_b64_tr_b16 a[204:205], v10 offset:2048\n"
"ds_read_b64_tr_b16 a[206:207], v10 offset:2560\n"
"s_waitcnt lgkmcnt(7)\n"
"v_mfma_f32_32x32x16_bf16 v[48:63], a[240:243], a[48:51], v[48:63]\n"
"s_waitcnt lgkmcnt(6)\n"
"v_mfma_f32_32x32x16_bf16 v[48:63], a[244:247], a[52:55], v[48:63]\n"
"s_waitcnt lgkmcnt(4)\n"
"v_mfma_f32_32x32x16_bf16 v[64:79], a[192:195], v[128:131], 0\n"
"ds_read_b64_tr_b16 a[208:209], v9 offset:4096\n"
"ds_read_b64_tr_b16 a[210:211], v9 offset:4608\n"
"v_mfma_f32_32x32x16_bf16 v[192:207], a[196:199], v[128:131], 0\n"
"ds_read_b64_tr_b16 a[212:213], v10 offset:4096\n"
"ds_read_b64_tr_b16 a[214:215], v10 offset:4608\n"
"v_cvt_pk_bf16_f32 v32, v32, v33\n"
"s_waitcnt lgkmcnt(4)\n"
"v_mfma_f32_32x32x16_bf16 v[64:79], a[200:203], v[136:139], v[64:79]\n"
"ds_read_b64_tr_b16 a[216:217], v9 offset:6144\n"
"ds_read_b64_tr_b16 a[218:219], v9 offset:6656\n"
"s_mov_b32 s74, 14692352\n"
"v_cvt_pk_bf16_f32 v33, v34, v35\n"
"v_mfma_f32_32x32x16_bf16 v[192:207], a[204:207], v[136:139], v[192:207]\n"
"ds_read_b64_tr_b16 a[220:221], v10 offset:6144\n"
"ds_read_b64_tr_b16 a[222:223], v10 offset:6656\n"
"v_cvt_pk_bf16_f32 v48, v48, v49\n"
"v_cvt_pk_bf16_f32 v49, v50, v51\n"
"v_cvt_pk_bf16_f32 v34, v36, v37\n"
"v_cvt_pk_bf16_f32 v35, v38, v39\n"
"s_waitcnt lgkmcnt(4)\n"
"v_mfma_f32_32x32x16_bf16 v[64:79], a[208:211], v[144:147], v[64:79]\n"
"ds_read_b64_tr_b16 a[192:193], v9 offset:16384\n"
"ds_read_b64_tr_b16 a[194:195], v9 offset:16896\n"
"v_cvt_pk_bf16_f32 v50, v52, v53\n"
"ds_write_b128 v17, v[32:35] offset:0\n"
"v_mfma_f32_32x32x16_bf16 v[192:207], a[212:215], v[144:147], v[192:207]\n"
"ds_read_b64_tr_b16 a[196:197], v10 offset:16384\n"
"ds_read_b64_tr_b16 a[198:199], v10 offset:16896\n"
"v_cvt_pk_bf16_f32 v51, v54, v55\n"
"v_cvt_pk_bf16_f32 v40, v40, v41\n"
"s_waitcnt lgkmcnt(5)\n"
"v_mfma_f32_32x32x16_bf16 v[64:79], a[216:219], v[152:155], v[64:79]\n"
"ds_read_b64_tr_b16 a[200:201], v9 offset:18432\n"
"ds_read_b64_tr_b16 a[202:203], v9 offset:18944\n"
"buffer_store_dwordx4 v[48:51], v23, s[64:67], s74 offen offset:0 sc1\n"
"v_cvt_pk_bf16_f32 v56, v56, v57\n"
"v_cvt_pk_bf16_f32 v41, v42, v43\n"
"v_mfma_f32_32x32x16_bf16 v[192:207], a[220:223], v[152:155], v[192:207]\n"
"ds_read_b64_tr_b16 a[204:205], v10 offset:18432\n"
"ds_read_b64_tr_b16 a[206:207], v10 offset:18944\n"
"v_cvt_pk_bf16_f32 v57, v58, v59\n"
"v_cvt_pk_bf16_f32 v42, v44, v45\n"
"v_cvt_pk_bf16_f32 v43, v46, v47\n"
"s_waitcnt lgkmcnt(4)\n"
"v_mfma_f32_32x32x16_bf16 v[64:79], a[192:195], v[160:163], v[64:79]\n"
"ds_read_b64_tr_b16 a[208:209], v9 offset:20480\n"
"ds_read_b64_tr_b16 a[210:211], v9 offset:20992\n"
"v_cvt_pk_bf16_f32 v58, v60, v61\n"
"v_cvt_pk_bf16_f32 v59, v62, v63\n"
"ds_write_b128 v17, v[40:43] offset:1024\n"
"v_mfma_f32_32x32x16_bf16 v[192:207], a[196:199], v[160:163], v[192:207]\n"
"ds_read_b64_tr_b16 a[212:213], v10 offset:20480\n"
"ds_read_b64_tr_b16 a[214:215], v10 offset:20992\n"
"buffer_store_dwordx4 v[56:59], v23, s[64:67], s74 offen offset:1024 sc1\n"
"s_waitcnt lgkmcnt(5)\n"
"v_mfma_f32_32x32x16_bf16 v[64:79], a[200:203], v[168:171], v[64:79]\n"
"ds_read_b64_tr_b16 a[216:217], v9 offset:22528\n"
"ds_read_b64_tr_b16 a[218:219], v9 offset:23040\n"
"v_mfma_f32_32x32x16_bf16 v[192:207], a[204:207], v[168:171], v[192:207]\n"
"ds_read_b64_tr_b16 a[220:221], v10 offset:22528\n"
"ds_read_b64_tr_b16 a[222:223], v10 offset:23040\n"
"s_waitcnt lgkmcnt(4)\n"
"v_mfma_f32_32x32x16_bf16 v[64:79], a[208:211], v[176:179], v[64:79]\n"
"s_waitcnt vmcnt(8)\n"
"s_barrier\n"
"ds_read_b64_tr_b16 a[192:193], v9 offset:32768\n"
"ds_read_b64_tr_b16 a[194:195], v9 offset:33280\n"
"ds_read_b128 a[48:51], v24 offset:0\n"
"ds_read_b128 a[52:55], v24 offset:1024\n"
"v_mfma_f32_32x32x16_bf16 v[192:207], a[212:215], v[176:179], v[192:207]\n"
"ds_read_b64_tr_b16 a[196:197], v10 offset:32768\n"
"ds_read_b64_tr_b16 a[198:199], v10 offset:33280\n"
"s_waitcnt lgkmcnt(6)\n"
"v_mfma_f32_32x32x16_bf16 v[64:79], a[216:219], v[184:187], v[64:79]\n"
"ds_read_b64_tr_b16 a[200:201], v9 offset:34816\n"
"ds_read_b64_tr_b16 a[202:203], v9 offset:35328\n"
"v_mfma_f32_32x32x16_bf16 v[192:207], a[220:223], v[184:187], v[192:207]\n"
"ds_read_b64_tr_b16 a[204:205], v10 offset:34816\n"
"ds_read_b64_tr_b16 a[206:207], v10 offset:35328\n"
"s_waitcnt lgkmcnt(7)\n"
"v_mfma_f32_32x32x16_bf16 v[80:95], a[240:243], a[48:51], v[80:95]\n"
"s_waitcnt lgkmcnt(6)\n"
"v_mfma_f32_32x32x16_bf16 v[80:95], a[244:247], a[52:55], v[80:95]\n"
"s_waitcnt lgkmcnt(4)\n"
"v_mfma_f32_32x32x16_bf16 v[32:47], a[192:195], v[128:131], 0\n"
"ds_read_b64_tr_b16 a[208:209], v9 offset:36864\n"
"ds_read_b64_tr_b16 a[210:211], v9 offset:37376\n"
"v_mfma_f32_32x32x16_bf16 v[48:63], a[196:199], v[128:131], 0\n"
"ds_read_b64_tr_b16 a[212:213], v10 offset:36864\n"
"ds_read_b64_tr_b16 a[214:215], v10 offset:37376\n"
"v_cvt_pk_bf16_f32 v64, v64, v65\n"
"s_waitcnt lgkmcnt(4)\n"
"v_mfma_f32_32x32x16_bf16 v[32:47], a[200:203], v[136:139], v[32:47]\n"
"ds_read_b64_tr_b16 a[216:217], v9 offset:38912\n"
"ds_read_b64_tr_b16 a[218:219], v9 offset:39424\n"
"s_mov_b32 s74, 14696448\n"
"v_cvt_pk_bf16_f32 v65, v66, v67\n"
"v_mfma_f32_32x32x16_bf16 v[48:63], a[204:207], v[136:139], v[48:63]\n"
"ds_read_b64_tr_b16 a[220:221], v10 offset:38912\n"
"ds_read_b64_tr_b16 a[222:223], v10 offset:39424\n"
"v_cvt_pk_bf16_f32 v80, v80, v81\n"
"v_cvt_pk_bf16_f32 v81, v82, v83\n"
"v_cvt_pk_bf16_f32 v66, v68, v69\n"
"v_cvt_pk_bf16_f32 v67, v70, v71\n"
"s_waitcnt lgkmcnt(4)\n"
"v_mfma_f32_32x32x16_bf16 v[32:47], a[208:211], v[144:147], v[32:47]\n"
"s_waitcnt vmcnt(4)\n"
"s_barrier\n"
"ds_read_b64_tr_b16 a[192:193], v9 offset:49152\n"
"ds_read_b64_tr_b16 a[194:195], v9 offset:49664\n"
"v_cvt_pk_bf16_f32 v82, v84, v85\n"
"ds_write_b128 v17, v[64:67] offset:2048\n"
"v_mfma_f32_32x32x16_bf16 v[48:63], a[212:215], v[144:147], v[48:63]\n"
"ds_read_b64_tr_b16 a[196:197], v10 offset:49152\n"
"ds_read_b64_tr_b16 a[198:199], v10 offset:49664\n"
"v_cvt_pk_bf16_f32 v83, v86, v87\n"
"v_cvt_pk_bf16_f32 v72, v72, v73\n"
"s_waitcnt lgkmcnt(5)\n"
"v_mfma_f32_32x32x16_bf16 v[32:47], a[216:219], v[152:155], v[32:47]\n"
"ds_read_b64_tr_b16 a[200:201], v9 offset:51200\n"
"ds_read_b64_tr_b16 a[202:203], v9 offset:51712\n"
"buffer_store_dwordx4 v[80:83], v23, s[64:67], s74 offen offset:0 sc1\n"
"v_cvt_pk_bf16_f32 v88, v88, v89\n"
"v_cvt_pk_bf16_f32 v73, v74, v75\n"
"v_mfma_f32_32x32x16_bf16 v[48:63], a[220:223], v[152:155], v[48:63]\n"
"ds_read_b64_tr_b16 a[204:205], v10 offset:51200\n"
"ds_read_b64_tr_b16 a[206:207], v10 offset:51712\n"
"v_cvt_pk_bf16_f32 v89, v90, v91\n"
"v_cvt_pk_bf16_f32 v74, v76, v77\n"
"v_cvt_pk_bf16_f32 v75, v78, v79\n"
"s_waitcnt lgkmcnt(4)\n"
"v_mfma_f32_32x32x16_bf16 v[32:47], a[192:195], v[160:163], v[32:47]\n"
"ds_read_b64_tr_b16 a[208:209], v9 offset:53248\n"
"ds_read_b64_tr_b16 a[210:211], v9 offset:53760\n"
"v_cvt_pk_bf16_f32 v90, v92, v93\n"
"v_cvt_pk_bf16_f32 v91, v94, v95\n"
"ds_write_b128 v17, v[72:75] offset:3072\n"
"s_waitcnt vmcnt(3)\n"
"s_mov_b32 s75, 69632\n"
"buffer_store_dword v11, v12, s[60:63], s75 offen sc1\n"
"v_mfma_f32_32x32x16_bf16 v[48:63], a[196:199], v[160:163], v[48:63]\n"
"ds_read_b64_tr_b16 a[212:213], v10 offset:53248\n"
"ds_read_b64_tr_b16 a[214:215], v10 offset:53760\n"
"buffer_store_dwordx4 v[88:91], v23, s[64:67], s74 offen offset:1024 sc1\n"
"s_waitcnt lgkmcnt(5)\n"
"v_mfma_f32_32x32x16_bf16 v[32:47], a[200:203], v[168:171], v[32:47]\n"
"ds_read_b64_tr_b16 a[216:217], v9 offset:55296\n"
"ds_read_b64_tr_b16 a[218:219], v9 offset:55808\n"
"v_mfma_f32_32x32x16_bf16 v[48:63], a[204:207], v[168:171], v[48:63]\n"
"ds_read_b64_tr_b16 a[220:221], v10 offset:55296\n"
"ds_read_b64_tr_b16 a[222:223], v10 offset:55808\n"
"s_waitcnt lgkmcnt(4)\n"
"v_mfma_f32_32x32x16_bf16 v[32:47], a[208:211], v[176:179], v[32:47]\n"
"s_barrier\n"
"ds_read_b128 a[48:51], v24 offset:2048\n"
"ds_read_b128 a[52:55], v24 offset:3072\n"
"v_mfma_f32_32x32x16_bf16 v[48:63], a[212:215], v[176:179], v[48:63]\n"
"s_waitcnt lgkmcnt(2)\n"
"v_mfma_f32_32x32x16_bf16 v[32:47], a[216:219], v[184:187], v[32:47]\n"
"v_mfma_f32_32x32x16_bf16 v[48:63], a[220:223], v[184:187], v[48:63]\n"
"s_mov_b32 s75, 69632\n"
"buffer_load_dword v31, v13, s[60:63], s75 offen sc1\n"
"s_waitcnt lgkmcnt(1)\n"
"v_mfma_f32_32x32x16_bf16 v[192:207], a[240:243], a[48:51], v[192:207]\n"
"s_waitcnt lgkmcnt(0)\n"
"v_mfma_f32_32x32x16_bf16 v[192:207], a[244:247], a[52:55], v[192:207]\n"
"v_cvt_pk_bf16_f32 v32, v32, v33\n"
"v_cvt_pk_bf16_f32 v33, v34, v35\n"
"v_cvt_pk_bf16_f32 v34, v36, v37\n"
"v_cvt_pk_bf16_f32 v35, v38, v39\n"
"ds_write_b128 v17, v[32:35] offset:0\n"
"v_cvt_pk_bf16_f32 v40, v40, v41\n"
"v_cvt_pk_bf16_f32 v41, v42, v43\n"
"v_cvt_pk_bf16_f32 v42, v44, v45\n"
"v_cvt_pk_bf16_f32 v43, v46, v47\n"
"ds_write_b128 v17, v[40:43] offset:1024\n"
"s_waitcnt lgkmcnt(0)\n"
"s_barrier\n"
"ds_read_b128 a[48:51], v24 offset:0\n"
"ds_read_b128 a[52:55], v24 offset:1024\n"
"s_mov_b32 s74, 14700544\n"
"v_cvt_pk_bf16_f32 v192, v192, v193\n"
"v_cvt_pk_bf16_f32 v193, v194, v195\n"
"v_cvt_pk_bf16_f32 v194, v196, v197\n"
"v_cvt_pk_bf16_f32 v195, v198, v199\n"
"buffer_store_dwordx4 v[192:195], v23, s[64:67], s74 offen offset:0 sc1\n"
"v_cvt_pk_bf16_f32 v200, v200, v201\n"
"v_cvt_pk_bf16_f32 v201, v202, v203\n"
"v_cvt_pk_bf16_f32 v202, v204, v205\n"
"v_cvt_pk_bf16_f32 v203, v206, v207\n"
"buffer_store_dwordx4 v[200:203], v23, s[64:67], s74 offen offset:1024 sc1\n"
"s_waitcnt lgkmcnt(1)\n"
"v_mfma_f32_32x32x16_bf16 v[48:63], a[240:243], a[48:51], v[48:63]\n"
"s_waitcnt lgkmcnt(0)\n"
"v_mfma_f32_32x32x16_bf16 v[48:63], a[244:247], a[52:55], v[48:63]\n"
"s_mov_b32 s74, 14704640\n"
"s_nop 10\n"
"v_cvt_pk_bf16_f32 v48, v48, v49\n"
"v_cvt_pk_bf16_f32 v49, v50, v51\n"
"v_cvt_pk_bf16_f32 v50, v52, v53\n"
"v_cvt_pk_bf16_f32 v51, v54, v55\n"
"buffer_store_dwordx4 v[48:51], v23, s[64:67], s74 offen offset:0 sc1\n"
"v_cvt_pk_bf16_f32 v56, v56, v57\n"
"v_cvt_pk_bf16_f32 v57, v58, v59\n"
"v_cvt_pk_bf16_f32 v58, v60, v61\n"
"v_cvt_pk_bf16_f32 v59, v62, v63\n"
"buffer_store_dwordx4 v[56:59], v23, s[64:67], s74 offen offset:1024 sc1\n"
".Lr2_%=:\n"
"s_waitcnt vmcnt(4)\n"
"v_cmp_ne_u32 vcc, s72, v31\n"
"s_cmp_eq_u64 vcc, 0\n"
"s_cbranch_scc1 .Lok9_%=\n"
"s_movk_i32 s84, 0x7000\n"
".Lsp9_%=:\n"
"s_mov_b32 s75, 69632\n"
"buffer_load_dword v31, v13, s[60:63], s75 offen sc1\n"
"s_waitcnt vmcnt(0)\n"
"v_cmp_ne_u32 vcc, s72, v31\n"
"s_cmp_eq_u64 vcc, 0\n"
"s_cbranch_scc1 .Lok9_%=\n"
"s_sub_u32 s84, s84, 1\n"
"s_cmp_lg_u32 s84, 0\n"
"s_cbranch_scc1 .Lsp9_%=\n"
".Lok9_%=:\n"
"s_mov_b32 s70, 0\n"
"buffer_load_dwordx4 a[64:67], v16, s[76:79], s70 offen sc1\n"
"s_mov_b32 s70, 7340032\n"
"buffer_load_dwordx4 a[68:71], v16, s[76:79], s70 offen sc1\n"
"s_mov_b32 s70, 4096\n"
"buffer_load_dwordx4 a[72:75], v16, s[76:79], s70 offen sc1\n"
"s_mov_b32 s70, 7344128\n"
"buffer_load_dwordx4 a[76:79], v16, s[76:79], s70 offen sc1\n"
"s_mov_b32 s70, 8192\n"
"buffer_load_dwordx4 a[80:83], v16, s[76:79], s70 offen sc1\n"
"s_mov_b32 s70, 7348224\n"
"buffer_load_dwordx4 a[84:87], v16, s[76:79], s70 offen sc1\n"
"s_add_u32 s76, s76, 14680064\n"
"s_addc_u32 s77, s77, 0\n"
"s_mov_b32 s70, 12288\n"
"buffer_load_dwordx4 v[136:139], v16, s[76:79], s70 offen sc1\n"
"s_mov_b32 s70, 7352320\n"
"buffer_load_dwordx4 v[140:143], v16, s[76:79], s70 offen sc1\n"
"s_mov_b32 s70, 16384\n"
"buffer_load_dwordx4 v[144:147], v16, s[76:79], s70 offen sc1\n"
"s_mov_b32 s70, 7356416\n"
"buffer_load_dwordx4 v[148:151], v16, s[76:79], s70 offen sc1\n"
"s_mov_b32 s70, 20480\n"
"buffer_load_dwordx4 v[152:155], v16, s[76:79], s70 offen sc1\n"
"s_mov_b32 s70, 7360512\n"
"buffer_load_dwordx4 v[156:159], v16, s[76:79], s70 offen sc1\n"
"s_mov_b32 s70, 24576\n"
"buffer_load_dwordx4 v[160:163], v16, s[76:79], s70 offen sc1\n"
"s_mov_b32 s70, 7364608\n"
"buffer_load_dwordx4 v[164:167], v16, s[76:79], s70 offen sc1\n"
"ds_read_b128 v[208:211], v22 offset:5376\n"
"ds_read_b128 v[212:215], v22 offset:5888\n"
"ds_read_b128 v[216:219], v22 offset:6400\n"
"ds_read_b128 v[220:223], v22 offset:6912\n"
"v_mov_b32 v29, 0x7ff00000\n"
"v_cmp_gt_u32 vcc, 32, v0\n"
"s_nop 1\n"
"v_cndmask_b32 v18, v29, v19, vcc\n"
"s_waitcnt vmcnt(13)\n"
"v_mfma_f32_16x16x32_bf16 v[96:99], a[180:183], a[64:67], 0\n"
"s_waitcnt vmcnt(12)\n"
"v_mfma_f32_16x16x32_bf16 v[96:99], a[180:183], a[68:71], v[96:99]\n"
"s_waitcnt vmcnt(11)\n"
"v_mfma_f32_16x16x32_bf16 v[96:99], a[184:187], a[72:75], v[96:99]\n"
"s_waitcnt vmcnt(10)\n"
"v_mfma_f32_16x16x32_bf16 v[96:99], a[184:187], a[76:79], v[96:99]\n"
"s_waitcnt vmcnt(9)\n"
"v_mfma_f32_16x16x32_bf16 v[100:103], a[180:183], a[80:83], 0\n"
"s_waitcnt vmcnt(8)\n"
"v_mfma_f32_16x16x32_bf16 v[100:103], a[180:183], a[84:87], v[100:103]\n"
"s_waitcnt vmcnt(7)\n"
"v_max_u32 v25, v136, v137\n"
"v_max3_u32 v25, v138, v139, v25\n"
"s_waitcnt vmcnt(6)\n"
"v_max3_u32 v25, v140, v141, v25\n"
"v_max3_u32 v25, v142, v143, v25\n"
"s_waitcnt vmcnt(5)\n"
"v_max3_u32 v25, v144, v145, v25\n"
"v_max3_u32 v25, v146, v147, v25\n"
"s_waitcnt vmcnt(4)\n"
"v_max3_u32 v25, v148, v149, v25\n"
"v_max3_u32 v25, v150, v151, v25\n"
"s_waitcnt vmcnt(3)\n"
"v_max3_u32 v25, v152, v153, v25\n"
"v_max3_u32 v25, v154, v155, v25\n"
"s_waitcnt vmcnt(2)\n"
"v_max3_u32 v25, v156, v157, v25\n"
"v_max3_u32 v25, v158, v159, v25\n"
"s_waitcnt vmcnt(1)\n"
"v_max3_u32 v25, v160, v161, v25\n"
"v_max3_u32 v25, v162, v163, v25\n"
"s_waitcnt vmcnt(0)\n"
"v_max3_u32 v25, v164, v165, v25\n"
"v_max3_u32 v25, v166, v167, v25\n"
"v_cmp_eq_u32 vcc, -1, v25\n"
"s_cmp_eq_u64 vcc, 0\n"
"s_cbranch_scc1 .Lok10_%=\n"
"s_movk_i32 s84, 0x7000\n"
".Lsp10_%=:\n"
"s_mov_b32 s70, 12288\n"
"buffer_load_dwordx4 v[136:139], v16, s[76:79], s70 offen sc1\n"
"s_mov_b32 s70, 7352320\n"
"buffer_load_dwordx4 v[140:143], v16, s[76:79], s70 offen sc1\n"
"s_mov_b32 s70, 16384\n"
"buffer_load_dwordx4 v[144:147], v16, s[76:79], s70 offen sc1\n"
"s_mov_b32 s70, 7356416\n"
"buffer_load_dwordx4 v[148:151], v16, s[76:79], s70 offen sc1\n"
"s_mov_b32 s70, 20480\n"
"buffer_load_dwordx4 v[152:155], v16, s[76:79], s70 offen sc1\n"
"s_mov_b32 s70, 7360512\n"
"buffer_load_dwordx4 v[156:159], v16, s[76:79], s70 offen sc1\n"
"s_mov_b32 s70, 24576\n"
"buffer_load_dwordx4 v[160:163], v16, s[76:79], s70 offen sc1\n"
"s_mov_b32 s70, 7364608\n"
"buffer_load_dwordx4 v[164:167], v16, s[76:79], s70 offen sc1\n"
"s_waitcnt vmcnt(0)\n"
"v_max_u32 v25, v136, v137\n"
"v_max3_u32 v25, v138, v139, v25\n"
"v_max3_u32 v25, v140, v141, v25\n"
"v_max3_u32 v25, v142, v143, v25\n"
"v_max3_u32 v25, v144, v145, v25\n"
"v_max3_u32 v25, v146, v147, v25\n"
"v_max3_u32 v25, v148, v149, v25\n"
"v_max3_u32 v25, v150, v151, v25\n"
"v_max3_u32 v25, v152, v153, v25\n"
"v_max3_u32 v25, v154, v155, v25\n"
"v_max3_u32 v25, v156, v157, v25\n"
"v_max3_u32 v25, v158, v159, v25\n"
"v_max3_u32 v25, v160, v161, v25\n"
"v_max3_u32 v25, v162, v163, v25\n"
"v_max3_u32 v25, v164, v165, v25\n"
"v_max3_u32 v25, v166, v167, v25\n"
"v_cmp_eq_u32 vcc, -1, v25\n"
"s_cmp_eq_u64 vcc, 0\n"
"s_cbranch_scc1 .Lok10_%=\n"
"s_sub_u32 s84, s84, 1\n"
"s_cmp_lg_u32 s84, 0\n"
"s_cbranch_scc1 .Lsp10_%=\n"
".Lok10_%=:\n"
"v_mfma_f32_16x16x32_bf16 v[100:103], a[184:187], v[136:139], v[100:103]\n"
"v_mfma_f32_16x16x32_bf16 v[100:103], a[184:187], v[140:143], v[100:103]\n"
"v_mfma_f32_16x16x32_bf16 v[104:107], a[180:183], v[144:147], 0\n"
"v_mfma_f32_16x16x32_bf16 v[104:107], a[180:183], v[148:151], v[104:107]\n"
"v_mfma_f32_16x16x32_bf16 v[104:107], a[184:187], v[152:155], v[104:107]\n"
"v_mfma_f32_16x16x32_bf16 v[104:107], a[184:187], v[156:159], v[104:107]\n"
"v_mfma_f32_16x16x32_bf16 v[108:111], a[180:183], v[160:163], 0\n"
"v_mfma_f32_16x16x32_bf16 v[108:111], a[180:183], v[164:167], v[108:111]\n"
"s_waitcnt lgkmcnt(3)\n"
"v_fma_f32 v96, v96, v208, v224\n"
"v_fma_f32 v97, v97, v209, v225\n"
"v_fma_f32 v98, v98, v210, v226\n"
"v_fma_f32 v99, v99, v211, v227\n"
"s_waitcnt lgkmcnt(2)\n"
"v_fma_f32 v100, v100, v212, v228\n"
"v_fma_f32 v101, v101, v213, v229\n"
"v_fma_f32 v102, v102, v214, v230\n"
"v_fma_f32 v103, v103, v215, v231\n"
"s_waitcnt lgkmcnt(1)\n"
"v_fma_f32 v104, v104, v216, v232\n"
"v_fma_f32 v105, v105, v217, v233\n"
"v_fma_f32 v106, v106, v218, v234\n"
"v_fma_f32 v107, v107, v219, v235\n"
"s_waitcnt lgkmcnt(0)\n"
"v_fma_f32 v108, v108, v220, v236\n"
"v_fma_f32 v109, v109, v221, v237\n"
"v_fma_f32 v110, v110, v222, v238\n"
"v_fma_f32 v111, v111, v223, v239\n"
"v_cvt_pk_bf16_f32 v96, v96, v97\n"
"v_cvt_pk_bf16_f32 v97, v98, v99\n"
"v_cvt_pk_bf16_f32 v98, v100, v101\n"
"v_cvt_pk_bf16_f32 v99, v102, v103\n"
"v_cvt_pk_bf16_f32 v100, v104, v105\n"
"v_cvt_pk_bf16_f32 v101, v106, v107\n"
"v_cvt_pk_bf16_f32 v102, v108, v109\n"
"v_cvt_pk_bf16_f32 v103, v110, v111\n"
"buffer_store_dwordx2 v[96:97], v19, s[48:51], 0 offen offset:0\n"
"buffer_store_dwordx2 v[98:99], v19, s[48:51], 0 offen offset:256\n"
"buffer_store_dwordx2 v[100:101], v19, s[48:51], 0 offen offset:512\n"
"buffer_store_dwordx2 v[102:103], v18, s[48:51], 0 offen offset:768\n"
"s_or_b32 s75, %[b], s71\n"
"s_cmp_lg_u32 s75, 0\n"
"s_cbranch_scc1 .Lend_%=\n"
"buffer_load_dwordx4 v[32:35], v23, s[60:63], 0 offen offset:1024 sc1\n"
"s_waitcnt vmcnt(0)\n"
"v_xor_b32 v32, s72, v32\n"
"v_xor_b32 v33, s72, v33\n"
"v_xor_b32 v34, s72, v34\n"
"v_xor_b32 v35, s72, v35\n"
"v_or_b32 v24, v32, v33\n"
"v_or3_b32 v24, v34, v35, v24\n"
"v_cmp_ne_u32 vcc, 0, v24\n"
"s_cmp_eq_u64 vcc, 0\n"
"s_cbranch_scc1 .Lok11_%=\n"
"s_movk_i32 s84, 0x7000\n"
".Lsp11_%=:\n"
"buffer_load_dwordx4 v[32:35], v23, s[60:63], 0 offen offset:1024 sc1\n"
"s_waitcnt vmcnt(0)\n"
"v_xor_b32 v32, s72, v32\n"
"v_xor_b32 v33, s72, v33\n"
"v_xor_b32 v34, s72, v34\n"
"v_xor_b32 v35, s72, v35\n"
"v_or_b32 v24, v32, v33\n"
"v_or3_b32 v24, v34, v35, v24\n"
"v_cmp_ne_u32 vcc, 0, v24\n"
"s_cmp_eq_u64 vcc, 0\n"
"s_cbranch_scc1 .Lok11_%=\n"
"s_sub_u32 s84, s84, 1\n"
"s_cmp_lg_u32 s84, 0\n"
"s_cbranch_scc1 .Lsp11_%=\n"
".Lok11_%=:\n"
"v_mov_b32 v24, 0\n"
"buffer_store_dword v11, v24, s[60:63], 0 offen sc1\n"
".Lend_%=:\n"
    : : [w]"s"(w), [ss]"s"(w & 1), [rr]"s"(w >> 1), [g]"s"(g), [T]"s"(T), [b]"s"(b), [go]"s"(go), [one]"s"(one), [nwm]"s"(nwm), [n1]"s"(n1), [n2]"s"(n2), [nx]"s"(nx),
        [x]"s"(x), [k]"s"(kc + ((size_t)g * 2048 + 1536) * 512), [v]"s"(vc + ((size_t)g * 2048 + 1536) * 512), [wq]"s"(wq + g * 1024),
        [wo]"s"(wo + (size_t)g * 512 * 896), [w1]"s"(w1 + f * 512), [w2]"s"(w2 + (size_t)f * 256 * 896), [c]"s"(c), [wm]"s"(wm + col0 * 2),
        [ws]"s"(ws), [out]"s"(out), [lds]"s"((unsigned)(size_t)lds)
    : "v0","v1","v2","v3","v4","v5","v6","v7","v8","v9","v10","v11","v12","v13","v14","v15","v16","v17","v18","v19","v20","v21","v22","v23","v24","v25","v26","v27","v28","v29","v30","v31","v32","v33","v34","v35","v36","v37","v38","v39","v40","v41","v42","v43","v44","v45","v46","v47","v48","v49","v50","v51","v52","v53","v54","v55","v56","v57","v58","v59","v60","v61","v62","v63","v64","v65","v66","v67","v68","v69","v70","v71","v72","v73","v74","v75","v76","v77","v78","v79","v80","v81","v82","v83","v84","v85","v86","v87","v88","v89","v90","v91","v92","v93","v94","v95","v96","v97","v98","v99","v100","v101","v102","v103","v104","v105","v106","v107","v108","v109","v110","v111","v112","v113","v114","v115","v116","v117","v118","v119","v120","v121","v122","v123","v124","v125","v126","v127","v128","v129","v130","v131","v132","v133","v134","v135","v136","v137","v138","v139","v140","v141","v142","v143","v144","v145","v146","v147","v148","v149","v150","v151","v152","v153","v154","v155","v156","v157","v158","v159","v160","v161","v162","v163","v164","v165","v166","v167","v168","v169","v170","v171","v172","v173","v174","v175","v176","v177","v178","v179","v180","v181","v182","v183","v184","v185","v186","v187","v188","v189","v190","v191","v192","v193","v194","v195","v196","v197","v198","v199","v200","v201","v202","v203","v204","v205","v206","v207","v208","v209","v210","v211","v212","v213","v214","v215","v216","v217","v218","v219","v220","v221","v222","v223","v224","v225","v226","v227","v228","v229","v230","v231","v232","v233","v234","v235","v236","v237","v238","v239","v240","v241","v242","v243","v244","v245","v246","v247","v248","v249","v250","v251","v252","v253","v254","v255","a0","a1","a2","a3","a4","a5","a6","a7","a8","a9","a10","a11","a12","a13","a14","a15","a16","a17","a18","a19","a20","a21","a22","a23","a24","a25","a26","a27","a28","a29","a30","a31","a32","a33","a34","a35","a36","a37","a38","a39","a40","a41","a42","a43","a44","a45","a46","a47","a48","a49","a50","a51","a52","a53","a54","a55","a56","a57","a58","a59","a60","a61","a62","a63","a64","a65","a66","a67","a68","a69","a70","a71","a72","a73","a74","a75","a76","a77","a78","a79","a80","a81","a82","a83","a84","a85","a86","a87","a88","a89","a90","a91","a92","a93","a94","a95","a96","a97","a98","a99","a100","a101","a102","a103","a104","a105","a106","a107","a108","a109","a110","a111","a112","a113","a114","a115","a116","a117","a118","a119","a120","a121","a122","a123","a124","a125","a126","a127","a128","a129","a130","a131","a132","a133","a134","a135","a136","a137","a138","a139","a140","a141","a142","a143","a144","a145","a146","a147","a148","a149","a150","a151","a152","a153","a154","a155","a156","a157","a158","a159","a160","a161","a162","a163","a164","a165","a166","a167","a168","a169","a170","a171","a172","a173","a174","a175","a176","a177","a178","a179","a180","a181","a182","a183","a184","a185","a186","a187","a188","a189","a190","a191","a192","a193","a194","a195","a196","a197","a198","a199","a200","a201","a202","a203","a204","a205","a206","a207","a208","a209","a210","a211","a212","a213","a214","a215","a216","a217","a218","a219","a220","a221","a222","a223","a224","a225","a226","a227","a228","a229","a230","a231","a232","a233","a234","a235","a236","a237","a238","a239","a240","a241","a242","a243","a244","a245","a246","a247","a248","a249","a250","a251","a252","a253","a254","a255","s40","s41","s42","s43","s44","s45","s46","s47","s48","s49","s50","s51","s52","s53","s54","s55","s56","s57","s58","s59","s60","s61","s62","s63","s64","s65","s66","s67","s68","s69","s70","s71","s72","s73","s74","s75","s76","s77","s78","s79","s80","s81","s82","s83","s84","s85","s86","s87","s88","s89","s90","s91","s92","s93","s94","s95","s96","s97","s98","s99","memory","vcc","scc");
}

extern "C" void kernel_launch(void* const* d_in, const int* in_sizes, int n_in, void* d_out, int out_size, void* d_ws,
                              size_t ws_size, hipStream_t stream) {
  k_block<<<256, 256, 0, stream>>>((const char*)d_in[0], (const char*)d_in[1], (const char*)d_in[2], (const char*)d_in[3],
                                   (const char*)d_in[4], (const char*)d_in[6], (const char*)d_in[7], (const char*)d_in[8],
                                   (const char*)d_in[9], (char*)d_ws, (char*)d_out);
}
```
